```python
import torch

def _softmax(x, axis=-1):
    return torch.softmax(x, dim=axis)

def _gelu(x):
    return torch.nn.functional.gelu(x, approximate='tanh')

def _silu(x):
    return torch.nn.functional.silu(x)

def _sigmoid(x):
    return torch.sigmoid(x)

def _layernorm(x, axis=-1, eps=1e-05):
    m = x.mean(dim=axis, keepdim=True)
    v = x.var(dim=axis, keepdim=True, unbiased=False)
    return (x - m) / torch.sqrt(v + eps)

def _scatter_add(tgt, idx, src, axis=0):
    return tgt.clone().index_add_(axis, idx, src)

def _segment_sum(src, seg, n):
    out = torch.zeros((n,) + src.shape[1:], dtype=src.dtype)
    return out.index_add_(0, seg, src)

def _pad(x, pw):
    flat = tuple((v for pair in reversed(pw) for v in pair))
    return torch.nn.functional.pad(x, flat)
kernel_name = 'interleave_layers__memory_aug_recurrent_mem_tr__dense_activation_gelu__prompt'

def setup_inputs():

    _w = lambda a: (lambda t: t.to(torch.bfloat16) if t.is_floating_point() else t)(torch.as_tensor(a))

    def _A_setup():
        B, S, Dm = (1, 2048, 576)
        H, Dh = (32, 128)
        M, seg = (8, 32)
        seg = min(seg, S)
        S = S // seg * seg
        return {'x': _w((lambda *s: torch.randn(*s, dtype=torch.bfloat16) * 0.05)(B, S, Dm)), 'mem0': _w((lambda *s: torch.randn(*s, dtype=torch.bfloat16) * 0.2)(B, M, Dm)), 'Wq': _w((lambda *s: torch.randn(*s, dtype=torch.bfloat16) * 1.6)(Dm, H, Dh) * Dm ** (-0.5)), 'Wk': _w((lambda *s: torch.randn(*s, dtype=torch.bfloat16) * 1.6)(Dm, H, Dh) * Dm ** (-0.5)), 'Wv': _w((lambda *s: torch.randn(*s, dtype=torch.bfloat16) * 0.2)(Dm, H, Dh) * Dm ** (-0.5)), 'Wo': _w((lambda *s: torch.randn(*s, dtype=torch.bfloat16) * 0.2)(H, Dh, Dm) * (H * Dh) ** (-0.5)), 'seg_len': seg}
    _A_in = _A_setup()

    def _B_setup():
        B, S, D, F = (1, 2048, 576, 4608)
        return {'x': _w((lambda *s: torch.randn(*s, dtype=torch.bfloat16) * 0.05)(B, S, D)), 'w1': _w((lambda *s: torch.randn(*s, dtype=torch.bfloat16) * 0.4)(D, F) * D ** (-0.5)), 'b1': _w((lambda *s: torch.randn(*s, dtype=torch.bfloat16) * 12.8)(F) * 0.01), 'w2': _w((lambda *s: torch.randn(*s, dtype=torch.bfloat16) * 0.1)(F, D) * F ** (-0.5)), 'b2': _w((lambda *s: torch.randn(*s, dtype=torch.bfloat16) * 0.8)(D) * 0.01)}
    _B_in = _B_setup()
    return {'A_x': _A_in['x'], 'A_mem0': _A_in['mem0'], 'A_Wq': _A_in['Wq'], 'A_Wk': _A_in['Wk'], 'A_Wv': _A_in['Wv'], 'A_Wo': _A_in['Wo'], 'A_seg_len': _A_in['seg_len'], 'B_x': _B_in['x'], 'B_w1': _B_in['w1'], 'B_b1': _B_in['b1'], 'B_w2': _B_in['w2'], 'B_b2': _B_in['b2']}

def reference(A_x, A_mem0, A_Wq, A_Wk, A_Wv, A_Wo, A_seg_len, B_x, B_w1, B_b1, B_w2, B_b2):

    def _A_ref(x, mem0, Wq, Wk, Wv, Wo, seg_len):
        B, S, Dm = x.shape
        H, Dh = (Wq.shape[1], Wq.shape[2])
        M = mem0.shape[1]
        scale = Dh ** (-0.5)
        n_seg = S // seg_len
        L = M + seg_len
        idx = torch.arange(L)
        mask = (idx[:, None] < M) | (idx[:, None] >= idx[None, :])
        mem = mem0
        outs = []
        for s in range(n_seg):
            xs = x[:, s * seg_len:(s + 1) * seg_len, :].clone()
            h = torch.concatenate([mem, xs], axis=1)
            q = torch.einsum('btd,dhe->bhte', h, Wq)
            k = torch.einsum('btd,dhe->bhte', h, Wk)
            v = torch.einsum('btd,dhe->bhte', h, Wv)
            scores = torch.einsum('bhqe,bhke->bhqk', q, k) * scale
            scores = torch.where(mask[None, None], scores, -1000000000.0)
            p = _softmax(scores, axis=-1)
            o = torch.einsum('bhqk,bhke->bhqe', p, v)
            o = torch.einsum('bhte,hed->btd', o, Wo)
            mem = o[:, :M, :].clone()
            outs.append(o[:, M:, :])
        return torch.concatenate(outs, axis=1)

    def _B_ref(x, w1, b1, w2, b2):
        h = torch.einsum('bsd,df->bsf', x, w1) + b1
        h = _gelu(h)
        return torch.einsum('bsf,fd->bsd', h, w2) + b2
    x = A_x
    x = x + _A_ref(_layernorm(x, axis=-1), A_mem0, A_Wq, A_Wk, A_Wv, A_Wo, A_seg_len)
    x = x + _B_ref(_layernorm(x, axis=-1), B_w1, B_b1, B_w2, B_b2)
    x = x + _A_ref(_layernorm(x, axis=-1), A_mem0, A_Wq, A_Wk, A_Wv, A_Wo, A_seg_len)
    x = x + _B_ref(_layernorm(x, axis=-1), B_w1, B_b1, B_w2, B_b2)
    return x
```

```cpp
#include <hip/hip_runtime.h>
#include <hip/hip_bf16.h>
typedef unsigned short u16;
__device__ __forceinline__ float b2f(u16 v) { return __uint_as_float((unsigned)v << 16); }
__device__ __forceinline__ u16 f2b(float f) { return __builtin_bit_cast(u16, (__bf16)f); }
__global__ void gemm(const u16* A, const u16* B, u16* C, int M, int N, int K, int mode, const u16* bias, const u16* res) {
  __shared__ float As[16][65], Bs[16][65];
  int tx = threadIdx.x & 15, ty = threadIdx.x >> 4; int m0 = blockIdx.y * 64, n0 = blockIdx.x * 64;
  float acc[4][4] = {};
  for (int k0 = 0; k0 < K; k0 += 16) {
    for (int i = threadIdx.x; i < 1024; i += 256) { int m = i >> 4, k = i & 15; As[k][m] = (m0 + m < M) ? b2f(A[(size_t)(m0 + m) * K + k0 + k]) : 0.f; int kk = i >> 6, n = i & 63; Bs[kk][n] = b2f(B[(size_t)(k0 + kk) * N + n0 + n]); }
    __syncthreads();
    for (int k = 0; k < 16; ++k) for (int i = 0; i < 4; ++i) for (int j = 0; j < 4; ++j) acc[i][j] += As[k][ty * 4 + i] * Bs[k][tx * 4 + j];
    __syncthreads();
  }
  for (int i = 0; i < 4; ++i) for (int j = 0; j < 4; ++j) { int m = m0 + ty * 4 + i, n = n0 + tx * 4 + j; if (m >= M) continue; float v = acc[i][j];
    if (mode == 1) { v += b2f(bias[n]); float u = 0.7978845608f * (v + 0.044715f * v * v * v); v = 0.5f * v * (1.f + tanhf(u)); }
    if (mode == 2) v += b2f(bias[n]);
    if (mode >= 2) v += b2f(res[(size_t)m * N + n]);
    C[(size_t)m * N + n] = f2b(v); }
}
__global__ void ln(const u16* x, u16* y) {
  int r = blockIdx.x, l = threadIdx.x; float v[9], s = 0; for (int i = 0; i < 9; ++i) { v[i] = b2f(x[r * 576 + l + 64 * i]); s += v[i]; }
  for (int o = 32; o; o >>= 1) s += __shfl_xor(s, o); float m = s / 576, q = 0; for (int i = 0; i < 9; ++i) q += (v[i] - m) * (v[i] - m);
  for (int o = 32; o; o >>= 1) q += __shfl_xor(q, o); float rs = rsqrtf(q / 576 + 1e-5f); for (int i = 0; i < 9; ++i) y[r * 576 + l + 64 * i] = f2b((v[i] - m) * rs);
}
__global__ void attn(const u16* q, const u16* km, const u16* vm, const u16* kx, const u16* vx, u16* o, int causal, int memstride) {
  int h = blockIdx.x, r = blockIdx.y, l = threadIdx.x; int seg = causal ? r / 32 : 0, t = r % 32;
  km += (size_t)seg * memstride; vm += (size_t)seg * memstride; kx += (size_t)seg * 32 * 4096; vx += (size_t)seg * 32 * 4096;
  __shared__ float p[64];
  float s = -1e30f;
  if (l < 40 && (!causal || l < 8 || l - 8 <= t)) { const u16* k = l < 8 ? km + l * 4096 : kx + (l - 8) * 4096; s = 0; for (int e = 0; e < 128; ++e) s += b2f(q[(size_t)r * 4096 + h * 128 + e]) * b2f(k[h * 128 + e]); s *= 0.08838834764f; }
  float m = s; for (int o_ = 32; o_; o_ >>= 1) m = fmaxf(m, __shfl_xor(m, o_)); float e_ = __expf(s - m), z = e_; for (int o_ = 32; o_; o_ >>= 1) z += __shfl_xor(z, o_);
  p[l] = b2f(f2b(e_ / z)); __syncthreads();
  for (int e = l; e < 128; e += 64) { float a = 0; for (int j = 0; j < 40; ++j) a += p[j] * b2f((j < 8 ? vm + j * 4096 : vx + (j - 8) * 4096)[h * 128 + e]); o[(size_t)r * 4096 + h * 128 + e] = f2b(a); }
}
extern "C" void kernel_launch(void* const* d_in, const int* in_sizes, int n_in, void* d_out, int out_size, void* d_ws, size_t ws_size, hipStream_t stream) {
  const u16 *x = (const u16*)d_in[0], *mem0 = (const u16*)d_in[1], *Wq = (const u16*)d_in[2], *Wk = (const u16*)d_in[3], *Wv = (const u16*)d_in[4], *Wo = (const u16*)d_in[5];
  const u16 *w1 = (const u16*)d_in[8], *b1 = (const u16*)d_in[9], *w2 = (const u16*)d_in[10], *b2 = (const u16*)d_in[11];
  u16* ws = (u16*)d_ws; u16 *xn = ws, *q = xn + 2048 * 576, *k = q + 2048 * 4096, *v = k + 2048 * 4096, *o = v + 2048 * 4096, *hh = o + 2048 * 4096, *x1 = hh + 2048 * 4608, *x2 = x1 + 2048 * 576, *km = x2 + 2048 * 576, *vm = km + 64 * 8 * 4096, *qm = vm + 64 * 8 * 4096, *om = qm + 8 * 4096, *mem = om + 8 * 4096;
  const u16* cur = x;
  for (int L = 0; L < 2; ++L) {
    ln<<<2048, 64, 0, stream>>>(cur, xn);
    gemm<<<dim3(64, 32), 256, 0, stream>>>(xn, Wq, q, 2048, 4096, 576, 0, 0, 0);
    gemm<<<dim3(64, 32), 256, 0, stream>>>(xn, Wk, k, 2048, 4096, 576, 0, 0, 0);
    gemm<<<dim3(64, 32), 256, 0, stream>>>(xn, Wv, v, 2048, 4096, 576, 0, 0, 0);
    for (int s = 0; s < 64; ++s) { const u16* m = s ? mem : mem0;
      gemm<<<dim3(64, 1), 256, 0, stream>>>(m, Wq, qm, 8, 4096, 576, 0, 0, 0);
      gemm<<<dim3(64, 1), 256, 0, stream>>>(m, Wk, km + s * 8 * 4096, 8, 4096, 576, 0, 0, 0);
      gemm<<<dim3(64, 1), 256, 0, stream>>>(m, Wv, vm + s * 8 * 4096, 8, 4096, 576, 0, 0, 0);
      attn<<<dim3(32, 8), 64, 0, stream>>>(qm, km + s * 8 * 4096, vm + s * 8 * 4096, k + (size_t)s * 32 * 4096, v + (size_t)s * 32 * 4096, om, 0, 0);
      gemm<<<dim3(9, 1), 256, 0, stream>>>(om, Wo, mem, 8, 576, 4096, 0, 0, 0);
    }
    attn<<<dim3(32, 2048), 64, 0, stream>>>(q, km, vm, k, v, o, 1, 8 * 4096);
    gemm<<<dim3(9, 32), 256, 0, stream>>>(o, Wo, x1, 2048, 576, 4096, 3, 0, cur);
    ln<<<2048, 64, 0, stream>>>(x1, xn);
    gemm<<<dim3(72, 32), 256, 0, stream>>>(xn, w1, hh, 2048, 4608, 576, 1, b1, 0);
    u16* dst = L ? (u16*)d_out : x2;
    gemm<<<dim3(9, 32), 256, 0, stream>>>(hh, w2, dst, 2048, 576, 4608, 2, b2, x1);
    cur = x2;
  }
}
```

```cpp
#include <hip/hip_runtime.h>
#include <hip/hip_bf16.h>
#include <stdint.h>

#pragma clang diagnostic ignored "-Winline-asm"

typedef __bf16 bf2 __attribute__((ext_vector_type(2)));
typedef __bf16 bf4 __attribute__((ext_vector_type(4)));
typedef __bf16 bf8 __attribute__((ext_vector_type(8)));
typedef __bf16 bf16v __attribute__((ext_vector_type(16)));
typedef short s4 __attribute__((ext_vector_type(4)));
typedef float v2f __attribute__((ext_vector_type(2)));
typedef float v4f __attribute__((ext_vector_type(4)));
typedef float v16f __attribute__((ext_vector_type(16)));
typedef unsigned v2u __attribute__((ext_vector_type(2)));
typedef unsigned v4u __attribute__((ext_vector_type(4)));
typedef __amdgpu_buffer_rsrc_t rsrc_t;
typedef unsigned long long u64;
#define LAS __attribute__((address_space(3)))
#define DI __device__ __forceinline__
#define SB() __builtin_amdgcn_sched_barrier(0)
#define OWNED "v255", "a255", "s99", "vcc", "memory"

constexpr int NSEG = 64;
constexpr int RING = 16;
constexpr unsigned KB = 1024, MB = 1024 * 1024;
constexpr unsigned OOB = 0x80000000u;
constexpr int SC1 = 16;
constexpr unsigned WS_USED = 28 * MB + RING * 4 * MB;
__host__ __device__ constexpr unsigned TR(unsigned off) { return ((off >> 13) << 14) | (off & 8191u); }
constexpr unsigned P_RS = 0;
constexpr unsigned L_FLAG = 864 * KB;
constexpr unsigned P_POL = 1920 * KB;
constexpr unsigned P_AG = 2 * MB;
constexpr unsigned P_XL = 12 * MB;
constexpr unsigned P_SLAB = 28 * MB;
constexpr unsigned L_K = 0, L_V = 256 * KB, L_OI = 512 * KB, L_KVM = 768 * KB;
constexpr unsigned L_PO = 0, L_X1 = 160 * KB, L_XN2 = 200 * KB, L_H = 240 * KB, L_PF = 528 * KB;
constexpr unsigned L_XN = 0, L_XR = 40 * KB;
constexpr int F_XK = 0, F_XO = 32, F_V = 64, F_O = 80, F_R1 = 96, F_F1 = 104, F_F2 = 128, F_R2 = 144, F_R0 = 148, F_C = 160;
constexpr int SPIN_CAP = 1 << 15;
constexpr int NEVER = -100000;
#define IPRE 1
#define PRE0 1
#define PRE1 3
#define DLY_L 10
#define DLY_O 4
#define DLY_F1 7
#define DLY_F2 9
struct Params {
  const void *x, *mem0, *Wq, *Wk, *Wv, *Wo, *w1, *b1, *w2, *b2;
  void* out;
  char* ws;
};

DI rsrc_t mkrsrc(const void* p, unsigned bytes) { return __builtin_amdgcn_make_buffer_rsrc((void*)p, 0, (int)bytes, 0x00020000); }
DI void dma_sc1(rsrc_t r, unsigned voff, unsigned soff, unsigned lds) {
  asm volatile("s_mov_b32 m0, %0\n\ts_nop 0\n\tbuffer_load_dwordx4 %1, %2, %3 offen sc1 lds" ::"s"(lds), "v"(voff), "s"(r), "s"(soff) : "m0", "memory");
}
DI void dma_plain(rsrc_t r, unsigned voff, unsigned soff, unsigned lds) {
  asm volatile("s_mov_b32 m0, %0\n\ts_nop 0\n\tbuffer_load_dwordx4 %1, %2, %3 offen lds" ::"s"(lds), "v"(voff), "s"(r), "s"(soff) : "m0", "memory");
}
template <int N> DI void vmwait() { asm volatile("s_waitcnt vmcnt(%0)" ::"i"(N) : "memory"); }
DI void lgkm0() { asm volatile("s_waitcnt lgkmcnt(0)" ::: "memory"); }
DI void bar() { asm volatile("s_barrier" ::: "memory"); }
DI void lbar() { asm volatile("s_waitcnt lgkmcnt(0)\n\ts_barrier" ::: "memory"); }
DI v4u ld16(rsrc_t r, unsigned v, unsigned s) { return __builtin_amdgcn_raw_buffer_load_b128(r, v, s, SC1); }
DI v2u ld8(rsrc_t r, unsigned v, unsigned s) { return __builtin_amdgcn_raw_buffer_load_b64(r, v, s, SC1); }
DI unsigned ld4(rsrc_t r, unsigned v, unsigned s) { return __builtin_amdgcn_raw_buffer_load_b32(r, v, s, SC1); }
DI void st16(v4u x, rsrc_t r, unsigned v, unsigned s) { __builtin_amdgcn_raw_buffer_store_b128(x, r, v, s, SC1); }
DI void st8(v2u x, rsrc_t r, unsigned v, unsigned s) { __builtin_amdgcn_raw_buffer_store_b64(x, r, v, s, SC1); }
DI void st4(unsigned x, rsrc_t r, unsigned v, unsigned s) { __builtin_amdgcn_raw_buffer_store_b32(x, r, v, s, SC1); }
DI bf8 lds16(unsigned a) { return *(LAS bf8*)(uintptr_t)a; }
DI void lds16w(unsigned a, v4u x) { *(LAS v4u*)(uintptr_t)a = x; }
DI void lds8w(unsigned a, v2u x) { *(LAS v2u*)(uintptr_t)a = x; }
DI bf4 ldstr(unsigned a) { return __builtin_amdgcn_ds_read_tr16_b64_v4bf16((LAS bf4*)(uintptr_t)a); }
DI bf8 cat(bf4 a, bf4 b) { return __builtin_shufflevector(a, b, 0, 1, 2, 3, 4, 5, 6, 7); }
DI bf16v cat(bf8 a, bf8 b) { return __builtin_shufflevector(a, b, 0, 1, 2, 3, 4, 5, 6, 7, 8, 9, 10, 11, 12, 13, 14, 15); }
DI unsigned pk(float a, float b) { v2f p = {a, b}; return __builtin_bit_cast(unsigned, __builtin_convertvector(p, bf2)); }
DI bf8 asbf(v4u x) { return __builtin_bit_cast(bf8, x); }
DI v4u asu(bf8 x) { return __builtin_bit_cast(v4u, x); }
DI float asf(unsigned x) { return __builtin_bit_cast(float, x); }
DI unsigned asu(float x) { return __builtin_bit_cast(unsigned, x); }
template <class T> DI void pinA(T& x) { asm volatile("" : "+a"(x)); }
template <class T> DI void pinV(T& x) { asm volatile("" : "+v"(x)); }
DI v4f mm16(bf8 a, bf8 b, v4f c) { return __builtin_amdgcn_mfma_f32_16x16x32_bf16(a, b, c, 0, 0, 0); }
DI v4f mm16h(v2u a, v2u b, v4f c) { return __builtin_amdgcn_mfma_f32_16x16x16bf16_1k(__builtin_bit_cast(s4, a), __builtin_bit_cast(s4, b), c, 0, 0, 0); }
DI v16f mm32(bf8 a, bf8 b, v16f c) { return __builtin_amdgcn_mfma_f32_32x32x16_bf16(a, b, c, 0, 0, 0); }
DI v4f smm(bf8 a, bf16v b, v4f c, int idx) { return __builtin_amdgcn_smfmac_f32_16x16x64_bf16(a, b, c, idx, 0, 0); }
DI unsigned umax3(unsigned a, unsigned b, unsigned c) { return max(max(a, b), c); }
DI unsigned mx4(v4u v, unsigned m) { return umax3(umax3(v.x, v.y, m), v.z, v.w); }
DI unsigned nosent(unsigned x) { return min(x, 0xFFFFFFFEu); }
DI float xmax16(float x) { auto r = __builtin_amdgcn_permlane16_swap(asu(x), asu(x), false, false); return fmaxf(asf(r[0]), asf(r[1])); }
DI float xmax32(float x) { auto r = __builtin_amdgcn_permlane32_swap(asu(x), asu(x), false, false); return fmaxf(asf(r[0]), asf(r[1])); }
DI float xadd16(float x) { auto r = __builtin_amdgcn_permlane16_swap(asu(x), asu(x), false, false); return asf(r[0]) + asf(r[1]); }
DI float xadd32(float x) { auto r = __builtin_amdgcn_permlane32_swap(asu(x), asu(x), false, false); return asf(r[0]) + asf(r[1]); }
DI float xadd8(float x) { return x + asf((unsigned)__builtin_amdgcn_update_dpp(0, (int)asu(x), 0x128, 0xf, 0xf, true)); }
DI float fold(float a, float b) { auto r = __builtin_amdgcn_permlane32_swap(asu(a), asu(b), false, false); return asf(r[0]) + asf(r[1]); }
DI v2u fold4(v4f a, v4f b) { return (v2u){pk(fold(a[0], b[0]), fold(a[1], b[1])), pk(fold(a[2], b[2]), fold(a[3], b[3]))}; }
DI v4u uni(v4u x) { return (v4u){(unsigned)__builtin_amdgcn_readfirstlane(x[0]), (unsigned)__builtin_amdgcn_readfirstlane(x[1]), (unsigned)__builtin_amdgcn_readfirstlane(x[2]), (unsigned)__builtin_amdgcn_readfirstlane(x[3])}; }
DI void nap(int n) { for (int i = 0; i < n; ++i) __builtin_amdgcn_s_sleep(1); }

#define STAMP(c, n, e) do {} while (0)
#define DGOFF(c) 0u

struct Ctx {
  rsrc_t ws;
  int lane, wave, L, vb;
  unsigned bit;
  int dead;
  DI unsigned slab0(int s) const { return P_SLAB + (unsigned)(s & (RING - 1)) * (4 * MB) + bit; }
  DI unsigned slab1(int s) const { return P_SLAB + (unsigned)(s & (RING - 1)) * (4 * MB) + 2 * MB + (bit ^ 8192u); }
  DI unsigned xin() const { return P_XL + bit; }
  DI unsigned xflag(int i) const { return flag(F_R0 + (L ? i & 3 : i)); }
  DI unsigned dflag(int i) const { return L ? flag(F_R2 + i) : oflag(F_R0 + i); }
  DI unsigned flag(int id) const { return TR(L_FLAG + (unsigned)id * 128u) + bit; }
  DI unsigned oflag(int id) const { return TR(L_FLAG + (unsigned)id * 128u) + (bit ^ 8192u); }
  DI void publish(int id, int n) const { if (wave == 0) st4((unsigned)n, ws, lane == 0 ? flag(id) : OOB, 0); }
};
struct Poll { unsigned off; int delta; };
DI unsigned poll_issue(const Ctx& c, const Poll& p) { asm volatile("" ::: "memory"); return ld4(c.ws, p.off, 0); }
DI bool poll_ok(unsigned v, const Poll& p, int n) { asm volatile("" : "+v"(v)); return __all((int)v >= n + p.delta); }
DI void poll_wait(Ctx& c, const Poll& p, int n) {
  for (int it = 0; !c.dead; ++it) {
    if (poll_ok(poll_issue(c, p), p, n)) break;
    if (it > SPIN_CAP) c.dead = 1;
    __builtin_amdgcn_s_sleep(1);
  }
}

#define COMMON "{s[36:39]}"(c.ws), "{s[56:59]}"(uni((v4u){w < 2 ? 65536u : 65536u + 256u - 4096u, w * 1024u, fword, srcb})), "{s[60:63]}"(uni((v4u){srcm, srck, dstb, DGOFF(c)}))
struct W32 { unsigned voff, ra, ab; };
DI W32 w32(const Ctx& c, unsigned pitch, unsigned col0) {
  const unsigned ln = (unsigned)c.lane, g = ln >> 4, q = (ln >> 2) & 3u, p = ln & 3u, w = (unsigned)c.wave;
  return W32{(4u * w + q) * pitch + g * 64u + p * 16u + col0, g * 2048u + w * 256u + q * 64u + p * 16u, g * 512u + (ln & 15u) * 16u};
}
DI void role_v(Ctx& c, const Params& p, int v) {
  const unsigned ln = (unsigned)c.lane, w = (unsigned)c.wave;
  const bool full = v < 10;
  const W32 a = w32(c, 8192u, (unsigned)v * 768u);
  const unsigned fword = c.flag(F_V + v);
  const unsigned srcb = c.xin() + w * 2048u, srcm = 256 * KB, srck = 63;
  const unsigned dstb = c.slab0(0) + TR(L_V) + (unsigned)(3 * v) * 16384u + w * 2048u;
  asm volatile(
  "s_getpc_b64 s[0:1]\n"
  "L_c0_%=:\n"
  "v_mbcnt_lo_u32_b32 v5, -1, 0\n"
  "v_mbcnt_hi_u32_b32 v5, -1, v5\n"
  "v_lshlrev_b32 v5, 4, v5\n"
  "v_add_lshl_u32 v5, v5, s57, 2\n"
  "v_min_u32 v5, L_c1_%=-L_c0_%=, v5\n"
  "global_load_dword v5, v5, s[0:1]\n"
  "s_nop 4\n"
  "s_add_u32 m0, s57, 0\n"
  "s_mov_b32 s74, 0\n"
  "buffer_load_dwordx4 v8, s[40:43], s74 offen lds\n"
  "s_add_u32 m0, s57, 4096\n"
  "s_mov_b32 s74, 131072\n"
  "buffer_load_dwordx4 v8, s[40:43], s74 offen lds\n"
  "s_add_u32 m0, s57, 8192\n"
  "s_mov_b32 s74, 262144\n"
  "buffer_load_dwordx4 v8, s[40:43], s74 offen lds\n"
  "s_add_u32 m0, s57, 12288\n"
  "s_mov_b32 s74, 393216\n"
  "buffer_load_dwordx4 v8, s[40:43], s74 offen lds\n"
  "s_add_u32 m0, s57, 16384\n"
  "s_mov_b32 s74, 524288\n"
  "buffer_load_dwordx4 v8, s[40:43], s74 offen lds\n"
  "s_add_u32 m0, s57, 20480\n"
  "s_mov_b32 s74, 655360\n"
  "buffer_load_dwordx4 v8, s[40:43], s74 offen lds\n"
  "s_add_u32 m0, s57, 24576\n"
  "s_mov_b32 s74, 786432\n"
  "buffer_load_dwordx4 v8, s[40:43], s74 offen lds\n"
  "s_add_u32 m0, s57, 28672\n"
  "s_mov_b32 s74, 917504\n"
  "buffer_load_dwordx4 v8, s[40:43], s74 offen lds\n"
  "s_add_u32 m0, s57, 32768\n"
  "s_mov_b32 s74, 1048576\n"
  "buffer_load_dwordx4 v8, s[40:43], s74 offen lds\n"
  "s_add_u32 m0, s57, 36864\n"
  "s_mov_b32 s74, 1179648\n"
  "buffer_load_dwordx4 v8, s[40:43], s74 offen lds\n"
  "s_add_u32 m0, s57, 40960\n"
  "s_mov_b32 s74, 1310720\n"
  "buffer_load_dwordx4 v8, s[40:43], s74 offen lds\n"
  "s_add_u32 m0, s57, 45056\n"
  "s_mov_b32 s74, 1441792\n"
  "buffer_load_dwordx4 v8, s[40:43], s74 offen lds\n"
  "s_add_u32 m0, s57, 49152\n"
  "s_mov_b32 s74, 1572864\n"
  "buffer_load_dwordx4 v8, s[40:43], s74 offen lds\n"
  "s_add_u32 m0, s57, 53248\n"
  "s_mov_b32 s74, 1703936\n"
  "buffer_load_dwordx4 v8, s[40:43], s74 offen lds\n"
  "s_add_u32 m0, s57, 57344\n"
  "s_mov_b32 s74, 1835008\n"
  "buffer_load_dwordx4 v8, s[40:43], s74 offen lds\n"
  "s_add_u32 m0, s57, 61440\n"
  "s_mov_b32 s74, 1966080\n"
  "buffer_load_dwordx4 v8, s[40:43], s74 offen lds\n"
  "s_add_u32 m0, s57, 65536\n"
  "s_mov_b32 s74, 2097152\n"
  "buffer_load_dwordx4 v8, s[40:43], s74 offen lds\n"
  "s_add_u32 m0, s57, 69632\n"
  "s_mov_b32 s74, 2228224\n"
  "buffer_load_dwordx4 v8, s[40:43], s74 offen lds\n"
  "s_add_u32 m0, s57, 73728\n"
  "s_mov_b32 s74, 2359296\n"
  "buffer_load_dwordx4 v8, s[40:43], s74 offen lds\n"
  "s_add_u32 m0, s57, 77824\n"
  "s_mov_b32 s74, 2490368\n"
  "buffer_load_dwordx4 v8, s[40:43], s74 offen lds\n"
  "s_add_u32 m0, s57, 81920\n"
  "s_mov_b32 s74, 2621440\n"
  "buffer_load_dwordx4 v8, s[40:43], s74 offen lds\n"
  "s_add_u32 m0, s57, 86016\n"
  "s_mov_b32 s74, 2752512\n"
  "buffer_load_dwordx4 v8, s[40:43], s74 offen lds\n"
  "s_add_u32 m0, s57, 90112\n"
  "s_mov_b32 s74, 2883584\n"
  "buffer_load_dwordx4 v8, s[40:43], s74 offen lds\n"
  "s_add_u32 m0, s57, 94208\n"
  "s_mov_b32 s74, 3014656\n"
  "buffer_load_dwordx4 v8, s[40:43], s74 offen lds\n"
  "s_waitcnt vmcnt(20) lgkmcnt(0)\n"
  "s_barrier\n"
  "s_add_u32 m0, s57, 98304\n"
  "s_mov_b32 s74, 3145728\n"
  "buffer_load_dwordx4 v8, s[40:43], s74 offen lds\n"
  "s_add_u32 m0, s57, 102400\n"
  "s_mov_b32 s74, 3276800\n"
  "buffer_load_dwordx4 v8, s[40:43], s74 offen lds\n"
  "s_add_u32 m0, s57, 106496\n"
  "s_mov_b32 s74, 3407872\n"
  "buffer_load_dwordx4 v8, s[40:43], s74 offen lds\n"
  "s_add_u32 m0, s57, 110592\n"
  "s_mov_b32 s74, 3538944\n"
  "buffer_load_dwordx4 v8, s[40:43], s74 offen lds\n"
  "v_add_u32 v4, 0, v9\n"
  "ds_read_b64_tr_b16 a[0:1], v4 offset:0\n"
  "ds_read_b64_tr_b16 a[2:3], v4 offset:1024\n"
  "ds_read_b64_tr_b16 a[4:5], v4 offset:8\n"
  "ds_read_b64_tr_b16 a[6:7], v4 offset:1032\n"
  "ds_read_b64_tr_b16 a[8:9], v4 offset:8192\n"
  "ds_read_b64_tr_b16 a[10:11], v4 offset:9216\n"
  "ds_read_b64_tr_b16 a[12:13], v4 offset:8200\n"
  "ds_read_b64_tr_b16 a[14:15], v4 offset:9224\n"
  "s_waitcnt vmcnt(20) lgkmcnt(0)\n"
  "s_barrier\n"
  "s_add_u32 m0, s57, 0\n"
  "s_mov_b32 s74, 3670016\n"
  "buffer_load_dwordx4 v8, s[40:43], s74 offen lds\n"
  "s_add_u32 m0, s57, 4096\n"
  "s_mov_b32 s74, 3801088\n"
  "buffer_load_dwordx4 v8, s[40:43], s74 offen lds\n"
  "s_add_u32 m0, s57, 8192\n"
  "s_mov_b32 s74, 3932160\n"
  "buffer_load_dwordx4 v8, s[40:43], s74 offen lds\n"
  "s_add_u32 m0, s57, 12288\n"
  "s_mov_b32 s74, 4063232\n"
  "buffer_load_dwordx4 v8, s[40:43], s74 offen lds\n"
  "v_add_u32 v4, 16384, v9\n"
  "ds_read_b64_tr_b16 a[16:17], v4 offset:0\n"
  "ds_read_b64_tr_b16 a[18:19], v4 offset:1024\n"
  "ds_read_b64_tr_b16 a[20:21], v4 offset:8\n"
  "ds_read_b64_tr_b16 a[22:23], v4 offset:1032\n"
  "ds_read_b64_tr_b16 a[24:25], v4 offset:8192\n"
  "ds_read_b64_tr_b16 a[26:27], v4 offset:9216\n"
  "ds_read_b64_tr_b16 a[28:29], v4 offset:8200\n"
  "ds_read_b64_tr_b16 a[30:31], v4 offset:9224\n"
  "s_waitcnt vmcnt(20) lgkmcnt(0)\n"
  "s_barrier\n"
  "s_add_u32 m0, s57, 16384\n"
  "s_mov_b32 s74, 4194304\n"
  "buffer_load_dwordx4 v8, s[40:43], s74 offen lds\n"
  "s_add_u32 m0, s57, 20480\n"
  "s_mov_b32 s74, 4325376\n"
  "buffer_load_dwordx4 v8, s[40:43], s74 offen lds\n"
  "s_add_u32 m0, s57, 24576\n"
  "s_mov_b32 s74, 4456448\n"
  "buffer_load_dwordx4 v8, s[40:43], s74 offen lds\n"
  "s_add_u32 m0, s57, 28672\n"
  "s_mov_b32 s74, 4587520\n"
  "buffer_load_dwordx4 v8, s[40:43], s74 offen lds\n"
  "v_add_u32 v4, 32768, v9\n"
  "ds_read_b64_tr_b16 a[32:33], v4 offset:0\n"
  "ds_read_b64_tr_b16 a[34:35], v4 offset:1024\n"
  "ds_read_b64_tr_b16 a[36:37], v4 offset:8\n"
  "ds_read_b64_tr_b16 a[38:39], v4 offset:1032\n"
  "ds_read_b64_tr_b16 a[40:41], v4 offset:8192\n"
  "ds_read_b64_tr_b16 a[42:43], v4 offset:9216\n"
  "ds_read_b64_tr_b16 a[44:45], v4 offset:8200\n"
  "ds_read_b64_tr_b16 a[46:47], v4 offset:9224\n"
  "s_waitcnt vmcnt(20) lgkmcnt(0)\n"
  "s_barrier\n"
  "s_add_u32 m0, s57, 32768\n"
  "s_mov_b32 s74, 256\n"
  "buffer_load_dwordx4 v8, s[40:43], s74 offen lds\n"
  "s_add_u32 m0, s57, 36864\n"
  "s_mov_b32 s74, 131328\n"
  "buffer_load_dwordx4 v8, s[40:43], s74 offen lds\n"
  "s_add_u32 m0, s57, 40960\n"
  "s_mov_b32 s74, 262400\n"
  "buffer_load_dwordx4 v8, s[40:43], s74 offen lds\n"
  "s_add_u32 m0, s57, 45056\n"
  "s_mov_b32 s74, 393472\n"
  "buffer_load_dwordx4 v8, s[40:43], s74 offen lds\n"
  "v_add_u32 v4, 49152, v9\n"
  "ds_read_b64_tr_b16 a[48:49], v4 offset:0\n"
  "ds_read_b64_tr_b16 a[50:51], v4 offset:1024\n"
  "ds_read_b64_tr_b16 a[52:53], v4 offset:8\n"
  "ds_read_b64_tr_b16 a[54:55], v4 offset:1032\n"
  "ds_read_b64_tr_b16 a[56:57], v4 offset:8192\n"
  "ds_read_b64_tr_b16 a[58:59], v4 offset:9216\n"
  "ds_read_b64_tr_b16 a[60:61], v4 offset:8200\n"
  "ds_read_b64_tr_b16 a[62:63], v4 offset:9224\n"
  "s_waitcnt vmcnt(20) lgkmcnt(0)\n"
  "s_barrier\n"
  "s_add_u32 m0, s57, 49152\n"
  "s_mov_b32 s74, 524544\n"
  "buffer_load_dwordx4 v8, s[40:43], s74 offen lds\n"
  "s_add_u32 m0, s57, 53248\n"
  "s_mov_b32 s74, 655616\n"
  "buffer_load_dwordx4 v8, s[40:43], s74 offen lds\n"
  "s_add_u32 m0, s57, 57344\n"
  "s_mov_b32 s74, 786688\n"
  "buffer_load_dwordx4 v8, s[40:43], s74 offen lds\n"
  "s_add_u32 m0, s57, 61440\n"
  "s_mov_b32 s74, 917760\n"
  "buffer_load_dwordx4 v8, s[40:43], s74 offen lds\n"
  "v_add_u32 v4, 65536, v9\n"
  "ds_read_b64_tr_b16 a[64:65], v4 offset:0\n"
  "ds_read_b64_tr_b16 a[66:67], v4 offset:1024\n"
  "ds_read_b64_tr_b16 a[68:69], v4 offset:8\n"
  "ds_read_b64_tr_b16 a[70:71], v4 offset:1032\n"
  "ds_read_b64_tr_b16 a[72:73], v4 offset:8192\n"
  "ds_read_b64_tr_b16 a[74:75], v4 offset:9216\n"
  "ds_read_b64_tr_b16 a[76:77], v4 offset:8200\n"
  "ds_read_b64_tr_b16 a[78:79], v4 offset:9224\n"
  "s_waitcnt vmcnt(20) lgkmcnt(0)\n"
  "s_barrier\n"
  "s_add_u32 m0, s57, 65536\n"
  "s_mov_b32 s74, 1048832\n"
  "buffer_load_dwordx4 v8, s[40:43], s74 offen lds\n"
  "s_add_u32 m0, s57, 69632\n"
  "s_mov_b32 s74, 1179904\n"
  "buffer_load_dwordx4 v8, s[40:43], s74 offen lds\n"
  "s_add_u32 m0, s57, 73728\n"
  "s_mov_b32 s74, 1310976\n"
  "buffer_load_dwordx4 v8, s[40:43], s74 offen lds\n"
  "s_add_u32 m0, s57, 77824\n"
  "s_mov_b32 s74, 1442048\n"
  "buffer_load_dwordx4 v8, s[40:43], s74 offen lds\n"
  "v_add_u32 v4, 81920, v9\n"
  "ds_read_b64_tr_b16 a[80:81], v4 offset:0\n"
  "ds_read_b64_tr_b16 a[82:83], v4 offset:1024\n"
  "ds_read_b64_tr_b16 a[84:85], v4 offset:8\n"
  "ds_read_b64_tr_b16 a[86:87], v4 offset:1032\n"
  "ds_read_b64_tr_b16 a[88:89], v4 offset:8192\n"
  "ds_read_b64_tr_b16 a[90:91], v4 offset:9216\n"
  "ds_read_b64_tr_b16 a[92:93], v4 offset:8200\n"
  "ds_read_b64_tr_b16 a[94:95], v4 offset:9224\n"
  "s_waitcnt vmcnt(20) lgkmcnt(0)\n"
  "s_barrier\n"
  "s_add_u32 m0, s57, 81920\n"
  "s_mov_b32 s74, 1573120\n"
  "buffer_load_dwordx4 v8, s[40:43], s74 offen lds\n"
  "s_add_u32 m0, s57, 86016\n"
  "s_mov_b32 s74, 1704192\n"
  "buffer_load_dwordx4 v8, s[40:43], s74 offen lds\n"
  "s_add_u32 m0, s57, 90112\n"
  "s_mov_b32 s74, 1835264\n"
  "buffer_load_dwordx4 v8, s[40:43], s74 offen lds\n"
  "s_add_u32 m0, s57, 94208\n"
  "s_mov_b32 s74, 1966336\n"
  "buffer_load_dwordx4 v8, s[40:43], s74 offen lds\n"
  "v_add_u32 v4, 98304, v9\n"
  "ds_read_b64_tr_b16 a[96:97], v4 offset:0\n"
  "ds_read_b64_tr_b16 a[98:99], v4 offset:1024\n"
  "ds_read_b64_tr_b16 a[100:101], v4 offset:8\n"
  "ds_read_b64_tr_b16 a[102:103], v4 offset:1032\n"
  "ds_read_b64_tr_b16 a[104:105], v4 offset:8192\n"
  "ds_read_b64_tr_b16 a[106:107], v4 offset:9216\n"
  "ds_read_b64_tr_b16 a[108:109], v4 offset:8200\n"
  "ds_read_b64_tr_b16 a[110:111], v4 offset:9224\n"
  "s_waitcnt vmcnt(20) lgkmcnt(0)\n"
  "s_barrier\n"
  "s_add_u32 m0, s57, 98304\n"
  "s_mov_b32 s74, 2097408\n"
  "buffer_load_dwordx4 v8, s[40:43], s74 offen lds\n"
  "s_add_u32 m0, s57, 102400\n"
  "s_mov_b32 s74, 2228480\n"
  "buffer_load_dwordx4 v8, s[40:43], s74 offen lds\n"
  "s_add_u32 m0, s57, 106496\n"
  "s_mov_b32 s74, 2359552\n"
  "buffer_load_dwordx4 v8, s[40:43], s74 offen lds\n"
  "s_add_u32 m0, s57, 110592\n"
  "s_mov_b32 s74, 2490624\n"
  "buffer_load_dwordx4 v8, s[40:43], s74 offen lds\n"
  "v_add_u32 v4, 0, v9\n"
  "ds_read_b64_tr_b16 a[112:113], v4 offset:0\n"
  "ds_read_b64_tr_b16 a[114:115], v4 offset:1024\n"
  "ds_read_b64_tr_b16 a[116:117], v4 offset:8\n"
  "ds_read_b64_tr_b16 a[118:119], v4 offset:1032\n"
  "ds_read_b64_tr_b16 a[120:121], v4 offset:8192\n"
  "ds_read_b64_tr_b16 a[122:123], v4 offset:9216\n"
  "ds_read_b64_tr_b16 a[124:125], v4 offset:8200\n"
  "ds_read_b64_tr_b16 a[126:127], v4 offset:9224\n"
  "s_waitcnt vmcnt(20) lgkmcnt(0)\n"
  "s_barrier\n"
  "s_add_u32 m0, s57, 0\n"
  "s_mov_b32 s74, 2621696\n"
  "buffer_load_dwordx4 v8, s[40:43], s74 offen lds\n"
  "s_add_u32 m0, s57, 4096\n"
  "s_mov_b32 s74, 2752768\n"
  "buffer_load_dwordx4 v8, s[40:43], s74 offen lds\n"
  "s_add_u32 m0, s57, 8192\n"
  "s_mov_b32 s74, 2883840\n"
  "buffer_load_dwordx4 v8, s[40:43], s74 offen lds\n"
  "s_add_u32 m0, s57, 12288\n"
  "s_mov_b32 s74, 3014912\n"
  "buffer_load_dwordx4 v8, s[40:43], s74 offen lds\n"
  "v_add_u32 v4, 16384, v9\n"
  "ds_read_b64_tr_b16 a[128:129], v4 offset:0\n"
  "ds_read_b64_tr_b16 a[130:131], v4 offset:1024\n"
  "ds_read_b64_tr_b16 a[132:133], v4 offset:8\n"
  "ds_read_b64_tr_b16 a[134:135], v4 offset:1032\n"
  "ds_read_b64_tr_b16 a[136:137], v4 offset:8192\n"
  "ds_read_b64_tr_b16 a[138:139], v4 offset:9216\n"
  "ds_read_b64_tr_b16 a[140:141], v4 offset:8200\n"
  "ds_read_b64_tr_b16 a[142:143], v4 offset:9224\n"
  "s_waitcnt vmcnt(20) lgkmcnt(0)\n"
  "s_barrier\n"
  "s_add_u32 m0, s57, 16384\n"
  "s_mov_b32 s74, 3145984\n"
  "buffer_load_dwordx4 v8, s[40:43], s74 offen lds\n"
  "s_add_u32 m0, s57, 20480\n"
  "s_mov_b32 s74, 3277056\n"
  "buffer_load_dwordx4 v8, s[40:43], s74 offen lds\n"
  "s_add_u32 m0, s57, 24576\n"
  "s_mov_b32 s74, 3408128\n"
  "buffer_load_dwordx4 v8, s[40:43], s74 offen lds\n"
  "s_add_u32 m0, s57, 28672\n"
  "s_mov_b32 s74, 3539200\n"
  "buffer_load_dwordx4 v8, s[40:43], s74 offen lds\n"
  "v_add_u32 v4, 32768, v9\n"
  "ds_read_b64_tr_b16 a[144:145], v4 offset:0\n"
  "ds_read_b64_tr_b16 a[146:147], v4 offset:1024\n"
  "ds_read_b64_tr_b16 a[148:149], v4 offset:8\n"
  "ds_read_b64_tr_b16 a[150:151], v4 offset:1032\n"
  "ds_read_b64_tr_b16 a[152:153], v4 offset:8192\n"
  "ds_read_b64_tr_b16 a[154:155], v4 offset:9216\n"
  "ds_read_b64_tr_b16 a[156:157], v4 offset:8200\n"
  "ds_read_b64_tr_b16 a[158:159], v4 offset:9224\n"
  "s_waitcnt vmcnt(20) lgkmcnt(0)\n"
  "s_barrier\n"
  "s_add_u32 m0, s57, 32768\n"
  "s_mov_b32 s74, 3670272\n"
  "buffer_load_dwordx4 v8, s[40:43], s74 offen lds\n"
  "s_add_u32 m0, s57, 36864\n"
  "s_mov_b32 s74, 3801344\n"
  "buffer_load_dwordx4 v8, s[40:43], s74 offen lds\n"
  "s_add_u32 m0, s57, 40960\n"
  "s_mov_b32 s74, 3932416\n"
  "buffer_load_dwordx4 v8, s[40:43], s74 offen lds\n"
  "s_add_u32 m0, s57, 45056\n"
  "s_mov_b32 s74, 4063488\n"
  "buffer_load_dwordx4 v8, s[40:43], s74 offen lds\n"
  "v_add_u32 v4, 49152, v9\n"
  "ds_read_b64_tr_b16 a[160:161], v4 offset:0\n"
  "ds_read_b64_tr_b16 a[162:163], v4 offset:1024\n"
  "ds_read_b64_tr_b16 a[164:165], v4 offset:8\n"
  "ds_read_b64_tr_b16 a[166:167], v4 offset:1032\n"
  "ds_read_b64_tr_b16 a[168:169], v4 offset:8192\n"
  "ds_read_b64_tr_b16 a[170:171], v4 offset:9216\n"
  "ds_read_b64_tr_b16 a[172:173], v4 offset:8200\n"
  "ds_read_b64_tr_b16 a[174:175], v4 offset:9224\n"
  "s_waitcnt vmcnt(20) lgkmcnt(0)\n"
  "s_barrier\n"
  "s_add_u32 m0, s57, 49152\n"
  "s_mov_b32 s74, 4194560\n"
  "buffer_load_dwordx4 v8, s[40:43], s74 offen lds\n"
  "s_add_u32 m0, s57, 53248\n"
  "s_mov_b32 s74, 4325632\n"
  "buffer_load_dwordx4 v8, s[40:43], s74 offen lds\n"
  "s_add_u32 m0, s57, 57344\n"
  "s_mov_b32 s74, 4456704\n"
  "buffer_load_dwordx4 v8, s[40:43], s74 offen lds\n"
  "s_add_u32 m0, s57, 61440\n"
  "s_mov_b32 s74, 4587776\n"
  "buffer_load_dwordx4 v8, s[40:43], s74 offen lds\n"
  "v_add_u32 v4, 65536, v9\n"
  "ds_read_b64_tr_b16 a[176:177], v4 offset:0\n"
  "ds_read_b64_tr_b16 a[178:179], v4 offset:1024\n"
  "ds_read_b64_tr_b16 a[180:181], v4 offset:8\n"
  "ds_read_b64_tr_b16 a[182:183], v4 offset:1032\n"
  "ds_read_b64_tr_b16 a[184:185], v4 offset:8192\n"
  "ds_read_b64_tr_b16 a[186:187], v4 offset:9216\n"
  "ds_read_b64_tr_b16 a[188:189], v4 offset:8200\n"
  "ds_read_b64_tr_b16 a[190:191], v4 offset:9224\n"
  "s_waitcnt vmcnt(20) lgkmcnt(0)\n"
  "s_barrier\n"
  "s_add_u32 m0, s57, 65536\n"
  "s_add_u32 s74, s67, 0\n"
  "buffer_load_dwordx4 v8, s[40:43], s74 offen lds\n"
  "s_add_u32 m0, s57, 69632\n"
  "s_add_u32 s74, s67, 131072\n"
  "buffer_load_dwordx4 v8, s[40:43], s74 offen lds\n"
  "s_add_u32 m0, s57, 73728\n"
  "s_add_u32 s74, s67, 262144\n"
  "buffer_load_dwordx4 v8, s[40:43], s74 offen lds\n"
  "s_add_u32 m0, s57, 77824\n"
  "s_add_u32 s74, s67, 393216\n"
  "buffer_load_dwordx4 v8, s[40:43], s74 offen lds\n"
  "v_add_u32 v4, 81920, v9\n"
  "ds_read_b64_tr_b16 a[192:193], v4 offset:0\n"
  "ds_read_b64_tr_b16 a[194:195], v4 offset:1024\n"
  "ds_read_b64_tr_b16 a[196:197], v4 offset:8\n"
  "ds_read_b64_tr_b16 a[198:199], v4 offset:1032\n"
  "ds_read_b64_tr_b16 a[200:201], v4 offset:8192\n"
  "ds_read_b64_tr_b16 a[202:203], v4 offset:9216\n"
  "ds_read_b64_tr_b16 a[204:205], v4 offset:8200\n"
  "ds_read_b64_tr_b16 a[206:207], v4 offset:9224\n"
  "s_waitcnt vmcnt(20) lgkmcnt(0)\n"
  "s_barrier\n"
  "s_add_u32 m0, s57, 81920\n"
  "s_add_u32 s74, s67, 524288\n"
  "buffer_load_dwordx4 v8, s[40:43], s74 offen lds\n"
  "s_add_u32 m0, s57, 86016\n"
  "s_add_u32 s74, s67, 655360\n"
  "buffer_load_dwordx4 v8, s[40:43], s74 offen lds\n"
  "s_add_u32 m0, s57, 90112\n"
  "s_add_u32 s74, s67, 786432\n"
  "buffer_load_dwordx4 v8, s[40:43], s74 offen lds\n"
  "s_add_u32 m0, s57, 94208\n"
  "s_add_u32 s74, s67, 917504\n"
  "buffer_load_dwordx4 v8, s[40:43], s74 offen lds\n"
  "v_add_u32 v4, 98304, v9\n"
  "ds_read_b64_tr_b16 a[208:209], v4 offset:0\n"
  "ds_read_b64_tr_b16 a[210:211], v4 offset:1024\n"
  "ds_read_b64_tr_b16 a[212:213], v4 offset:8\n"
  "ds_read_b64_tr_b16 a[214:215], v4 offset:1032\n"
  "ds_read_b64_tr_b16 a[216:217], v4 offset:8192\n"
  "ds_read_b64_tr_b16 a[218:219], v4 offset:9216\n"
  "ds_read_b64_tr_b16 a[220:221], v4 offset:8200\n"
  "ds_read_b64_tr_b16 a[222:223], v4 offset:9224\n"
  "s_waitcnt vmcnt(20) lgkmcnt(0)\n"
  "s_barrier\n"
  "s_add_u32 m0, s57, 98304\n"
  "s_add_u32 s74, s67, 1048576\n"
  "buffer_load_dwordx4 v8, s[40:43], s74 offen lds\n"
  "s_add_u32 m0, s57, 102400\n"
  "s_add_u32 s74, s67, 1179648\n"
  "buffer_load_dwordx4 v8, s[40:43], s74 offen lds\n"
  "s_add_u32 m0, s57, 106496\n"
  "s_add_u32 s74, s67, 1310720\n"
  "buffer_load_dwordx4 v8, s[40:43], s74 offen lds\n"
  "s_add_u32 m0, s57, 110592\n"
  "s_add_u32 s74, s67, 1441792\n"
  "buffer_load_dwordx4 v8, s[40:43], s74 offen lds\n"
  "v_add_u32 v4, 0, v9\n"
  "ds_read_b64_tr_b16 a[224:225], v4 offset:0\n"
  "ds_read_b64_tr_b16 a[226:227], v4 offset:1024\n"
  "ds_read_b64_tr_b16 a[228:229], v4 offset:8\n"
  "ds_read_b64_tr_b16 a[230:231], v4 offset:1032\n"
  "ds_read_b64_tr_b16 a[232:233], v4 offset:8192\n"
  "ds_read_b64_tr_b16 a[234:235], v4 offset:9216\n"
  "ds_read_b64_tr_b16 a[236:237], v4 offset:8200\n"
  "ds_read_b64_tr_b16 a[238:239], v4 offset:9224\n"
  "s_waitcnt vmcnt(20) lgkmcnt(0)\n"
  "s_barrier\n"
  "s_add_u32 m0, s57, 0\n"
  "s_add_u32 s74, s67, 1572864\n"
  "buffer_load_dwordx4 v8, s[40:43], s74 offen lds\n"
  "s_add_u32 m0, s57, 4096\n"
  "s_add_u32 s74, s67, 1703936\n"
  "buffer_load_dwordx4 v8, s[40:43], s74 offen lds\n"
  "s_add_u32 m0, s57, 8192\n"
  "s_add_u32 s74, s67, 1835008\n"
  "buffer_load_dwordx4 v8, s[40:43], s74 offen lds\n"
  "s_add_u32 m0, s57, 12288\n"
  "s_add_u32 s74, s67, 1966080\n"
  "buffer_load_dwordx4 v8, s[40:43], s74 offen lds\n"
  "v_add_u32 v4, 16384, v9\n"
  "ds_read_b64_tr_b16 a[240:241], v4 offset:0\n"
  "ds_read_b64_tr_b16 a[242:243], v4 offset:1024\n"
  "ds_read_b64_tr_b16 a[244:245], v4 offset:8\n"
  "ds_read_b64_tr_b16 a[246:247], v4 offset:1032\n"
  "ds_read_b64_tr_b16 a[248:249], v4 offset:8192\n"
  "ds_read_b64_tr_b16 a[250:251], v4 offset:9216\n"
  "ds_read_b64_tr_b16 a[252:253], v4 offset:8200\n"
  "ds_read_b64_tr_b16 a[254:255], v4 offset:9224\n"
  "s_waitcnt vmcnt(20) lgkmcnt(0)\n"
  "s_barrier\n"
  "s_add_u32 m0, s57, 16384\n"
  "s_add_u32 s74, s67, 2097152\n"
  "buffer_load_dwordx4 v8, s[40:43], s74 offen lds\n"
  "s_add_u32 m0, s57, 20480\n"
  "s_add_u32 s74, s67, 2228224\n"
  "buffer_load_dwordx4 v8, s[40:43], s74 offen lds\n"
  "s_add_u32 m0, s57, 24576\n"
  "s_add_u32 s74, s67, 2359296\n"
  "buffer_load_dwordx4 v8, s[40:43], s74 offen lds\n"
  "s_add_u32 m0, s57, 28672\n"
  "s_add_u32 s74, s67, 2490368\n"
  "buffer_load_dwordx4 v8, s[40:43], s74 offen lds\n"
  "v_add_u32 v4, 32768, v9\n"
  "ds_read_b64_tr_b16 v[80:81], v4 offset:0\n"
  "ds_read_b64_tr_b16 v[82:83], v4 offset:1024\n"
  "ds_read_b64_tr_b16 v[84:85], v4 offset:8\n"
  "ds_read_b64_tr_b16 v[86:87], v4 offset:1032\n"
  "ds_read_b64_tr_b16 v[88:89], v4 offset:8192\n"
  "ds_read_b64_tr_b16 v[90:91], v4 offset:9216\n"
  "ds_read_b64_tr_b16 v[92:93], v4 offset:8200\n"
  "ds_read_b64_tr_b16 v[94:95], v4 offset:9224\n"
  "s_waitcnt vmcnt(20) lgkmcnt(0)\n"
  "s_barrier\n"
  "s_add_u32 m0, s57, 32768\n"
  "s_add_u32 s74, s67, 2621440\n"
  "buffer_load_dwordx4 v8, s[40:43], s74 offen lds\n"
  "s_add_u32 m0, s57, 36864\n"
  "s_add_u32 s74, s67, 2752512\n"
  "buffer_load_dwordx4 v8, s[40:43], s74 offen lds\n"
  "s_add_u32 m0, s57, 40960\n"
  "s_add_u32 s74, s67, 2883584\n"
  "buffer_load_dwordx4 v8, s[40:43], s74 offen lds\n"
  "s_add_u32 m0, s57, 45056\n"
  "s_add_u32 s74, s67, 3014656\n"
  "buffer_load_dwordx4 v8, s[40:43], s74 offen lds\n"
  "v_add_u32 v4, 49152, v9\n"
  "ds_read_b64_tr_b16 v[96:97], v4 offset:0\n"
  "ds_read_b64_tr_b16 v[98:99], v4 offset:1024\n"
  "ds_read_b64_tr_b16 v[100:101], v4 offset:8\n"
  "ds_read_b64_tr_b16 v[102:103], v4 offset:1032\n"
  "ds_read_b64_tr_b16 v[104:105], v4 offset:8192\n"
  "ds_read_b64_tr_b16 v[106:107], v4 offset:9216\n"
  "ds_read_b64_tr_b16 v[108:109], v4 offset:8200\n"
  "ds_read_b64_tr_b16 v[110:111], v4 offset:9224\n"
  "s_waitcnt vmcnt(20) lgkmcnt(0)\n"
  "s_barrier\n"
  "s_add_u32 m0, s57, 49152\n"
  "s_add_u32 s74, s67, 3145728\n"
  "buffer_load_dwordx4 v8, s[40:43], s74 offen lds\n"
  "s_add_u32 m0, s57, 53248\n"
  "s_add_u32 s74, s67, 3276800\n"
  "buffer_load_dwordx4 v8, s[40:43], s74 offen lds\n"
  "s_add_u32 m0, s57, 57344\n"
  "s_add_u32 s74, s67, 3407872\n"
  "buffer_load_dwordx4 v8, s[40:43], s74 offen lds\n"
  "s_add_u32 m0, s57, 61440\n"
  "s_add_u32 s74, s67, 3538944\n"
  "buffer_load_dwordx4 v8, s[40:43], s74 offen lds\n"
  "v_add_u32 v4, 65536, v9\n"
  "ds_read_b64_tr_b16 v[112:113], v4 offset:0\n"
  "ds_read_b64_tr_b16 v[114:115], v4 offset:1024\n"
  "ds_read_b64_tr_b16 v[116:117], v4 offset:8\n"
  "ds_read_b64_tr_b16 v[118:119], v4 offset:1032\n"
  "ds_read_b64_tr_b16 v[120:121], v4 offset:8192\n"
  "ds_read_b64_tr_b16 v[122:123], v4 offset:9216\n"
  "ds_read_b64_tr_b16 v[124:125], v4 offset:8200\n"
  "ds_read_b64_tr_b16 v[126:127], v4 offset:9224\n"
  "s_waitcnt vmcnt(20) lgkmcnt(0)\n"
  "s_barrier\n"
  "s_add_u32 m0, s57, 65536\n"
  "s_add_u32 s74, s67, 3670016\n"
  "buffer_load_dwordx4 v8, s[40:43], s74 offen lds\n"
  "s_add_u32 m0, s57, 69632\n"
  "s_add_u32 s74, s67, 3801088\n"
  "buffer_load_dwordx4 v8, s[40:43], s74 offen lds\n"
  "s_add_u32 m0, s57, 73728\n"
  "s_add_u32 s74, s67, 3932160\n"
  "buffer_load_dwordx4 v8, s[40:43], s74 offen lds\n"
  "s_add_u32 m0, s57, 77824\n"
  "s_add_u32 s74, s67, 4063232\n"
  "buffer_load_dwordx4 v8, s[40:43], s74 offen lds\n"
  "v_add_u32 v4, 81920, v9\n"
  "ds_read_b64_tr_b16 v[128:129], v4 offset:0\n"
  "ds_read_b64_tr_b16 v[130:131], v4 offset:1024\n"
  "ds_read_b64_tr_b16 v[132:133], v4 offset:8\n"
  "ds_read_b64_tr_b16 v[134:135], v4 offset:1032\n"
  "ds_read_b64_tr_b16 v[136:137], v4 offset:8192\n"
  "ds_read_b64_tr_b16 v[138:139], v4 offset:9216\n"
  "ds_read_b64_tr_b16 v[140:141], v4 offset:8200\n"
  "ds_read_b64_tr_b16 v[142:143], v4 offset:9224\n"
  "s_waitcnt vmcnt(20) lgkmcnt(0)\n"
  "s_barrier\n"
  "s_add_u32 m0, s57, 81920\n"
  "s_add_u32 s74, s67, 4194304\n"
  "buffer_load_dwordx4 v8, s[40:43], s74 offen lds\n"
  "s_add_u32 m0, s57, 86016\n"
  "s_add_u32 s74, s67, 4325376\n"
  "buffer_load_dwordx4 v8, s[40:43], s74 offen lds\n"
  "s_add_u32 m0, s57, 90112\n"
  "s_add_u32 s74, s67, 4456448\n"
  "buffer_load_dwordx4 v8, s[40:43], s74 offen lds\n"
  "s_add_u32 m0, s57, 94208\n"
  "s_add_u32 s74, s67, 4587520\n"
  "buffer_load_dwordx4 v8, s[40:43], s74 offen lds\n"
  "v_add_u32 v4, 98304, v9\n"
  "ds_read_b64_tr_b16 v[144:145], v4 offset:0\n"
  "ds_read_b64_tr_b16 v[146:147], v4 offset:1024\n"
  "ds_read_b64_tr_b16 v[148:149], v4 offset:8\n"
  "ds_read_b64_tr_b16 v[150:151], v4 offset:1032\n"
  "ds_read_b64_tr_b16 v[152:153], v4 offset:8192\n"
  "ds_read_b64_tr_b16 v[154:155], v4 offset:9216\n"
  "ds_read_b64_tr_b16 v[156:157], v4 offset:8200\n"
  "ds_read_b64_tr_b16 v[158:159], v4 offset:9224\n"
  "s_waitcnt vmcnt(20) lgkmcnt(0)\n"
  "s_barrier\n"
  "v_add_u32 v4, 0, v9\n"
  "ds_read_b64_tr_b16 v[160:161], v4 offset:0\n"
  "ds_read_b64_tr_b16 v[162:163], v4 offset:1024\n"
  "ds_read_b64_tr_b16 v[164:165], v4 offset:8\n"
  "ds_read_b64_tr_b16 v[166:167], v4 offset:1032\n"
  "ds_read_b64_tr_b16 v[168:169], v4 offset:8192\n"
  "ds_read_b64_tr_b16 v[170:171], v4 offset:9216\n"
  "ds_read_b64_tr_b16 v[172:173], v4 offset:8200\n"
  "ds_read_b64_tr_b16 v[174:175], v4 offset:9224\n"
  "s_waitcnt vmcnt(16) lgkmcnt(0)\n"
  "s_barrier\n"
  "v_add_u32 v4, 16384, v9\n"
  "ds_read_b64_tr_b16 v[176:177], v4 offset:0\n"
  "ds_read_b64_tr_b16 v[178:179], v4 offset:1024\n"
  "ds_read_b64_tr_b16 v[180:181], v4 offset:8\n"
  "ds_read_b64_tr_b16 v[182:183], v4 offset:1032\n"
  "ds_read_b64_tr_b16 v[184:185], v4 offset:8192\n"
  "ds_read_b64_tr_b16 v[186:187], v4 offset:9216\n"
  "ds_read_b64_tr_b16 v[188:189], v4 offset:8200\n"
  "ds_read_b64_tr_b16 v[190:191], v4 offset:9224\n"
  "s_waitcnt vmcnt(12) lgkmcnt(0)\n"
  "s_barrier\n"
  "v_add_u32 v4, 32768, v9\n"
  "ds_read_b64_tr_b16 v[192:193], v4 offset:0\n"
  "ds_read_b64_tr_b16 v[194:195], v4 offset:1024\n"
  "ds_read_b64_tr_b16 v[196:197], v4 offset:8\n"
  "ds_read_b64_tr_b16 v[198:199], v4 offset:1032\n"
  "ds_read_b64_tr_b16 v[200:201], v4 offset:8192\n"
  "ds_read_b64_tr_b16 v[202:203], v4 offset:9216\n"
  "ds_read_b64_tr_b16 v[204:205], v4 offset:8200\n"
  "ds_read_b64_tr_b16 v[206:207], v4 offset:9224\n"
  "s_waitcnt vmcnt(8) lgkmcnt(0)\n"
  "s_barrier\n"
  "v_add_u32 v4, 49152, v9\n"
  "ds_read_b64_tr_b16 v[208:209], v4 offset:0\n"
  "ds_read_b64_tr_b16 v[210:211], v4 offset:1024\n"
  "ds_read_b64_tr_b16 v[212:213], v4 offset:8\n"
  "ds_read_b64_tr_b16 v[214:215], v4 offset:1032\n"
  "ds_read_b64_tr_b16 v[216:217], v4 offset:8192\n"
  "ds_read_b64_tr_b16 v[218:219], v4 offset:9216\n"
  "ds_read_b64_tr_b16 v[220:221], v4 offset:8200\n"
  "ds_read_b64_tr_b16 v[222:223], v4 offset:9224\n"
  "s_waitcnt vmcnt(4) lgkmcnt(0)\n"
  "s_barrier\n"
  "v_add_u32 v4, 65536, v9\n"
  "ds_read_b64_tr_b16 v[224:225], v4 offset:0\n"
  "ds_read_b64_tr_b16 v[226:227], v4 offset:1024\n"
  "ds_read_b64_tr_b16 v[228:229], v4 offset:8\n"
  "ds_read_b64_tr_b16 v[230:231], v4 offset:1032\n"
  "ds_read_b64_tr_b16 v[232:233], v4 offset:8192\n"
  "ds_read_b64_tr_b16 v[234:235], v4 offset:9216\n"
  "ds_read_b64_tr_b16 v[236:237], v4 offset:8200\n"
  "ds_read_b64_tr_b16 v[238:239], v4 offset:9224\n"
  "s_waitcnt vmcnt(0) lgkmcnt(0)\n"
  "s_barrier\n"
  "v_add_u32 v4, 81920, v9\n"
  "ds_read_b64_tr_b16 v[240:241], v4 offset:0\n"
  "ds_read_b64_tr_b16 v[242:243], v4 offset:1024\n"
  "ds_read_b64_tr_b16 v[244:245], v4 offset:8\n"
  "ds_read_b64_tr_b16 v[246:247], v4 offset:1032\n"
  "ds_read_b64_tr_b16 v[248:249], v4 offset:8192\n"
  "ds_read_b64_tr_b16 v[250:251], v4 offset:9216\n"
  "ds_read_b64_tr_b16 v[252:253], v4 offset:8200\n"
  "ds_read_b64_tr_b16 v[254:255], v4 offset:9224\n"
  "s_waitcnt lgkmcnt(0)\n"
  "s_barrier\n"
  "v_lshrrev_b32 v4, 2, v0\n"
  "v_add_u32 v4, 163584, v4\n"
  "v_mov_b32 v6, 0\n"
  "ds_write_b32 v4, v6\n"
  "v_min_u32 v5, 1, v0\n"
  "v_lshlrev_b32 v5, 30, v5\n"
  "s_mov_b32 s70, 0\n"
  "s_mov_b32 s71, 0\n"
  "s_mov_b32 s72, 0\n"
  "s_mov_b32 s81, 0\n"
  "s_mov_b32 s82, 0\n"
  "s_mov_b32 s83, 0\n"
  "s_mov_b32 s95, -1\n"
  "s_cmp_eq_u32 s57, 0\n"
  "s_cselect_b32 s92, s58, 0x80000000\n"
  "s_waitcnt lgkmcnt(0)\n"
  "s_barrier\n"
  "s_branch L_first_%=\n"
  "L_pf_%=:\n"
  "s_waitcnt vmcnt(9)\n"
  "L_go_%=:\n"
  "s_barrier\n"
  "ds_read_b128 v[8:11], v1 offset:0\n"
  "ds_read_b128 v[12:15], v1 offset:1024\n"
  "s_waitcnt lgkmcnt(1)\n"
  "v_mfma_f32_16x16x32_bf16 v[32:35], v[8:11], a[0:3], 0\n"
  "ds_read_b128 v[16:19], v1 offset:2048\n"
  "v_mfma_f32_16x16x32_bf16 v[40:43], v[8:11], a[4:7], 0\n"
  "v_mfma_f32_16x16x32_bf16 v[48:51], v[8:11], a[144:147], 0\n"
  "v_mfma_f32_16x16x32_bf16 v[56:59], v[8:11], a[148:151], 0\n"
  "v_mfma_f32_16x16x32_bf16 v[64:67], v[8:11], v[112:115], 0\n"
  "v_mfma_f32_16x16x32_bf16 v[72:75], v[8:11], v[116:119], 0\n"
  "s_waitcnt lgkmcnt(1)\n"
  "v_mfma_f32_16x16x32_bf16 v[32:35], v[12:15], a[8:11], v[32:35]\n"
  "ds_read_b128 v[20:23], v1 offset:3072\n"
  "v_mfma_f32_16x16x32_bf16 v[40:43], v[12:15], a[12:15], v[40:43]\n"
  "s_add_u32 m0, s79, 20480\n"
  "s_add_u32 s76, s78, 256\n"
  "v_mfma_f32_16x16x32_bf16 v[48:51], v[12:15], a[152:155], v[48:51]\n"
  "buffer_load_dwordx4 v7, s[36:39], s76 offen sc1 lds\n"
  "v_mfma_f32_16x16x32_bf16 v[56:59], v[12:15], a[156:159], v[56:59]\n"
  "v_mfma_f32_16x16x32_bf16 v[64:67], v[12:15], v[120:123], v[64:67]\n"
  "v_mfma_f32_16x16x32_bf16 v[72:75], v[12:15], v[124:127], v[72:75]\n"
  "s_waitcnt lgkmcnt(1)\n"
  "v_mfma_f32_16x16x32_bf16 v[32:35], v[16:19], a[16:19], v[32:35]\n"
  "ds_read_b128 v[24:27], v1 offset:4096\n"
  "v_mfma_f32_16x16x32_bf16 v[40:43], v[16:19], a[20:23], v[40:43]\n"
  "s_add_u32 m0, s79, 24576\n"
  "s_add_u32 s76, s78, 16640\n"
  "v_mfma_f32_16x16x32_bf16 v[48:51], v[16:19], a[160:163], v[48:51]\n"
  "buffer_load_dwordx4 v7, s[36:39], s76 offen sc1 lds\n"
  "v_mfma_f32_16x16x32_bf16 v[56:59], v[16:19], a[164:167], v[56:59]\n"
  "v_mfma_f32_16x16x32_bf16 v[64:67], v[16:19], v[128:131], v[64:67]\n"
  "v_mfma_f32_16x16x32_bf16 v[72:75], v[16:19], v[132:135], v[72:75]\n"
  "s_waitcnt lgkmcnt(1)\n"
  "v_mfma_f32_16x16x32_bf16 v[32:35], v[20:23], a[24:27], v[32:35]\n"
  "ds_read_b128 v[28:31], v1 offset:5120\n"
  "v_mfma_f32_16x16x32_bf16 v[40:43], v[20:23], a[28:31], v[40:43]\n"
  "s_add_u32 m0, s79, 28672\n"
  "s_add_u32 s76, s78, 33024\n"
  "v_mfma_f32_16x16x32_bf16 v[48:51], v[20:23], a[168:171], v[48:51]\n"
  "buffer_load_dwordx4 v7, s[36:39], s76 offen sc1 lds\n"
  "v_mfma_f32_16x16x32_bf16 v[56:59], v[20:23], a[172:175], v[56:59]\n"
  "v_mfma_f32_16x16x32_bf16 v[64:67], v[20:23], v[136:139], v[64:67]\n"
  "v_mfma_f32_16x16x32_bf16 v[72:75], v[20:23], v[140:143], v[72:75]\n"
  "s_waitcnt lgkmcnt(1)\n"
  "v_mfma_f32_16x16x32_bf16 v[32:35], v[24:27], a[32:35], v[32:35]\n"
  "ds_read_b128 v[8:11], v1 offset:6144\n"
  "v_mfma_f32_16x16x32_bf16 v[40:43], v[24:27], a[36:39], v[40:43]\n"
  "s_add_u32 m0, s79, 32768\n"
  "s_add_u32 s76, s78, 49408\n"
  "v_mfma_f32_16x16x32_bf16 v[48:51], v[24:27], a[176:179], v[48:51]\n"
  "buffer_load_dwordx4 v7, s[36:39], s76 offen sc1 lds\n"
  "v_mfma_f32_16x16x32_bf16 v[56:59], v[24:27], a[180:183], v[56:59]\n"
  "v_mfma_f32_16x16x32_bf16 v[64:67], v[24:27], v[144:147], v[64:67]\n"
  "v_mfma_f32_16x16x32_bf16 v[72:75], v[24:27], v[148:151], v[72:75]\n"
  "s_waitcnt lgkmcnt(1)\n"
  "v_mfma_f32_16x16x32_bf16 v[32:35], v[28:31], a[40:43], v[32:35]\n"
  "ds_read_b128 v[12:15], v1 offset:7168\n"
  "v_mfma_f32_16x16x32_bf16 v[40:43], v[28:31], a[44:47], v[40:43]\n"
  "v_mfma_f32_16x16x32_bf16 v[48:51], v[28:31], a[184:187], v[48:51]\n"
  "v_mfma_f32_16x16x32_bf16 v[56:59], v[28:31], a[188:191], v[56:59]\n"
  "v_mfma_f32_16x16x32_bf16 v[64:67], v[28:31], v[152:155], v[64:67]\n"
  "v_mfma_f32_16x16x32_bf16 v[72:75], v[28:31], v[156:159], v[72:75]\n"
  "s_waitcnt lgkmcnt(1)\n"
  "v_mfma_f32_16x16x32_bf16 v[32:35], v[8:11], a[48:51], v[32:35]\n"
  "ds_read_b128 v[16:19], v1 offset:8192\n"
  "v_mfma_f32_16x16x32_bf16 v[40:43], v[8:11], a[52:55], v[40:43]\n"
  "v_mfma_f32_16x16x32_bf16 v[48:51], v[8:11], a[192:195], v[48:51]\n"
  "v_mfma_f32_16x16x32_bf16 v[56:59], v[8:11], a[196:199], v[56:59]\n"
  "v_mfma_f32_16x16x32_bf16 v[64:67], v[8:11], v[160:163], v[64:67]\n"
  "v_mfma_f32_16x16x32_bf16 v[72:75], v[8:11], v[164:167], v[72:75]\n"
  "s_add_u32 s75, s70, -1\n"
  "v_mov_b32 v6, s75\n"
  "s_waitcnt lgkmcnt(1)\n"
  "v_mfma_f32_16x16x32_bf16 v[32:35], v[12:15], a[56:59], v[32:35]\n"
  "ds_read_b128 v[20:23], v1 offset:9216\n"
  "v_mfma_f32_16x16x32_bf16 v[40:43], v[12:15], a[60:63], v[40:43]\n"
  "v_mfma_f32_16x16x32_bf16 v[48:51], v[12:15], a[200:203], v[48:51]\n"
  "s_waitcnt vmcnt(4)\n"
  "s_barrier\n"
  "v_mfma_f32_16x16x32_bf16 v[56:59], v[12:15], a[204:207], v[56:59]\n"
  "buffer_store_dword v6, v5, s[36:39], s92 offen sc1\n"
  "v_mfma_f32_16x16x32_bf16 v[64:67], v[12:15], v[168:171], v[64:67]\n"
  "v_mfma_f32_16x16x32_bf16 v[72:75], v[12:15], v[172:175], v[72:75]\n"
  "s_waitcnt lgkmcnt(1)\n"
  "v_mfma_f32_16x16x32_bf16 v[32:35], v[16:19], a[64:67], v[32:35]\n"
  "ds_read_b128 v[24:27], v1 offset:10240\n"
  "v_mfma_f32_16x16x32_bf16 v[40:43], v[16:19], a[68:71], v[40:43]\n"
  "v_mfma_f32_16x16x32_bf16 v[48:51], v[16:19], a[208:211], v[48:51]\n"
  "v_mfma_f32_16x16x32_bf16 v[56:59], v[16:19], a[212:215], v[56:59]\n"
  "v_mfma_f32_16x16x32_bf16 v[64:67], v[16:19], v[176:179], v[64:67]\n"
  "v_mfma_f32_16x16x32_bf16 v[72:75], v[16:19], v[180:183], v[72:75]\n"
  "s_waitcnt lgkmcnt(1)\n"
  "v_mfma_f32_16x16x32_bf16 v[32:35], v[20:23], a[72:75], v[32:35]\n"
  "ds_read_b128 v[28:31], v1 offset:11264\n"
  "v_mfma_f32_16x16x32_bf16 v[40:43], v[20:23], a[76:79], v[40:43]\n"
  "v_mfma_f32_16x16x32_bf16 v[48:51], v[20:23], a[216:219], v[48:51]\n"
  "v_mfma_f32_16x16x32_bf16 v[56:59], v[20:23], a[220:223], v[56:59]\n"
  "v_mfma_f32_16x16x32_bf16 v[64:67], v[20:23], v[184:187], v[64:67]\n"
  "v_mfma_f32_16x16x32_bf16 v[72:75], v[20:23], v[188:191], v[72:75]\n"
  "s_waitcnt lgkmcnt(1)\n"
  "v_mfma_f32_16x16x32_bf16 v[32:35], v[24:27], a[80:83], v[32:35]\n"
  "ds_read_b128 v[8:11], v1 offset:12288\n"
  "v_mfma_f32_16x16x32_bf16 v[40:43], v[24:27], a[84:87], v[40:43]\n"
  "v_mfma_f32_16x16x32_bf16 v[48:51], v[24:27], a[224:227], v[48:51]\n"
  "s_add_u32 s78, s70, 1\n"
  "s_and_b32 s78, s78, s61\n"
  "v_mfma_f32_16x16x32_bf16 v[56:59], v[24:27], a[228:231], v[56:59]\n"
  "s_mul_i32 s78, s78, s60\n"
  "s_lshr_b32 s76, s78, 8\n"
  "v_mfma_f32_16x16x32_bf16 v[64:67], v[24:27], v[192:195], v[64:67]\n"
  "s_and_b32 s76, s76, 8192\n"
  "s_add_u32 s78, s78, s59\n"
  "v_mfma_f32_16x16x32_bf16 v[72:75], v[24:27], v[196:199], v[72:75]\n"
  "s_xor_b32 s78, s78, s76\n"
  "s_xor_b32 s79, s81, 65536\n"
  "s_waitcnt lgkmcnt(1)\n"
  "v_mfma_f32_16x16x32_bf16 v[32:35], v[28:31], a[88:91], v[32:35]\n"
  "ds_read_b128 v[12:15], v1 offset:13312\n"
  "s_add_u32 s79, s79, s57\n"
  "s_add_u32 s80, s70, 0\n"
  "v_mfma_f32_16x16x32_bf16 v[40:43], v[28:31], a[92:95], v[40:43]\n"
  "s_and_b32 s80, s80, 15\n"
  "s_lshl_b32 s80, s80, 22\n"
  "v_mfma_f32_16x16x32_bf16 v[48:51], v[28:31], a[232:235], v[48:51]\n"
  "s_add_u32 s80, s80, s62\n"
  "v_mfma_f32_16x16x32_bf16 v[56:59], v[28:31], a[236:239], v[56:59]\n"
  "v_mfma_f32_16x16x32_bf16 v[64:67], v[28:31], v[200:203], v[64:67]\n"
  "v_mfma_f32_16x16x32_bf16 v[72:75], v[28:31], v[204:207], v[72:75]\n"
  "s_waitcnt lgkmcnt(1)\n"
  "v_mfma_f32_16x16x32_bf16 v[32:35], v[8:11], a[96:99], v[32:35]\n"
  "ds_read_b128 v[16:19], v1 offset:14336\n"
  "v_mfma_f32_16x16x32_bf16 v[40:43], v[8:11], a[100:103], v[40:43]\n"
  "v_mfma_f32_16x16x32_bf16 v[48:51], v[8:11], a[240:243], v[48:51]\n"
  "v_mfma_f32_16x16x32_bf16 v[56:59], v[8:11], a[244:247], v[56:59]\n"
  "v_mfma_f32_16x16x32_bf16 v[64:67], v[8:11], v[208:211], v[64:67]\n"
  "v_mfma_f32_16x16x32_bf16 v[72:75], v[8:11], v[212:215], v[72:75]\n"
  "s_waitcnt lgkmcnt(1)\n"
  "v_mfma_f32_16x16x32_bf16 v[32:35], v[12:15], a[104:107], v[32:35]\n"
  "ds_read_b128 v[20:23], v1 offset:15360\n"
  "v_mfma_f32_16x16x32_bf16 v[40:43], v[12:15], a[108:111], v[40:43]\n"
  "v_mfma_f32_16x16x32_bf16 v[48:51], v[12:15], a[248:251], v[48:51]\n"
  "v_mfma_f32_16x16x32_bf16 v[56:59], v[12:15], a[252:255], v[56:59]\n"
  "v_mfma_f32_16x16x32_bf16 v[64:67], v[12:15], v[216:219], v[64:67]\n"
  "v_mfma_f32_16x16x32_bf16 v[72:75], v[12:15], v[220:223], v[72:75]\n"
  "s_waitcnt lgkmcnt(1)\n"
  "v_mfma_f32_16x16x32_bf16 v[32:35], v[16:19], a[112:115], v[32:35]\n"
  "ds_read_b128 v[24:27], v1 offset:16384\n"
  "v_mfma_f32_16x16x32_bf16 v[40:43], v[16:19], a[116:119], v[40:43]\n"
  "v_mfma_f32_16x16x32_bf16 v[48:51], v[16:19], v[80:83], v[48:51]\n"
  "v_mfma_f32_16x16x32_bf16 v[56:59], v[16:19], v[84:87], v[56:59]\n"
  "v_mfma_f32_16x16x32_bf16 v[64:67], v[16:19], v[224:227], v[64:67]\n"
  "v_mfma_f32_16x16x32_bf16 v[72:75], v[16:19], v[228:231], v[72:75]\n"
  "s_waitcnt lgkmcnt(1)\n"
  "v_mfma_f32_16x16x32_bf16 v[32:35], v[20:23], a[120:123], v[32:35]\n"
  "ds_read_b128 v[28:31], v1 offset:17408\n"
  "v_mfma_f32_16x16x32_bf16 v[40:43], v[20:23], a[124:127], v[40:43]\n"
  "v_mfma_f32_16x16x32_bf16 v[48:51], v[20:23], v[88:91], v[48:51]\n"
  "v_mfma_f32_16x16x32_bf16 v[56:59], v[20:23], v[92:95], v[56:59]\n"
  "v_mfma_f32_16x16x32_bf16 v[64:67], v[20:23], v[232:235], v[64:67]\n"
  "v_mfma_f32_16x16x32_bf16 v[72:75], v[20:23], v[236:239], v[72:75]\n"
  "s_waitcnt lgkmcnt(1)\n"
  "v_mfma_f32_16x16x32_bf16 v[32:35], v[24:27], a[128:131], v[32:35]\n"
  "s_waitcnt vmcnt(1)\n"
  "s_barrier\n"
  "ds_read_b128 v[8:11], v1 offset:20480\n"
  "v_mfma_f32_16x16x32_bf16 v[40:43], v[24:27], a[132:135], v[40:43]\n"
  "v_mfma_f32_16x16x32_bf16 v[48:51], v[24:27], v[96:99], v[48:51]\n"
  "v_mfma_f32_16x16x32_bf16 v[56:59], v[24:27], v[100:103], v[56:59]\n"
  "buffer_load_dword v4, v2, s[36:39], 0 offen sc1\n"
  "v_mfma_f32_16x16x32_bf16 v[64:67], v[24:27], v[240:243], v[64:67]\n"
  "v_mfma_f32_16x16x32_bf16 v[72:75], v[24:27], v[244:247], v[72:75]\n"
  "s_waitcnt lgkmcnt(1)\n"
  "v_mfma_f32_16x16x32_bf16 v[32:35], v[28:31], a[136:139], v[32:35]\n"
  "ds_read_b128 v[12:15], v1 offset:21504\n"
  "v_mfma_f32_16x16x32_bf16 v[40:43], v[28:31], a[140:143], v[40:43]\n"
  "v_mfma_f32_16x16x32_bf16 v[48:51], v[28:31], v[104:107], v[48:51]\n"
  "v_mfma_f32_16x16x32_bf16 v[56:59], v[28:31], v[108:111], v[56:59]\n"
  "v_mfma_f32_16x16x32_bf16 v[64:67], v[28:31], v[248:251], v[64:67]\n"
  "v_mfma_f32_16x16x32_bf16 v[72:75], v[28:31], v[252:255], v[72:75]\n"
  "s_waitcnt lgkmcnt(1)\n"
  "v_mfma_f32_16x16x32_bf16 v[36:39], v[8:11], a[0:3], 0\n"
  "ds_read_b128 v[16:19], v1 offset:22528\n"
  "v_mfma_f32_16x16x32_bf16 v[44:47], v[8:11], a[4:7], 0\n"
  "v_mfma_f32_16x16x32_bf16 v[52:55], v[8:11], a[144:147], 0\n"
  "v_cvt_pk_bf16_f32 v32, v32, v33\n"
  "v_cvt_pk_bf16_f32 v33, v34, v35\n"
  "v_mfma_f32_16x16x32_bf16 v[60:63], v[8:11], a[148:151], 0\n"
  "v_cvt_pk_bf16_f32 v40, v40, v41\n"
  "v_cvt_pk_bf16_f32 v41, v42, v43\n"
  "s_waitcnt lgkmcnt(1)\n"
  "v_mfma_f32_16x16x32_bf16 v[36:39], v[12:15], a[8:11], v[36:39]\n"
  "ds_read_b128 v[20:23], v1 offset:23552\n"
  "v_cvt_pk_bf16_f32 v48, v48, v49\n"
  "v_cvt_pk_bf16_f32 v49, v50, v51\n"
  "v_mfma_f32_16x16x32_bf16 v[44:47], v[12:15], a[12:15], v[44:47]\n"
  "v_cvt_pk_bf16_f32 v56, v56, v57\n"
  "v_cvt_pk_bf16_f32 v57, v58, v59\n"
  "v_mfma_f32_16x16x32_bf16 v[52:55], v[12:15], a[152:155], v[52:55]\n"
  "v_cvt_pk_bf16_f32 v64, v64, v65\n"
  "v_cvt_pk_bf16_f32 v65, v66, v67\n"
  "v_mfma_f32_16x16x32_bf16 v[60:63], v[12:15], a[156:159], v[60:63]\n"
  "v_cvt_pk_bf16_f32 v72, v72, v73\n"
  "v_cvt_pk_bf16_f32 v73, v74, v75\n"
  "s_waitcnt lgkmcnt(1)\n"
  "v_mfma_f32_16x16x32_bf16 v[36:39], v[16:19], a[16:19], v[36:39]\n"
  "ds_read_b128 v[24:27], v1 offset:24576\n"
  "v_mfma_f32_16x16x32_bf16 v[44:47], v[16:19], a[20:23], v[44:47]\n"
  "v_mfma_f32_16x16x32_bf16 v[52:55], v[16:19], a[160:163], v[52:55]\n"
  "v_mfma_f32_16x16x32_bf16 v[60:63], v[16:19], a[164:167], v[60:63]\n"
  "s_waitcnt lgkmcnt(1)\n"
  "v_mfma_f32_16x16x32_bf16 v[36:39], v[20:23], a[24:27], v[36:39]\n"
  "ds_read_b128 v[28:31], v1 offset:25600\n"
  "v_mfma_f32_16x16x32_bf16 v[44:47], v[20:23], a[28:31], v[44:47]\n"
  "v_mfma_f32_16x16x32_bf16 v[52:55], v[20:23], a[168:171], v[52:55]\n"
  "v_mfma_f32_16x16x32_bf16 v[60:63], v[20:23], a[172:175], v[60:63]\n"
  "s_waitcnt lgkmcnt(1)\n"
  "v_mfma_f32_16x16x32_bf16 v[36:39], v[24:27], a[32:35], v[36:39]\n"
  "ds_read_b128 v[8:11], v1 offset:26624\n"
  "v_mfma_f32_16x16x32_bf16 v[44:47], v[24:27], a[36:39], v[44:47]\n"
  "v_mfma_f32_16x16x32_bf16 v[52:55], v[24:27], a[176:179], v[52:55]\n"
  "v_mfma_f32_16x16x32_bf16 v[60:63], v[24:27], a[180:183], v[60:63]\n"
  "s_waitcnt lgkmcnt(1)\n"
  "v_mfma_f32_16x16x32_bf16 v[36:39], v[28:31], a[40:43], v[36:39]\n"
  "ds_read_b128 v[12:15], v1 offset:27648\n"
  "v_mfma_f32_16x16x32_bf16 v[44:47], v[28:31], a[44:47], v[44:47]\n"
  "v_mfma_f32_16x16x32_bf16 v[52:55], v[28:31], a[184:187], v[52:55]\n"
  "v_mfma_f32_16x16x32_bf16 v[60:63], v[28:31], a[188:191], v[60:63]\n"
  "s_waitcnt lgkmcnt(1)\n"
  "v_mfma_f32_16x16x32_bf16 v[36:39], v[8:11], a[48:51], v[36:39]\n"
  "ds_read_b128 v[16:19], v1 offset:28672\n"
  "v_mfma_f32_16x16x32_bf16 v[44:47], v[8:11], a[52:55], v[44:47]\n"
  "v_mfma_f32_16x16x32_bf16 v[52:55], v[8:11], a[192:195], v[52:55]\n"
  "v_mfma_f32_16x16x32_bf16 v[60:63], v[8:11], a[196:199], v[60:63]\n"
  "s_waitcnt lgkmcnt(1)\n"
  "v_mfma_f32_16x16x32_bf16 v[36:39], v[12:15], a[56:59], v[36:39]\n"
  "ds_read_b128 v[20:23], v1 offset:29696\n"
  "v_mfma_f32_16x16x32_bf16 v[44:47], v[12:15], a[60:63], v[44:47]\n"
  "v_mfma_f32_16x16x32_bf16 v[52:55], v[12:15], a[200:203], v[52:55]\n"
  "v_mfma_f32_16x16x32_bf16 v[60:63], v[12:15], a[204:207], v[60:63]\n"
  "s_waitcnt lgkmcnt(1)\n"
  "v_mfma_f32_16x16x32_bf16 v[36:39], v[16:19], a[64:67], v[36:39]\n"
  "ds_read_b128 v[24:27], v1 offset:30720\n"
  "v_mfma_f32_16x16x32_bf16 v[44:47], v[16:19], a[68:71], v[44:47]\n"
  "v_mfma_f32_16x16x32_bf16 v[52:55], v[16:19], a[208:211], v[52:55]\n"
  "v_mfma_f32_16x16x32_bf16 v[60:63], v[16:19], a[212:215], v[60:63]\n"
  "s_waitcnt lgkmcnt(1)\n"
  "v_mfma_f32_16x16x32_bf16 v[36:39], v[20:23], a[72:75], v[36:39]\n"
  "ds_read_b128 v[28:31], v1 offset:31744\n"
  "v_mfma_f32_16x16x32_bf16 v[44:47], v[20:23], a[76:79], v[44:47]\n"
  "v_mfma_f32_16x16x32_bf16 v[52:55], v[20:23], a[216:219], v[52:55]\n"
  "v_mfma_f32_16x16x32_bf16 v[60:63], v[20:23], a[220:223], v[60:63]\n"
  "s_waitcnt lgkmcnt(1)\n"
  "v_mfma_f32_16x16x32_bf16 v[36:39], v[24:27], a[80:83], v[36:39]\n"
  "ds_read_b128 v[8:11], v1 offset:32768\n"
  "s_waitcnt vmcnt(0)\n"
  "v_subrev_u32 v6, v3, v4\n"
  "v_cmp_lt_i32 vcc, s70, v6\n"
  "v_mfma_f32_16x16x32_bf16 v[44:47], v[24:27], a[84:87], v[44:47]\n"
  "s_cmp_lt_i32 s70, 63\n"
  "s_cselect_b32 s94, -1, 0\n"
  "v_mfma_f32_16x16x32_bf16 v[52:55], v[24:27], a[224:227], v[52:55]\n"
  "v_mfma_f32_16x16x32_bf16 v[60:63], v[24:27], a[228:231], v[60:63]\n"
  "s_cmp_eq_u64 vcc, s[94:95]\n"
  "s_cselect_b32 s71, 1, 0\n"
  "s_cselect_b32 s78, s78, 0x80000000\n"
  "s_waitcnt lgkmcnt(1)\n"
  "v_mfma_f32_16x16x32_bf16 v[36:39], v[28:31], a[88:91], v[36:39]\n"
  "ds_read_b128 v[12:15], v1 offset:33792\n"
  "v_mfma_f32_16x16x32_bf16 v[44:47], v[28:31], a[92:95], v[44:47]\n"
  "s_add_u32 m0, s79, 0\n"
  "s_add_u32 s76, s78, 0\n"
  "v_mfma_f32_16x16x32_bf16 v[52:55], v[28:31], a[232:235], v[52:55]\n"
  "buffer_load_dwordx4 v7, s[36:39], s76 offen sc1 lds\n"
  "v_mfma_f32_16x16x32_bf16 v[60:63], v[28:31], a[236:239], v[60:63]\n"
  "s_waitcnt lgkmcnt(1)\n"
  "v_mfma_f32_16x16x32_bf16 v[36:39], v[8:11], a[96:99], v[36:39]\n"
  "ds_read_b128 v[16:19], v1 offset:34816\n"
  "v_mfma_f32_16x16x32_bf16 v[44:47], v[8:11], a[100:103], v[44:47]\n"
  "v_mfma_f32_16x16x32_bf16 v[52:55], v[8:11], a[240:243], v[52:55]\n"
  "v_mfma_f32_16x16x32_bf16 v[60:63], v[8:11], a[244:247], v[60:63]\n"
  "s_add_u32 m0, s79, 4096\n"
  "s_add_u32 s76, s78, 16384\n"
  "s_waitcnt lgkmcnt(1)\n"
  "v_mfma_f32_16x16x32_bf16 v[36:39], v[12:15], a[104:107], v[36:39]\n"
  "ds_read_b128 v[20:23], v1 offset:35840\n"
  "buffer_load_dwordx4 v7, s[36:39], s76 offen sc1 lds\n"
  "v_mfma_f32_16x16x32_bf16 v[44:47], v[12:15], a[108:111], v[44:47]\n"
  "v_mfma_f32_16x16x32_bf16 v[52:55], v[12:15], a[248:251], v[52:55]\n"
  "v_mfma_f32_16x16x32_bf16 v[60:63], v[12:15], a[252:255], v[60:63]\n"
  "s_waitcnt lgkmcnt(1)\n"
  "v_mfma_f32_16x16x32_bf16 v[36:39], v[16:19], a[112:115], v[36:39]\n"
  "ds_read_b128 v[24:27], v1 offset:18432\n"
  "v_mfma_f32_16x16x32_bf16 v[44:47], v[16:19], a[116:119], v[44:47]\n"
  "ds_read_b128 v[28:31], v1 offset:19456\n"
  "s_add_u32 m0, s79, 8192\n"
  "s_add_u32 s76, s78, 32768\n"
  "v_mfma_f32_16x16x32_bf16 v[52:55], v[16:19], v[80:83], v[52:55]\n"
  "ds_read_b128 v[8:11], v1 offset:20480\n"
  "buffer_load_dwordx4 v7, s[36:39], s76 offen sc1 lds\n"
  "v_mfma_f32_16x16x32_bf16 v[60:63], v[16:19], v[84:87], v[60:63]\n"
  "s_waitcnt lgkmcnt(3)\n"
  "v_mfma_f32_16x16x32_bf16 v[36:39], v[20:23], a[120:123], v[36:39]\n"
  "ds_read_b128 v[12:15], v1 offset:21504\n"
  "v_mfma_f32_16x16x32_bf16 v[44:47], v[20:23], a[124:127], v[44:47]\n"
  "v_mfma_f32_16x16x32_bf16 v[52:55], v[20:23], v[88:91], v[52:55]\n"
  "v_mfma_f32_16x16x32_bf16 v[60:63], v[20:23], v[92:95], v[60:63]\n"
  "s_add_u32 m0, s79, 12288\n"
  "s_add_u32 s76, s78, 49152\n"
  "s_waitcnt lgkmcnt(3)\n"
  "v_mfma_f32_16x16x32_bf16 v[36:39], v[24:27], a[128:131], v[36:39]\n"
  "ds_read_b128 v[16:19], v1 offset:22528\n"
  "buffer_load_dwordx4 v7, s[36:39], s76 offen sc1 lds\n"
  "v_mfma_f32_16x16x32_bf16 v[44:47], v[24:27], a[132:135], v[44:47]\n"
  "v_mfma_f32_16x16x32_bf16 v[52:55], v[24:27], v[96:99], v[52:55]\n"
  "v_mfma_f32_16x16x32_bf16 v[60:63], v[24:27], v[100:103], v[60:63]\n"
  "s_waitcnt lgkmcnt(3)\n"
  "v_mfma_f32_16x16x32_bf16 v[36:39], v[28:31], a[136:139], v[36:39]\n"
  "ds_read_b128 v[20:23], v1 offset:23552\n"
  "v_mfma_f32_16x16x32_bf16 v[44:47], v[28:31], a[140:143], v[44:47]\n"
  "s_add_u32 m0, s79, 16384\n"
  "s_add_u32 s76, s78, s56\n"
  "v_mfma_f32_16x16x32_bf16 v[52:55], v[28:31], v[104:107], v[52:55]\n"
  "buffer_load_dwordx4 v7, s[36:39], s76 offen sc1 lds\n"
  "v_mfma_f32_16x16x32_bf16 v[60:63], v[28:31], v[108:111], v[60:63]\n"
  "s_waitcnt lgkmcnt(2)\n"
  "v_mfma_f32_16x16x32_bf16 v[68:71], v[8:11], v[112:115], 0\n"
  "ds_read_b128 v[24:27], v1 offset:24576\n"
  "v_mfma_f32_16x16x32_bf16 v[76:79], v[8:11], v[116:119], 0\n"
  "v_mfma_f32_16x16x32_bf16 v[68:71], v[12:15], v[120:123], v[68:71]\n"
  "ds_read_b128 v[28:31], v1 offset:25600\n"
  "v_cvt_pk_bf16_f32 v34, v36, v37\n"
  "v_cvt_pk_bf16_f32 v35, v38, v39\n"
  "v_mfma_f32_16x16x32_bf16 v[76:79], v[12:15], v[124:127], v[76:79]\n"
  "s_add_u32 s77, s80, s64\n"
  "buffer_store_dwordx4 v[32:35], v0, s[36:39], s77 offen offset:0 sc1\n"
  "s_waitcnt lgkmcnt(2)\n"
  "v_mfma_f32_16x16x32_bf16 v[68:71], v[16:19], v[128:131], v[68:71]\n"
  "ds_read_b128 v[8:11], v1 offset:26624\n"
  "v_mfma_f32_16x16x32_bf16 v[76:79], v[16:19], v[132:135], v[76:79]\n"
  "v_mfma_f32_16x16x32_bf16 v[68:71], v[20:23], v[136:139], v[68:71]\n"
  "ds_read_b128 v[12:15], v1 offset:27648\n"
  "v_mfma_f32_16x16x32_bf16 v[76:79], v[20:23], v[140:143], v[76:79]\n"
  "s_waitcnt lgkmcnt(2)\n"
  "v_mfma_f32_16x16x32_bf16 v[68:71], v[24:27], v[144:147], v[68:71]\n"
  "ds_read_b128 v[16:19], v1 offset:28672\n"
  "v_mfma_f32_16x16x32_bf16 v[76:79], v[24:27], v[148:151], v[76:79]\n"
  "v_cvt_pk_bf16_f32 v42, v44, v45\n"
  "v_cvt_pk_bf16_f32 v43, v46, v47\n"
  "v_mfma_f32_16x16x32_bf16 v[68:71], v[28:31], v[152:155], v[68:71]\n"
  "ds_read_b128 v[20:23], v1 offset:29696\n"
  "s_add_u32 s77, s80, s64\n"
  "buffer_store_dwordx4 v[40:43], v0, s[36:39], s77 offen offset:1024 sc1\n"
  "v_mfma_f32_16x16x32_bf16 v[76:79], v[28:31], v[156:159], v[76:79]\n"
  "s_waitcnt lgkmcnt(2)\n"
  "v_mfma_f32_16x16x32_bf16 v[68:71], v[8:11], v[160:163], v[68:71]\n"
  "ds_read_b128 v[24:27], v1 offset:30720\n"
  "v_mfma_f32_16x16x32_bf16 v[76:79], v[8:11], v[164:167], v[76:79]\n"
  "buffer_load_dword v4, v2, s[36:39], 0 offen sc1\n"
  "v_mfma_f32_16x16x32_bf16 v[68:71], v[12:15], v[168:171], v[68:71]\n"
  "ds_read_b128 v[28:31], v1 offset:31744\n"
  "v_mfma_f32_16x16x32_bf16 v[76:79], v[12:15], v[172:175], v[76:79]\n"
  "s_waitcnt lgkmcnt(2)\n"
  "v_mfma_f32_16x16x32_bf16 v[68:71], v[16:19], v[176:179], v[68:71]\n"
  "ds_read_b128 v[8:11], v1 offset:32768\n"
  "v_cvt_pk_bf16_f32 v50, v52, v53\n"
  "v_cvt_pk_bf16_f32 v51, v54, v55\n"
  "v_mfma_f32_16x16x32_bf16 v[76:79], v[16:19], v[180:183], v[76:79]\n"
  "s_add_u32 s77, s80, s65\n"
  "buffer_store_dwordx4 v[48:51], v0, s[36:39], s77 offen offset:0 sc1\n"
  "v_mfma_f32_16x16x32_bf16 v[68:71], v[20:23], v[184:187], v[68:71]\n"
  "ds_read_b128 v[12:15], v1 offset:33792\n"
  "v_mfma_f32_16x16x32_bf16 v[76:79], v[20:23], v[188:191], v[76:79]\n"
  "s_waitcnt lgkmcnt(2)\n"
  "v_mfma_f32_16x16x32_bf16 v[68:71], v[24:27], v[192:195], v[68:71]\n"
  "ds_read_b128 v[16:19], v1 offset:34816\n"
  "v_mfma_f32_16x16x32_bf16 v[76:79], v[24:27], v[196:199], v[76:79]\n"
  "v_mfma_f32_16x16x32_bf16 v[68:71], v[28:31], v[200:203], v[68:71]\n"
  "ds_read_b128 v[20:23], v1 offset:35840\n"
  "v_mfma_f32_16x16x32_bf16 v[76:79], v[28:31], v[204:207], v[76:79]\n"
  "v_cvt_pk_bf16_f32 v58, v60, v61\n"
  "v_cvt_pk_bf16_f32 v59, v62, v63\n"
  "s_waitcnt lgkmcnt(2)\n"
  "v_mfma_f32_16x16x32_bf16 v[68:71], v[8:11], v[208:211], v[68:71]\n"
  "ds_read_b128 v[24:27], v1 offset:18432\n"
  "s_add_u32 s77, s80, s65\n"
  "buffer_store_dwordx4 v[56:59], v0, s[36:39], s77 offen offset:1024 sc1\n"
  "v_mfma_f32_16x16x32_bf16 v[76:79], v[8:11], v[212:215], v[76:79]\n"
  "v_mfma_f32_16x16x32_bf16 v[68:71], v[12:15], v[216:219], v[68:71]\n"
  "ds_read_b128 v[28:31], v1 offset:19456\n"
  "v_mfma_f32_16x16x32_bf16 v[76:79], v[12:15], v[220:223], v[76:79]\n"
  "s_waitcnt lgkmcnt(2)\n"
  "v_mfma_f32_16x16x32_bf16 v[68:71], v[16:19], v[224:227], v[68:71]\n"
  "v_mfma_f32_16x16x32_bf16 v[76:79], v[16:19], v[228:231], v[76:79]\n"
  "v_mfma_f32_16x16x32_bf16 v[68:71], v[20:23], v[232:235], v[68:71]\n"
  "v_mfma_f32_16x16x32_bf16 v[76:79], v[20:23], v[236:239], v[76:79]\n"
  "s_add_u32 s77, s80, s66\n"
  "s_waitcnt lgkmcnt(0)\n"
  "v_mfma_f32_16x16x32_bf16 v[68:71], v[24:27], v[240:243], v[68:71]\n"
  "v_mfma_f32_16x16x32_bf16 v[76:79], v[24:27], v[244:247], v[76:79]\n"
  "v_mfma_f32_16x16x32_bf16 v[68:71], v[28:31], v[248:251], v[68:71]\n"
  "v_mfma_f32_16x16x32_bf16 v[76:79], v[28:31], v[252:255], v[76:79]\n"
  "s_nop 3\n"
  "v_cvt_pk_bf16_f32 v66, v68, v69\n"
  "v_cvt_pk_bf16_f32 v67, v70, v71\n"
  "buffer_store_dwordx4 v[64:67], v0, s[36:39], s77 offen offset:0 sc1\n"
  "s_nop 0\n"
  "v_cvt_pk_bf16_f32 v74, v76, v77\n"
  "v_cvt_pk_bf16_f32 v75, v78, v79\n"
  "buffer_store_dwordx4 v[72:75], v0, s[36:39], s77 offen offset:1024 sc1\n"
  "s_xor_b32 s81, s81, 65536\n"
  "v_xor_b32 v1, 65536, v1\n"
  "s_add_u32 s70, s70, 1\n"
  "s_mov_b32 s82, s71\n"
  "s_cmp_lg_u32 s71, 0\n"
  "s_cbranch_scc1 L_pf_%=\n"
  "s_waitcnt vmcnt(4)\n"
  "v_subrev_u32 v6, v3, v4\n"
  "s_add_u32 s74, s70, 0\n"
  "v_cmp_le_i32 vcc, s74, v6\n"
  "s_cmp_eq_u64 vcc, exec\n"
  "s_cbranch_scc0 L_idle_%=\n"
  "s_cmp_eq_u32 s70, 64\n"
  "s_cbranch_scc1 L_idle_%=\n"
  "s_add_u32 s78, s70, 0\n"
  "s_and_b32 s78, s78, s61\n"
  "s_mul_i32 s78, s78, s60\n"
  "s_lshr_b32 s76, s78, 8\n"
  "s_and_b32 s76, s76, 8192\n"
  "s_add_u32 s78, s78, s59\n"
  "s_xor_b32 s78, s78, s76\n"
  "s_add_u32 s79, s81, s57\n"
  "s_add_u32 m0, s79, 0\n"
  "s_add_u32 s76, s78, 0\n"
  "buffer_load_dwordx4 v7, s[36:39], s76 offen sc1 lds\n"
  "s_add_u32 m0, s79, 4096\n"
  "s_add_u32 s76, s78, 16384\n"
  "buffer_load_dwordx4 v7, s[36:39], s76 offen sc1 lds\n"
  "s_add_u32 m0, s79, 8192\n"
  "s_add_u32 s76, s78, 32768\n"
  "buffer_load_dwordx4 v7, s[36:39], s76 offen sc1 lds\n"
  "s_add_u32 m0, s79, 12288\n"
  "s_add_u32 s76, s78, 49152\n"
  "buffer_load_dwordx4 v7, s[36:39], s76 offen sc1 lds\n"
  "s_add_u32 m0, s79, 16384\n"
  "s_add_u32 s76, s78, s56\n"
  "buffer_load_dwordx4 v7, s[36:39], s76 offen sc1 lds\n"
  "s_waitcnt vmcnt(5)\n"
  "s_add_u32 s75, s70, -1\n"
  "s_lshl_b32 s74, s75, 2\n"
  "s_add_u32 s74, s74, 163584\n"
  "v_mov_b32 v16, s74\n"
  "v_mov_b32 v6, 1\n"
  "s_mov_b64 exec, 1\n"
  "ds_add_rtn_u32 v16, v16, v6\n"
  "s_mov_b64 exec, -1\n"
  "v_mov_b32 v6, s75\n"
  "s_waitcnt lgkmcnt(0)\n"
  "v_readfirstlane_b32 s74, v16\n"
  "s_cmp_eq_u32 s74, 3\n"
  "s_cselect_b32 s74, s58, 0x80000000\n"
  "buffer_store_dword v6, v5, s[36:39], s74 offen sc1\n"
  "s_waitcnt vmcnt(3)\n"
  "s_branch L_go_%=\n"
  "L_idle_%=:\n"
  "buffer_load_dword v4, v2, s[36:39], 0 offen sc1\n"
  "s_waitcnt vmcnt(1)\n"
  "s_add_u32 s75, s70, -1\n"
  "s_lshl_b32 s74, s75, 2\n"
  "s_add_u32 s74, s74, 163584\n"
  "v_mov_b32 v16, s74\n"
  "v_mov_b32 v6, 1\n"
  "s_mov_b64 exec, 1\n"
  "ds_add_rtn_u32 v16, v16, v6\n"
  "s_mov_b64 exec, -1\n"
  "v_mov_b32 v6, s75\n"
  "s_waitcnt lgkmcnt(0)\n"
  "v_readfirstlane_b32 s74, v16\n"
  "s_cmp_eq_u32 s74, 3\n"
  "s_cselect_b32 s74, s58, 0x80000000\n"
  "buffer_store_dword v6, v5, s[36:39], s74 offen sc1\n"
  "s_waitcnt vmcnt(1)\n"
  "s_cmp_eq_u32 s70, 64\n"
  "s_cbranch_scc1 L_fin_%=\n"
  "v_subrev_u32 v6, v3, v4\n"
  "s_add_u32 s74, s70, 0\n"
  "v_cmp_le_i32 vcc, s74, v6\n"
  "s_cmp_eq_u64 vcc, exec\n"
  "s_cbranch_scc1 L_poll_x_%=\n"
  "L_first_%=:\n"
  "s_cmp_lg_u32 s72, 0\n"
  "s_cselect_b32 s73, 0, 32768\n"
  "v_add_u32 v6, s70, v3\n"
  "L_poll_%=:\n"
  "buffer_load_dword v4, v2, s[36:39], 0 offen sc1\n"
  "s_sub_u32 s73, s73, 1\n"
  "s_cselect_b32 s72, 1, s72\n"
  "s_cselect_b64 s[2:3], 0, -1\n"
  "s_waitcnt vmcnt(0)\n"
  "v_cmp_gt_i32 vcc, v6, v4\n"
  "s_and_b64 vcc, vcc, s[2:3]\n"
  "s_cbranch_scc1 L_poll_%=\n"
  "L_poll_x_%=:\n"
  "s_add_u32 s78, s70, 0\n"
  "s_and_b32 s78, s78, s61\n"
  "s_mul_i32 s78, s78, s60\n"
  "s_lshr_b32 s76, s78, 8\n"
  "s_and_b32 s76, s76, 8192\n"
  "s_add_u32 s78, s78, s59\n"
  "s_xor_b32 s78, s78, s76\n"
  "s_add_u32 s79, s81, s57\n"
  "s_add_u32 m0, s79, 0\n"
  "s_add_u32 s76, s78, 0\n"
  "buffer_load_dwordx4 v7, s[36:39], s76 offen sc1 lds\n"
  "s_add_u32 m0, s79, 4096\n"
  "s_add_u32 s76, s78, 16384\n"
  "buffer_load_dwordx4 v7, s[36:39], s76 offen sc1 lds\n"
  "s_add_u32 m0, s79, 8192\n"
  "s_add_u32 s76, s78, 32768\n"
  "buffer_load_dwordx4 v7, s[36:39], s76 offen sc1 lds\n"
  "s_add_u32 m0, s79, 12288\n"
  "s_add_u32 s76, s78, 49152\n"
  "buffer_load_dwordx4 v7, s[36:39], s76 offen sc1 lds\n"
  "s_add_u32 m0, s79, 16384\n"
  "s_add_u32 s76, s78, s56\n"
  "buffer_load_dwordx4 v7, s[36:39], s76 offen sc1 lds\n"
  "s_waitcnt vmcnt(2)\n"
  "s_branch L_go_%=\n"
  "L_fin_%=:\n"
  "s_waitcnt vmcnt(0)\n"
  "L_c1_%=:\n"
  "s_endpgm\n"
      :
      : "{v0}"(ln * 16u), "{v1}"(ln * 16u), "{v2}"(ln < 4 ? c.dflag(c.lane) : (ln < 12 ? c.xflag(c.lane - 4) : OOB)),
        "{v3}"(ln < 4 ? 1 - RING : (ln < 12 ? 0 : NEVER)),
        "{v7}"(a.ab), "{v8}"(a.voff), "{v9}"(a.ra), "{s[40:43]}"(mkrsrc(p.Wv, 576 * 4096 * 2)), COMMON,
        "{s[64:67]}"(uni((v4u){0u, 16384u, full ? 32768u : OOB, full ? 512u : 256u}))
      : OWNED);
}
DI void role_f1(Ctx& c, const Params& p, int f) {
  const unsigned ln = (unsigned)c.lane, w = (unsigned)c.wave;
  const W32 a = w32(c, 9216u, (unsigned)(512 * f));
  const rsrc_t rb = mkrsrc(p.b1, 4608 * 2);
  v16f bias;
#pragma unroll
  for (int t = 0; t < 2; ++t) {
    const v4u x = __builtin_amdgcn_raw_buffer_load_b128(rb, (unsigned)(512 * f + 256 * t) + 64u * w + 16u * (ln >> 4), 0, 0);
#pragma unroll
    for (int j = 0; j < 4; ++j) { bias[8 * t + 2 * j] = asf(x[j] << 16); bias[8 * t + 2 * j + 1] = asf(x[j] & 0xffff0000u); }
  }
  const unsigned fword = c.flag(F_F1 + f);
  const unsigned srcb = c.slab1(0) + TR(L_XN2) + w * 2048u, srcm = 4 * MB, srck = RING - 1;
  const unsigned dstb = c.slab1(0) + TR(L_H + (unsigned)f * 16384u) + w * 2048u;
  asm volatile(
  "s_getpc_b64 s[0:1]\n"
  "L_c0_%=:\n"
  "v_mbcnt_lo_u32_b32 v5, -1, 0\n"
  "v_mbcnt_hi_u32_b32 v5, -1, v5\n"
  "v_lshlrev_b32 v5, 4, v5\n"
  "v_add_lshl_u32 v5, v5, s57, 2\n"
  "v_min_u32 v5, L_c1_%=-L_c0_%=, v5\n"
  "global_load_dword v5, v5, s[0:1]\n"
  "s_nop 4\n"
  "s_add_u32 m0, s57, 0\n"
  "s_mov_b32 s74, 0\n"
  "buffer_load_dwordx4 v8, s[40:43], s74 offen lds\n"
  "s_add_u32 m0, s57, 4096\n"
  "s_mov_b32 s74, 147456\n"
  "buffer_load_dwordx4 v8, s[40:43], s74 offen lds\n"
  "s_add_u32 m0, s57, 8192\n"
  "s_mov_b32 s74, 294912\n"
  "buffer_load_dwordx4 v8, s[40:43], s74 offen lds\n"
  "s_add_u32 m0, s57, 12288\n"
  "s_mov_b32 s74, 442368\n"
  "buffer_load_dwordx4 v8, s[40:43], s74 offen lds\n"
  "s_add_u32 m0, s57, 16384\n"
  "s_mov_b32 s74, 589824\n"
  "buffer_load_dwordx4 v8, s[40:43], s74 offen lds\n"
  "s_add_u32 m0, s57, 20480\n"
  "s_mov_b32 s74, 737280\n"
  "buffer_load_dwordx4 v8, s[40:43], s74 offen lds\n"
  "s_add_u32 m0, s57, 24576\n"
  "s_mov_b32 s74, 884736\n"
  "buffer_load_dwordx4 v8, s[40:43], s74 offen lds\n"
  "s_add_u32 m0, s57, 28672\n"
  "s_mov_b32 s74, 1032192\n"
  "buffer_load_dwordx4 v8, s[40:43], s74 offen lds\n"
  "s_add_u32 m0, s57, 32768\n"
  "s_mov_b32 s74, 1179648\n"
  "buffer_load_dwordx4 v8, s[40:43], s74 offen lds\n"
  "s_add_u32 m0, s57, 36864\n"
  "s_mov_b32 s74, 1327104\n"
  "buffer_load_dwordx4 v8, s[40:43], s74 offen lds\n"
  "s_add_u32 m0, s57, 40960\n"
  "s_mov_b32 s74, 1474560\n"
  "buffer_load_dwordx4 v8, s[40:43], s74 offen lds\n"
  "s_add_u32 m0, s57, 45056\n"
  "s_mov_b32 s74, 1622016\n"
  "buffer_load_dwordx4 v8, s[40:43], s74 offen lds\n"
  "s_add_u32 m0, s57, 49152\n"
  "s_mov_b32 s74, 1769472\n"
  "buffer_load_dwordx4 v8, s[40:43], s74 offen lds\n"
  "s_add_u32 m0, s57, 53248\n"
  "s_mov_b32 s74, 1916928\n"
  "buffer_load_dwordx4 v8, s[40:43], s74 offen lds\n"
  "s_add_u32 m0, s57, 57344\n"
  "s_mov_b32 s74, 2064384\n"
  "buffer_load_dwordx4 v8, s[40:43], s74 offen lds\n"
  "s_add_u32 m0, s57, 61440\n"
  "s_mov_b32 s74, 2211840\n"
  "buffer_load_dwordx4 v8, s[40:43], s74 offen lds\n"
  "s_add_u32 m0, s57, 65536\n"
  "s_mov_b32 s74, 2359296\n"
  "buffer_load_dwordx4 v8, s[40:43], s74 offen lds\n"
  "s_add_u32 m0, s57, 69632\n"
  "s_mov_b32 s74, 2506752\n"
  "buffer_load_dwordx4 v8, s[40:43], s74 offen lds\n"
  "s_add_u32 m0, s57, 73728\n"
  "s_mov_b32 s74, 2654208\n"
  "buffer_load_dwordx4 v8, s[40:43], s74 offen lds\n"
  "s_add_u32 m0, s57, 77824\n"
  "s_mov_b32 s74, 2801664\n"
  "buffer_load_dwordx4 v8, s[40:43], s74 offen lds\n"
  "s_add_u32 m0, s57, 81920\n"
  "s_mov_b32 s74, 2949120\n"
  "buffer_load_dwordx4 v8, s[40:43], s74 offen lds\n"
  "s_add_u32 m0, s57, 86016\n"
  "s_mov_b32 s74, 3096576\n"
  "buffer_load_dwordx4 v8, s[40:43], s74 offen lds\n"
  "s_add_u32 m0, s57, 90112\n"
  "s_mov_b32 s74, 3244032\n"
  "buffer_load_dwordx4 v8, s[40:43], s74 offen lds\n"
  "s_add_u32 m0, s57, 94208\n"
  "s_mov_b32 s74, 3391488\n"
  "buffer_load_dwordx4 v8, s[40:43], s74 offen lds\n"
  "s_waitcnt vmcnt(20) lgkmcnt(0)\n"
  "s_barrier\n"
  "s_add_u32 m0, s57, 98304\n"
  "s_mov_b32 s74, 3538944\n"
  "buffer_load_dwordx4 v8, s[40:43], s74 offen lds\n"
  "s_add_u32 m0, s57, 102400\n"
  "s_mov_b32 s74, 3686400\n"
  "buffer_load_dwordx4 v8, s[40:43], s74 offen lds\n"
  "s_add_u32 m0, s57, 106496\n"
  "s_mov_b32 s74, 3833856\n"
  "buffer_load_dwordx4 v8, s[40:43], s74 offen lds\n"
  "s_add_u32 m0, s57, 110592\n"
  "s_mov_b32 s74, 3981312\n"
  "buffer_load_dwordx4 v8, s[40:43], s74 offen lds\n"
  "v_add_u32 v4, 0, v9\n"
  "ds_read_b64_tr_b16 a[0:1], v4 offset:0\n"
  "ds_read_b64_tr_b16 a[2:3], v4 offset:1024\n"
  "ds_read_b64_tr_b16 a[4:5], v4 offset:8\n"
  "ds_read_b64_tr_b16 a[6:7], v4 offset:1032\n"
  "ds_read_b64_tr_b16 a[8:9], v4 offset:8192\n"
  "ds_read_b64_tr_b16 a[10:11], v4 offset:9216\n"
  "ds_read_b64_tr_b16 a[12:13], v4 offset:8200\n"
  "ds_read_b64_tr_b16 a[14:15], v4 offset:9224\n"
  "s_waitcnt vmcnt(20) lgkmcnt(0)\n"
  "s_barrier\n"
  "s_add_u32 m0, s57, 0\n"
  "s_mov_b32 s74, 4128768\n"
  "buffer_load_dwordx4 v8, s[40:43], s74 offen lds\n"
  "s_add_u32 m0, s57, 4096\n"
  "s_mov_b32 s74, 4276224\n"
  "buffer_load_dwordx4 v8, s[40:43], s74 offen lds\n"
  "s_add_u32 m0, s57, 8192\n"
  "s_mov_b32 s74, 4423680\n"
  "buffer_load_dwordx4 v8, s[40:43], s74 offen lds\n"
  "s_add_u32 m0, s57, 12288\n"
  "s_mov_b32 s74, 4571136\n"
  "buffer_load_dwordx4 v8, s[40:43], s74 offen lds\n"
  "v_add_u32 v4, 16384, v9\n"
  "ds_read_b64_tr_b16 a[16:17], v4 offset:0\n"
  "ds_read_b64_tr_b16 a[18:19], v4 offset:1024\n"
  "ds_read_b64_tr_b16 a[20:21], v4 offset:8\n"
  "ds_read_b64_tr_b16 a[22:23], v4 offset:1032\n"
  "ds_read_b64_tr_b16 a[24:25], v4 offset:8192\n"
  "ds_read_b64_tr_b16 a[26:27], v4 offset:9216\n"
  "ds_read_b64_tr_b16 a[28:29], v4 offset:8200\n"
  "ds_read_b64_tr_b16 a[30:31], v4 offset:9224\n"
  "s_waitcnt vmcnt(20) lgkmcnt(0)\n"
  "s_barrier\n"
  "s_add_u32 m0, s57, 16384\n"
  "s_mov_b32 s74, 4718592\n"
  "buffer_load_dwordx4 v8, s[40:43], s74 offen lds\n"
  "s_add_u32 m0, s57, 20480\n"
  "s_mov_b32 s74, 4866048\n"
  "buffer_load_dwordx4 v8, s[40:43], s74 offen lds\n"
  "s_add_u32 m0, s57, 24576\n"
  "s_mov_b32 s74, 5013504\n"
  "buffer_load_dwordx4 v8, s[40:43], s74 offen lds\n"
  "s_add_u32 m0, s57, 28672\n"
  "s_mov_b32 s74, 5160960\n"
  "buffer_load_dwordx4 v8, s[40:43], s74 offen lds\n"
  "v_add_u32 v4, 32768, v9\n"
  "ds_read_b64_tr_b16 a[32:33], v4 offset:0\n"
  "ds_read_b64_tr_b16 a[34:35], v4 offset:1024\n"
  "ds_read_b64_tr_b16 a[36:37], v4 offset:8\n"
  "ds_read_b64_tr_b16 a[38:39], v4 offset:1032\n"
  "ds_read_b64_tr_b16 a[40:41], v4 offset:8192\n"
  "ds_read_b64_tr_b16 a[42:43], v4 offset:9216\n"
  "ds_read_b64_tr_b16 a[44:45], v4 offset:8200\n"
  "ds_read_b64_tr_b16 a[46:47], v4 offset:9224\n"
  "s_waitcnt vmcnt(20) lgkmcnt(0)\n"
  "s_barrier\n"
  "s_add_u32 m0, s57, 32768\n"
  "s_mov_b32 s74, 256\n"
  "buffer_load_dwordx4 v8, s[40:43], s74 offen lds\n"
  "s_add_u32 m0, s57, 36864\n"
  "s_mov_b32 s74, 147712\n"
  "buffer_load_dwordx4 v8, s[40:43], s74 offen lds\n"
  "s_add_u32 m0, s57, 40960\n"
  "s_mov_b32 s74, 295168\n"
  "buffer_load_dwordx4 v8, s[40:43], s74 offen lds\n"
  "s_add_u32 m0, s57, 45056\n"
  "s_mov_b32 s74, 442624\n"
  "buffer_load_dwordx4 v8, s[40:43], s74 offen lds\n"
  "v_add_u32 v4, 49152, v9\n"
  "ds_read_b64_tr_b16 a[48:49], v4 offset:0\n"
  "ds_read_b64_tr_b16 a[50:51], v4 offset:1024\n"
  "ds_read_b64_tr_b16 a[52:53], v4 offset:8\n"
  "ds_read_b64_tr_b16 a[54:55], v4 offset:1032\n"
  "ds_read_b64_tr_b16 a[56:57], v4 offset:8192\n"
  "ds_read_b64_tr_b16 a[58:59], v4 offset:9216\n"
  "ds_read_b64_tr_b16 a[60:61], v4 offset:8200\n"
  "ds_read_b64_tr_b16 a[62:63], v4 offset:9224\n"
  "s_waitcnt vmcnt(20) lgkmcnt(0)\n"
  "s_barrier\n"
  "s_add_u32 m0, s57, 49152\n"
  "s_mov_b32 s74, 590080\n"
  "buffer_load_dwordx4 v8, s[40:43], s74 offen lds\n"
  "s_add_u32 m0, s57, 53248\n"
  "s_mov_b32 s74, 737536\n"
  "buffer_load_dwordx4 v8, s[40:43], s74 offen lds\n"
  "s_add_u32 m0, s57, 57344\n"
  "s_mov_b32 s74, 884992\n"
  "buffer_load_dwordx4 v8, s[40:43], s74 offen lds\n"
  "s_add_u32 m0, s57, 61440\n"
  "s_mov_b32 s74, 1032448\n"
  "buffer_load_dwordx4 v8, s[40:43], s74 offen lds\n"
  "v_add_u32 v4, 65536, v9\n"
  "ds_read_b64_tr_b16 a[64:65], v4 offset:0\n"
  "ds_read_b64_tr_b16 a[66:67], v4 offset:1024\n"
  "ds_read_b64_tr_b16 a[68:69], v4 offset:8\n"
  "ds_read_b64_tr_b16 a[70:71], v4 offset:1032\n"
  "ds_read_b64_tr_b16 a[72:73], v4 offset:8192\n"
  "ds_read_b64_tr_b16 a[74:75], v4 offset:9216\n"
  "ds_read_b64_tr_b16 a[76:77], v4 offset:8200\n"
  "ds_read_b64_tr_b16 a[78:79], v4 offset:9224\n"
  "s_waitcnt vmcnt(20) lgkmcnt(0)\n"
  "s_barrier\n"
  "s_add_u32 m0, s57, 65536\n"
  "s_mov_b32 s74, 1179904\n"
  "buffer_load_dwordx4 v8, s[40:43], s74 offen lds\n"
  "s_add_u32 m0, s57, 69632\n"
  "s_mov_b32 s74, 1327360\n"
  "buffer_load_dwordx4 v8, s[40:43], s74 offen lds\n"
  "s_add_u32 m0, s57, 73728\n"
  "s_mov_b32 s74, 1474816\n"
  "buffer_load_dwordx4 v8, s[40:43], s74 offen lds\n"
  "s_add_u32 m0, s57, 77824\n"
  "s_mov_b32 s74, 1622272\n"
  "buffer_load_dwordx4 v8, s[40:43], s74 offen lds\n"
  "v_add_u32 v4, 81920, v9\n"
  "ds_read_b64_tr_b16 a[80:81], v4 offset:0\n"
  "ds_read_b64_tr_b16 a[82:83], v4 offset:1024\n"
  "ds_read_b64_tr_b16 a[84:85], v4 offset:8\n"
  "ds_read_b64_tr_b16 a[86:87], v4 offset:1032\n"
  "ds_read_b64_tr_b16 a[88:89], v4 offset:8192\n"
  "ds_read_b64_tr_b16 a[90:91], v4 offset:9216\n"
  "ds_read_b64_tr_b16 a[92:93], v4 offset:8200\n"
  "ds_read_b64_tr_b16 a[94:95], v4 offset:9224\n"
  "s_waitcnt vmcnt(20) lgkmcnt(0)\n"
  "s_barrier\n"
  "s_add_u32 m0, s57, 81920\n"
  "s_mov_b32 s74, 1769728\n"
  "buffer_load_dwordx4 v8, s[40:43], s74 offen lds\n"
  "s_add_u32 m0, s57, 86016\n"
  "s_mov_b32 s74, 1917184\n"
  "buffer_load_dwordx4 v8, s[40:43], s74 offen lds\n"
  "s_add_u32 m0, s57, 90112\n"
  "s_mov_b32 s74, 2064640\n"
  "buffer_load_dwordx4 v8, s[40:43], s74 offen lds\n"
  "s_add_u32 m0, s57, 94208\n"
  "s_mov_b32 s74, 2212096\n"
  "buffer_load_dwordx4 v8, s[40:43], s74 offen lds\n"
  "v_add_u32 v4, 98304, v9\n"
  "ds_read_b64_tr_b16 a[96:97], v4 offset:0\n"
  "ds_read_b64_tr_b16 a[98:99], v4 offset:1024\n"
  "ds_read_b64_tr_b16 a[100:101], v4 offset:8\n"
  "ds_read_b64_tr_b16 a[102:103], v4 offset:1032\n"
  "ds_read_b64_tr_b16 a[104:105], v4 offset:8192\n"
  "ds_read_b64_tr_b16 a[106:107], v4 offset:9216\n"
  "ds_read_b64_tr_b16 a[108:109], v4 offset:8200\n"
  "ds_read_b64_tr_b16 a[110:111], v4 offset:9224\n"
  "s_waitcnt vmcnt(20) lgkmcnt(0)\n"
  "s_barrier\n"
  "s_add_u32 m0, s57, 98304\n"
  "s_mov_b32 s74, 2359552\n"
  "buffer_load_dwordx4 v8, s[40:43], s74 offen lds\n"
  "s_add_u32 m0, s57, 102400\n"
  "s_mov_b32 s74, 2507008\n"
  "buffer_load_dwordx4 v8, s[40:43], s74 offen lds\n"
  "s_add_u32 m0, s57, 106496\n"
  "s_mov_b32 s74, 2654464\n"
  "buffer_load_dwordx4 v8, s[40:43], s74 offen lds\n"
  "s_add_u32 m0, s57, 110592\n"
  "s_mov_b32 s74, 2801920\n"
  "buffer_load_dwordx4 v8, s[40:43], s74 offen lds\n"
  "v_add_u32 v4, 0, v9\n"
  "ds_read_b64_tr_b16 a[112:113], v4 offset:0\n"
  "ds_read_b64_tr_b16 a[114:115], v4 offset:1024\n"
  "ds_read_b64_tr_b16 a[116:117], v4 offset:8\n"
  "ds_read_b64_tr_b16 a[118:119], v4 offset:1032\n"
  "ds_read_b64_tr_b16 a[120:121], v4 offset:8192\n"
  "ds_read_b64_tr_b16 a[122:123], v4 offset:9216\n"
  "ds_read_b64_tr_b16 a[124:125], v4 offset:8200\n"
  "ds_read_b64_tr_b16 a[126:127], v4 offset:9224\n"
  "s_waitcnt vmcnt(20) lgkmcnt(0)\n"
  "s_barrier\n"
  "s_add_u32 m0, s57, 0\n"
  "s_mov_b32 s74, 2949376\n"
  "buffer_load_dwordx4 v8, s[40:43], s74 offen lds\n"
  "s_add_u32 m0, s57, 4096\n"
  "s_mov_b32 s74, 3096832\n"
  "buffer_load_dwordx4 v8, s[40:43], s74 offen lds\n"
  "s_add_u32 m0, s57, 8192\n"
  "s_mov_b32 s74, 3244288\n"
  "buffer_load_dwordx4 v8, s[40:43], s74 offen lds\n"
  "s_add_u32 m0, s57, 12288\n"
  "s_mov_b32 s74, 3391744\n"
  "buffer_load_dwordx4 v8, s[40:43], s74 offen lds\n"
  "v_add_u32 v4, 16384, v9\n"
  "ds_read_b64_tr_b16 a[128:129], v4 offset:0\n"
  "ds_read_b64_tr_b16 a[130:131], v4 offset:1024\n"
  "ds_read_b64_tr_b16 a[132:133], v4 offset:8\n"
  "ds_read_b64_tr_b16 a[134:135], v4 offset:1032\n"
  "ds_read_b64_tr_b16 a[136:137], v4 offset:8192\n"
  "ds_read_b64_tr_b16 a[138:139], v4 offset:9216\n"
  "ds_read_b64_tr_b16 a[140:141], v4 offset:8200\n"
  "ds_read_b64_tr_b16 a[142:143], v4 offset:9224\n"
  "s_waitcnt vmcnt(20) lgkmcnt(0)\n"
  "s_barrier\n"
  "s_add_u32 m0, s57, 16384\n"
  "s_mov_b32 s74, 3539200\n"
  "buffer_load_dwordx4 v8, s[40:43], s74 offen lds\n"
  "s_add_u32 m0, s57, 20480\n"
  "s_mov_b32 s74, 3686656\n"
  "buffer_load_dwordx4 v8, s[40:43], s74 offen lds\n"
  "s_add_u32 m0, s57, 24576\n"
  "s_mov_b32 s74, 3834112\n"
  "buffer_load_dwordx4 v8, s[40:43], s74 offen lds\n"
  "s_add_u32 m0, s57, 28672\n"
  "s_mov_b32 s74, 3981568\n"
  "buffer_load_dwordx4 v8, s[40:43], s74 offen lds\n"
  "v_add_u32 v4, 32768, v9\n"
  "ds_read_b64_tr_b16 a[144:145], v4 offset:0\n"
  "ds_read_b64_tr_b16 a[146:147], v4 offset:1024\n"
  "ds_read_b64_tr_b16 a[148:149], v4 offset:8\n"
  "ds_read_b64_tr_b16 a[150:151], v4 offset:1032\n"
  "ds_read_b64_tr_b16 a[152:153], v4 offset:8192\n"
  "ds_read_b64_tr_b16 a[154:155], v4 offset:9216\n"
  "ds_read_b64_tr_b16 a[156:157], v4 offset:8200\n"
  "ds_read_b64_tr_b16 a[158:159], v4 offset:9224\n"
  "s_waitcnt vmcnt(20) lgkmcnt(0)\n"
  "s_barrier\n"
  "s_add_u32 m0, s57, 32768\n"
  "s_mov_b32 s74, 4129024\n"
  "buffer_load_dwordx4 v8, s[40:43], s74 offen lds\n"
  "s_add_u32 m0, s57, 36864\n"
  "s_mov_b32 s74, 4276480\n"
  "buffer_load_dwordx4 v8, s[40:43], s74 offen lds\n"
  "s_add_u32 m0, s57, 40960\n"
  "s_mov_b32 s74, 4423936\n"
  "buffer_load_dwordx4 v8, s[40:43], s74 offen lds\n"
  "s_add_u32 m0, s57, 45056\n"
  "s_mov_b32 s74, 4571392\n"
  "buffer_load_dwordx4 v8, s[40:43], s74 offen lds\n"
  "v_add_u32 v4, 49152, v9\n"
  "ds_read_b64_tr_b16 a[160:161], v4 offset:0\n"
  "ds_read_b64_tr_b16 a[162:163], v4 offset:1024\n"
  "ds_read_b64_tr_b16 a[164:165], v4 offset:8\n"
  "ds_read_b64_tr_b16 a[166:167], v4 offset:1032\n"
  "ds_read_b64_tr_b16 a[168:169], v4 offset:8192\n"
  "ds_read_b64_tr_b16 a[170:171], v4 offset:9216\n"
  "ds_read_b64_tr_b16 a[172:173], v4 offset:8200\n"
  "ds_read_b64_tr_b16 a[174:175], v4 offset:9224\n"
  "s_waitcnt vmcnt(20) lgkmcnt(0)\n"
  "s_barrier\n"
  "s_add_u32 m0, s57, 49152\n"
  "s_mov_b32 s74, 4718848\n"
  "buffer_load_dwordx4 v8, s[40:43], s74 offen lds\n"
  "s_add_u32 m0, s57, 53248\n"
  "s_mov_b32 s74, 4866304\n"
  "buffer_load_dwordx4 v8, s[40:43], s74 offen lds\n"
  "s_add_u32 m0, s57, 57344\n"
  "s_mov_b32 s74, 5013760\n"
  "buffer_load_dwordx4 v8, s[40:43], s74 offen lds\n"
  "s_add_u32 m0, s57, 61440\n"
  "s_mov_b32 s74, 5161216\n"
  "buffer_load_dwordx4 v8, s[40:43], s74 offen lds\n"
  "v_add_u32 v4, 65536, v9\n"
  "ds_read_b64_tr_b16 a[176:177], v4 offset:0\n"
  "ds_read_b64_tr_b16 a[178:179], v4 offset:1024\n"
  "ds_read_b64_tr_b16 a[180:181], v4 offset:8\n"
  "ds_read_b64_tr_b16 a[182:183], v4 offset:1032\n"
  "ds_read_b64_tr_b16 a[184:185], v4 offset:8192\n"
  "ds_read_b64_tr_b16 a[186:187], v4 offset:9216\n"
  "ds_read_b64_tr_b16 a[188:189], v4 offset:8200\n"
  "ds_read_b64_tr_b16 a[190:191], v4 offset:9224\n"
  "s_waitcnt vmcnt(20) lgkmcnt(0)\n"
  "s_barrier\n"
  "v_add_u32 v4, 81920, v9\n"
  "ds_read_b64_tr_b16 a[192:193], v4 offset:0\n"
  "ds_read_b64_tr_b16 a[194:195], v4 offset:1024\n"
  "ds_read_b64_tr_b16 a[196:197], v4 offset:8\n"
  "ds_read_b64_tr_b16 a[198:199], v4 offset:1032\n"
  "ds_read_b64_tr_b16 a[200:201], v4 offset:8192\n"
  "ds_read_b64_tr_b16 a[202:203], v4 offset:9216\n"
  "ds_read_b64_tr_b16 a[204:205], v4 offset:8200\n"
  "ds_read_b64_tr_b16 a[206:207], v4 offset:9224\n"
  "s_waitcnt vmcnt(16) lgkmcnt(0)\n"
  "s_barrier\n"
  "v_add_u32 v4, 98304, v9\n"
  "ds_read_b64_tr_b16 a[208:209], v4 offset:0\n"
  "ds_read_b64_tr_b16 a[210:211], v4 offset:1024\n"
  "ds_read_b64_tr_b16 a[212:213], v4 offset:8\n"
  "ds_read_b64_tr_b16 a[214:215], v4 offset:1032\n"
  "ds_read_b64_tr_b16 a[216:217], v4 offset:8192\n"
  "ds_read_b64_tr_b16 a[218:219], v4 offset:9216\n"
  "ds_read_b64_tr_b16 a[220:221], v4 offset:8200\n"
  "ds_read_b64_tr_b16 a[222:223], v4 offset:9224\n"
  "s_waitcnt vmcnt(12) lgkmcnt(0)\n"
  "s_barrier\n"
  "v_add_u32 v4, 0, v9\n"
  "ds_read_b64_tr_b16 a[224:225], v4 offset:0\n"
  "ds_read_b64_tr_b16 a[226:227], v4 offset:1024\n"
  "ds_read_b64_tr_b16 a[228:229], v4 offset:8\n"
  "ds_read_b64_tr_b16 a[230:231], v4 offset:1032\n"
  "ds_read_b64_tr_b16 a[232:233], v4 offset:8192\n"
  "ds_read_b64_tr_b16 a[234:235], v4 offset:9216\n"
  "ds_read_b64_tr_b16 a[236:237], v4 offset:8200\n"
  "ds_read_b64_tr_b16 a[238:239], v4 offset:9224\n"
  "s_waitcnt vmcnt(8) lgkmcnt(0)\n"
  "s_barrier\n"
  "v_add_u32 v4, 16384, v9\n"
  "ds_read_b64_tr_b16 a[240:241], v4 offset:0\n"
  "ds_read_b64_tr_b16 a[242:243], v4 offset:1024\n"
  "ds_read_b64_tr_b16 a[244:245], v4 offset:8\n"
  "ds_read_b64_tr_b16 a[246:247], v4 offset:1032\n"
  "ds_read_b64_tr_b16 a[248:249], v4 offset:8192\n"
  "ds_read_b64_tr_b16 a[250:251], v4 offset:9216\n"
  "ds_read_b64_tr_b16 a[252:253], v4 offset:8200\n"
  "ds_read_b64_tr_b16 a[254:255], v4 offset:9224\n"
  "s_waitcnt vmcnt(4) lgkmcnt(0)\n"
  "s_barrier\n"
  "v_add_u32 v4, 32768, v9\n"
  "ds_read_b64_tr_b16 v[224:225], v4 offset:0\n"
  "ds_read_b64_tr_b16 v[226:227], v4 offset:1024\n"
  "ds_read_b64_tr_b16 v[228:229], v4 offset:8\n"
  "ds_read_b64_tr_b16 v[230:231], v4 offset:1032\n"
  "ds_read_b64_tr_b16 v[232:233], v4 offset:8192\n"
  "ds_read_b64_tr_b16 v[234:235], v4 offset:9216\n"
  "ds_read_b64_tr_b16 v[236:237], v4 offset:8200\n"
  "ds_read_b64_tr_b16 v[238:239], v4 offset:9224\n"
  "s_waitcnt vmcnt(0) lgkmcnt(0)\n"
  "s_barrier\n"
  "v_add_u32 v4, 49152, v9\n"
  "ds_read_b64_tr_b16 v[240:241], v4 offset:0\n"
  "ds_read_b64_tr_b16 v[242:243], v4 offset:1024\n"
  "ds_read_b64_tr_b16 v[244:245], v4 offset:8\n"
  "ds_read_b64_tr_b16 v[246:247], v4 offset:1032\n"
  "ds_read_b64_tr_b16 v[248:249], v4 offset:8192\n"
  "ds_read_b64_tr_b16 v[250:251], v4 offset:9216\n"
  "ds_read_b64_tr_b16 v[252:253], v4 offset:8200\n"
  "ds_read_b64_tr_b16 v[254:255], v4 offset:9224\n"
  "s_waitcnt lgkmcnt(0)\n"
  "s_barrier\n"
  "v_lshrrev_b32 v4, 2, v0\n"
  "v_add_u32 v4, 163584, v4\n"
  "v_mov_b32 v6, 0\n"
  "ds_write_b32 v4, v6\n"
  "v_min_u32 v5, 1, v0\n"
  "v_lshlrev_b32 v5, 30, v5\n"
  "s_mov_b32 s70, 0\n"
  "s_mov_b32 s71, 0\n"
  "s_mov_b32 s72, 0\n"
  "s_mov_b32 s81, 0\n"
  "s_mov_b32 s82, 0\n"
  "s_mov_b32 s83, 0\n"
  "s_mov_b32 s95, -1\n"
  "s_cmp_eq_u32 s57, 0\n"
  "s_cselect_b32 s92, s58, 0x80000000\n"
  "s_waitcnt lgkmcnt(0)\n"
  "s_barrier\n"
  "v_mov_b32 v48, 0xc0135761\n"
  "s_mov_b32 s90, 0xbdd2d3e8\n"
  "s_branch L_first_%=\n"
  "L_pf_%=:\n"
  "s_waitcnt vmcnt(5)\n"
  "L_go_%=:\n"
  "s_barrier\n"
  "ds_read_b128 v[16:19], v1 offset:0\n"
  "ds_read_b128 v[20:23], v1 offset:1024\n"
  "ds_read_b128 v[24:27], v1 offset:2048\n"
  "ds_read_b128 v[28:31], v1 offset:3072\n"
  "s_waitcnt lgkmcnt(3)\n"
  "v_mfma_f32_16x16x32_bf16 v[96:99], a[0:3], v[16:19], v[64:67]\n"
  "ds_read_b128 v[32:35], v1 offset:4096\n"
  "v_mfma_f32_16x16x32_bf16 v[100:103], a[4:7], v[16:19], v[68:71]\n"
  "s_waitcnt lgkmcnt(3)\n"
  "v_mfma_f32_16x16x32_bf16 v[96:99], a[8:11], v[20:23], v[96:99]\n"
  "ds_read_b128 v[36:39], v1 offset:5120\n"
  "v_mfma_f32_16x16x32_bf16 v[100:103], a[12:15], v[20:23], v[100:103]\n"
  "s_add_u32 m0, s79, 20480\n"
  "s_add_u32 s76, s78, 256\n"
  "s_waitcnt lgkmcnt(2)\n"
  "v_mfma_f32_16x16x32_bf16 v[96:99], a[16:19], v[24:27], v[96:99]\n"
  "ds_read_b128 v[16:19], v1 offset:6144\n"
  "buffer_load_dwordx4 v7, s[36:39], s76 offen sc1 lds\n"
  "v_mfma_f32_16x16x32_bf16 v[100:103], a[20:23], v[24:27], v[100:103]\n"
  "v_mfma_f32_16x16x32_bf16 v[96:99], a[24:27], v[28:31], v[96:99]\n"
  "ds_read_b128 v[20:23], v1 offset:7168\n"
  "v_mfma_f32_16x16x32_bf16 v[100:103], a[28:31], v[28:31], v[100:103]\n"
  "s_waitcnt lgkmcnt(2)\n"
  "v_mfma_f32_16x16x32_bf16 v[96:99], a[32:35], v[32:35], v[96:99]\n"
  "ds_read_b128 v[24:27], v1 offset:8192\n"
  "s_add_u32 m0, s79, 24576\n"
  "s_add_u32 s76, s78, 16640\n"
  "v_mfma_f32_16x16x32_bf16 v[100:103], a[36:39], v[32:35], v[100:103]\n"
  "buffer_load_dwordx4 v7, s[36:39], s76 offen sc1 lds\n"
  "v_mfma_f32_16x16x32_bf16 v[96:99], a[40:43], v[36:39], v[96:99]\n"
  "ds_read_b128 v[28:31], v1 offset:9216\n"
  "v_mfma_f32_16x16x32_bf16 v[100:103], a[44:47], v[36:39], v[100:103]\n"
  "s_waitcnt lgkmcnt(2)\n"
  "v_mfma_f32_16x16x32_bf16 v[96:99], a[48:51], v[16:19], v[96:99]\n"
  "ds_read_b128 v[32:35], v1 offset:10240\n"
  "v_mfma_f32_16x16x32_bf16 v[100:103], a[52:55], v[16:19], v[100:103]\n"
  "s_add_u32 m0, s79, 28672\n"
  "s_add_u32 s76, s78, 33024\n"
  "v_mfma_f32_16x16x32_bf16 v[96:99], a[56:59], v[20:23], v[96:99]\n"
  "ds_read_b128 v[36:39], v1 offset:11264\n"
  "buffer_load_dwordx4 v7, s[36:39], s76 offen sc1 lds\n"
  "v_mfma_f32_16x16x32_bf16 v[100:103], a[60:63], v[20:23], v[100:103]\n"
  "s_cmp_lg_u32 s82, 0\n"
  "s_waitcnt lgkmcnt(2)\n"
  "v_mfma_f32_16x16x32_bf16 v[96:99], a[64:67], v[24:27], v[96:99]\n"
  "s_waitcnt vmcnt(6)\n"
  "s_cbranch_scc0 L_bn_%=\n"
  "L_bm_%=:\n"
  "s_barrier\n"
  "ds_read_b128 v[16:19], v1 offset:12288\n"
  "v_mfma_f32_16x16x32_bf16 v[100:103], a[68:71], v[24:27], v[100:103]\n"
  "v_mfma_f32_16x16x32_bf16 v[96:99], a[72:75], v[28:31], v[96:99]\n"
  "ds_read_b128 v[20:23], v1 offset:13312\n"
  "s_add_u32 m0, s79, 32768\n"
  "s_add_u32 s76, s78, 49408\n"
  "v_mfma_f32_16x16x32_bf16 v[100:103], a[76:79], v[28:31], v[100:103]\n"
  "buffer_load_dwordx4 v7, s[36:39], s76 offen sc1 lds\n"
  "s_waitcnt lgkmcnt(2)\n"
  "v_mfma_f32_16x16x32_bf16 v[96:99], a[80:83], v[32:35], v[96:99]\n"
  "ds_read_b128 v[24:27], v1 offset:14336\n"
  "v_mfma_f32_16x16x32_bf16 v[100:103], a[84:87], v[32:35], v[100:103]\n"
  "s_add_u32 s75, s70, -1\n"
  "v_mov_b32 v6, s75\n"
  "v_mfma_f32_16x16x32_bf16 v[96:99], a[88:91], v[36:39], v[96:99]\n"
  "ds_read_b128 v[28:31], v1 offset:15360\n"
  "s_add_u32 s78, s70, 1\n"
  "s_and_b32 s78, s78, s61\n"
  "v_mfma_f32_16x16x32_bf16 v[100:103], a[92:95], v[36:39], v[100:103]\n"
  "s_mul_i32 s78, s78, s60\n"
  "s_lshr_b32 s76, s78, 8\n"
  "s_waitcnt lgkmcnt(2)\n"
  "v_mfma_f32_16x16x32_bf16 v[96:99], a[96:99], v[16:19], v[96:99]\n"
  "ds_read_b128 v[32:35], v1 offset:16384\n"
  "s_and_b32 s76, s76, 8192\n"
  "s_add_u32 s78, s78, s59\n"
  "v_mfma_f32_16x16x32_bf16 v[100:103], a[100:103], v[16:19], v[100:103]\n"
  "s_xor_b32 s78, s78, s76\n"
  "s_xor_b32 s79, s81, 65536\n"
  "v_mfma_f32_16x16x32_bf16 v[96:99], a[104:107], v[20:23], v[96:99]\n"
  "ds_read_b128 v[36:39], v1 offset:17408\n"
  "s_add_u32 s79, s79, s57\n"
  "s_add_u32 s80, s70, 0\n"
  "v_mfma_f32_16x16x32_bf16 v[100:103], a[108:111], v[20:23], v[100:103]\n"
  "s_and_b32 s80, s80, 15\n"
  "s_lshl_b32 s80, s80, 22\n"
  "s_waitcnt lgkmcnt(2)\n"
  "v_mfma_f32_16x16x32_bf16 v[96:99], a[112:115], v[24:27], v[96:99]\n"
  "ds_read_b128 v[16:19], v1 offset:0\n"
  "s_add_u32 s80, s80, s62\n"
  "v_mfma_f32_16x16x32_bf16 v[100:103], a[116:119], v[24:27], v[100:103]\n"
  "s_add_u32 s77, s80, 16384\n"
  "v_mfma_f32_16x16x32_bf16 v[96:99], a[120:123], v[28:31], v[96:99]\n"
  "ds_read_b128 v[20:23], v1 offset:1024\n"
  "v_mfma_f32_16x16x32_bf16 v[100:103], a[124:127], v[28:31], v[100:103]\n"
  "s_waitcnt lgkmcnt(2)\n"
  "v_mfma_f32_16x16x32_bf16 v[96:99], a[128:131], v[32:35], v[96:99]\n"
  "ds_read_b128 v[24:27], v1 offset:2048\n"
  "v_mfma_f32_16x16x32_bf16 v[100:103], a[132:135], v[32:35], v[100:103]\n"
  "v_mfma_f32_16x16x32_bf16 v[96:99], a[136:139], v[36:39], v[96:99]\n"
  "ds_read_b128 v[28:31], v1 offset:3072\n"
  "v_mfma_f32_16x16x32_bf16 v[100:103], a[140:143], v[36:39], v[100:103]\n"
  "s_waitcnt lgkmcnt(2)\n"
  "v_mfma_f32_16x16x32_bf16 v[104:107], a[144:147], v[16:19], v[72:75]\n"
  "ds_read_b128 v[32:35], v1 offset:4096\n"
  "v_mfma_f32_16x16x32_bf16 v[108:111], a[148:151], v[16:19], v[76:79]\n"
  "v_mul_f32 v52, v96, v96\n"
  "v_mul_f32 v53, v97, v97\n"
  "v_mfma_f32_16x16x32_bf16 v[104:107], a[152:155], v[20:23], v[104:107]\n"
  "ds_read_b128 v[36:39], v1 offset:5120\n"
  "v_fmamk_f32 v52, v52, 0xbdd2d3e8, v48\n"
  "v_fmamk_f32 v53, v53, 0xbdd2d3e8, v48\n"
  "v_mfma_f32_16x16x32_bf16 v[108:111], a[156:159], v[20:23], v[108:111]\n"
  "v_mul_f32 v52, v52, v96\n"
  "v_mul_f32 v53, v53, v97\n"
  "s_waitcnt lgkmcnt(2)\n"
  "v_mfma_f32_16x16x32_bf16 v[104:107], a[160:163], v[24:27], v[104:107]\n"
  "ds_read_b128 v[16:19], v1 offset:6144\n"
  "v_exp_f32 v52, v52\n"
  "v_mfma_f32_16x16x32_bf16 v[108:111], a[164:167], v[24:27], v[108:111]\n"
  "v_exp_f32 v53, v53\n"
  "v_mfma_f32_16x16x32_bf16 v[104:107], a[168:171], v[28:31], v[104:107]\n"
  "ds_read_b128 v[20:23], v1 offset:7168\n"
  "v_add_f32 v52, 1.0, v52\n"
  "v_add_f32 v53, 1.0, v53\n"
  "v_mfma_f32_16x16x32_bf16 v[108:111], a[172:175], v[28:31], v[108:111]\n"
  "v_rcp_f32 v52, v52\n"
  "s_waitcnt lgkmcnt(2)\n"
  "v_mfma_f32_16x16x32_bf16 v[104:107], a[176:179], v[32:35], v[104:107]\n"
  "ds_read_b128 v[24:27], v1 offset:8192\n"
  "v_rcp_f32 v53, v53\n"
  "v_mfma_f32_16x16x32_bf16 v[108:111], a[180:183], v[32:35], v[108:111]\n"
  "v_mul_f32 v96, v96, v52\n"
  "v_mul_f32 v97, v97, v53\n"
  "v_cvt_pk_bf16_f32 v40, v96, v97\n"
  "v_mfma_f32_16x16x32_bf16 v[104:107], a[184:187], v[36:39], v[104:107]\n"
  "ds_read_b128 v[28:31], v1 offset:9216\n"
  "v_mul_f32 v52, v98, v98\n"
  "v_mul_f32 v53, v99, v99\n"
  "v_mfma_f32_16x16x32_bf16 v[108:111], a[188:191], v[36:39], v[108:111]\n"
  "v_fmamk_f32 v52, v52, 0xbdd2d3e8, v48\n"
  "v_fmamk_f32 v53, v53, 0xbdd2d3e8, v48\n"
  "s_waitcnt lgkmcnt(2)\n"
  "v_mfma_f32_16x16x32_bf16 v[104:107], a[192:195], v[16:19], v[104:107]\n"
  "ds_read_b128 v[32:35], v1 offset:10240\n"
  "v_mul_f32 v52, v52, v98\n"
  "v_mul_f32 v53, v53, v99\n"
  "v_mfma_f32_16x16x32_bf16 v[108:111], a[196:199], v[16:19], v[108:111]\n"
  "v_exp_f32 v52, v52\n"
  "v_mfma_f32_16x16x32_bf16 v[104:107], a[200:203], v[20:23], v[104:107]\n"
  "ds_read_b128 v[36:39], v1 offset:11264\n"
  "v_exp_f32 v53, v53\n"
  "v_mfma_f32_16x16x32_bf16 v[108:111], a[204:207], v[20:23], v[108:111]\n"
  "v_add_f32 v52, 1.0, v52\n"
  "v_add_f32 v53, 1.0, v53\n"
  "s_waitcnt lgkmcnt(2)\n"
  "v_mfma_f32_16x16x32_bf16 v[104:107], a[208:211], v[24:27], v[104:107]\n"
  "ds_read_b128 v[16:19], v1 offset:12288\n"
  "v_rcp_f32 v52, v52\n"
  "v_mfma_f32_16x16x32_bf16 v[108:111], a[212:215], v[24:27], v[108:111]\n"
  "s_waitcnt vmcnt(0)\n"
  "s_barrier\n"
  "v_rcp_f32 v53, v53\n"
  "v_mfma_f32_16x16x32_bf16 v[104:107], a[216:219], v[28:31], v[104:107]\n"
  "ds_read_b128 v[20:23], v1 offset:13312\n"
  "v_mul_f32 v98, v98, v52\n"
  "v_mul_f32 v99, v99, v53\n"
  "v_cvt_pk_bf16_f32 v41, v98, v99\n"
  "v_mfma_f32_16x16x32_bf16 v[108:111], a[220:223], v[28:31], v[108:111]\n"
  "buffer_store_dword v6, v5, s[36:39], s92 offen sc1\n"
  "v_mul_f32 v52, v100, v100\n"
  "v_mul_f32 v53, v101, v101\n"
  "s_waitcnt lgkmcnt(2)\n"
  "v_mfma_f32_16x16x32_bf16 v[104:107], a[224:227], v[32:35], v[104:107]\n"
  "ds_read_b128 v[24:27], v1 offset:14336\n"
  "v_fmamk_f32 v52, v52, 0xbdd2d3e8, v48\n"
  "v_fmamk_f32 v53, v53, 0xbdd2d3e8, v48\n"
  "v_mfma_f32_16x16x32_bf16 v[108:111], a[228:231], v[32:35], v[108:111]\n"
  "v_mul_f32 v52, v52, v100\n"
  "v_mul_f32 v53, v53, v101\n"
  "v_mfma_f32_16x16x32_bf16 v[104:107], a[232:235], v[36:39], v[104:107]\n"
  "ds_read_b128 v[28:31], v1 offset:15360\n"
  "v_exp_f32 v52, v52\n"
  "v_mfma_f32_16x16x32_bf16 v[108:111], a[236:239], v[36:39], v[108:111]\n"
  "v_exp_f32 v53, v53\n"
  "s_waitcnt lgkmcnt(2)\n"
  "v_mfma_f32_16x16x32_bf16 v[104:107], a[240:243], v[16:19], v[104:107]\n"
  "ds_read_b128 v[32:35], v1 offset:16384\n"
  "v_add_f32 v52, 1.0, v52\n"
  "v_add_f32 v53, 1.0, v53\n"
  "v_mfma_f32_16x16x32_bf16 v[108:111], a[244:247], v[16:19], v[108:111]\n"
  "v_rcp_f32 v52, v52\n"
  "v_mfma_f32_16x16x32_bf16 v[104:107], a[248:251], v[20:23], v[104:107]\n"
  "ds_read_b128 v[36:39], v1 offset:17408\n"
  "v_rcp_f32 v53, v53\n"
  "v_mfma_f32_16x16x32_bf16 v[108:111], a[252:255], v[20:23], v[108:111]\n"
  "v_mul_f32 v100, v100, v52\n"
  "v_mul_f32 v101, v101, v53\n"
  "v_cvt_pk_bf16_f32 v42, v100, v101\n"
  "s_waitcnt lgkmcnt(2)\n"
  "v_mfma_f32_16x16x32_bf16 v[104:107], v[224:227], v[24:27], v[104:107]\n"
  "ds_read_b128 v[16:19], v1 offset:20480\n"
  "v_mul_f32 v52, v102, v102\n"
  "v_mul_f32 v53, v103, v103\n"
  "v_mfma_f32_16x16x32_bf16 v[108:111], v[228:231], v[24:27], v[108:111]\n"
  "v_fmamk_f32 v52, v52, 0xbdd2d3e8, v48\n"
  "v_fmamk_f32 v53, v53, 0xbdd2d3e8, v48\n"
  "v_mfma_f32_16x16x32_bf16 v[104:107], v[232:235], v[28:31], v[104:107]\n"
  "ds_read_b128 v[20:23], v1 offset:21504\n"
  "v_mul_f32 v52, v52, v102\n"
  "v_mul_f32 v53, v53, v103\n"
  "v_mfma_f32_16x16x32_bf16 v[108:111], v[236:239], v[28:31], v[108:111]\n"
  "v_exp_f32 v52, v52\n"
  "s_waitcnt lgkmcnt(2)\n"
  "v_mfma_f32_16x16x32_bf16 v[104:107], v[240:243], v[32:35], v[104:107]\n"
  "ds_read_b128 v[24:27], v1 offset:22528\n"
  "v_exp_f32 v53, v53\n"
  "v_mfma_f32_16x16x32_bf16 v[108:111], v[244:247], v[32:35], v[108:111]\n"
  "buffer_load_dword v4, v2, s[36:39], 0 offen sc1\n"
  "v_add_f32 v52, 1.0, v52\n"
  "v_add_f32 v53, 1.0, v53\n"
  "v_mfma_f32_16x16x32_bf16 v[104:107], v[248:251], v[36:39], v[104:107]\n"
  "ds_read_b128 v[28:31], v1 offset:23552\n"
  "v_rcp_f32 v52, v52\n"
  "v_mfma_f32_16x16x32_bf16 v[108:111], v[252:255], v[36:39], v[108:111]\n"
  "v_rcp_f32 v53, v53\n"
  "s_waitcnt lgkmcnt(2)\n"
  "v_mfma_f32_16x16x32_bf16 v[112:115], a[0:3], v[16:19], v[64:67]\n"
  "ds_read_b128 v[32:35], v1 offset:24576\n"
  "v_mul_f32 v102, v102, v52\n"
  "v_mul_f32 v103, v103, v53\n"
  "v_cvt_pk_bf16_f32 v43, v102, v103\n"
  "v_mfma_f32_16x16x32_bf16 v[116:119], a[4:7], v[16:19], v[68:71]\n"
  "buffer_store_dwordx4 v[40:43], v7, s[36:39], s80 offen offset:0 sc1\n"
  "v_mul_f32 v52, v104, v104\n"
  "v_mul_f32 v53, v105, v105\n"
  "v_mfma_f32_16x16x32_bf16 v[112:115], a[8:11], v[20:23], v[112:115]\n"
  "ds_read_b128 v[36:39], v1 offset:25600\n"
  "v_fmamk_f32 v52, v52, 0xbdd2d3e8, v48\n"
  "v_fmamk_f32 v53, v53, 0xbdd2d3e8, v48\n"
  "v_mfma_f32_16x16x32_bf16 v[116:119], a[12:15], v[20:23], v[116:119]\n"
  "v_mul_f32 v52, v52, v104\n"
  "v_mul_f32 v53, v53, v105\n"
  "s_waitcnt lgkmcnt(2)\n"
  "v_mfma_f32_16x16x32_bf16 v[112:115], a[16:19], v[24:27], v[112:115]\n"
  "ds_read_b128 v[16:19], v1 offset:26624\n"
  "v_exp_f32 v52, v52\n"
  "v_mfma_f32_16x16x32_bf16 v[116:119], a[20:23], v[24:27], v[116:119]\n"
  "v_exp_f32 v53, v53\n"
  "v_mfma_f32_16x16x32_bf16 v[112:115], a[24:27], v[28:31], v[112:115]\n"
  "ds_read_b128 v[20:23], v1 offset:27648\n"
  "v_add_f32 v52, 1.0, v52\n"
  "v_add_f32 v53, 1.0, v53\n"
  "v_mfma_f32_16x16x32_bf16 v[116:119], a[28:31], v[28:31], v[116:119]\n"
  "v_rcp_f32 v52, v52\n"
  "s_waitcnt lgkmcnt(2)\n"
  "v_mfma_f32_16x16x32_bf16 v[112:115], a[32:35], v[32:35], v[112:115]\n"
  "ds_read_b128 v[24:27], v1 offset:28672\n"
  "v_rcp_f32 v53, v53\n"
  "v_mfma_f32_16x16x32_bf16 v[116:119], a[36:39], v[32:35], v[116:119]\n"
  "v_mul_f32 v104, v104, v52\n"
  "v_mul_f32 v105, v105, v53\n"
  "v_cvt_pk_bf16_f32 v44, v104, v105\n"
  "v_mfma_f32_16x16x32_bf16 v[112:115], a[40:43], v[36:39], v[112:115]\n"
  "ds_read_b128 v[28:31], v1 offset:29696\n"
  "v_mul_f32 v52, v106, v106\n"
  "v_mul_f32 v53, v107, v107\n"
  "v_mfma_f32_16x16x32_bf16 v[116:119], a[44:47], v[36:39], v[116:119]\n"
  "v_fmamk_f32 v52, v52, 0xbdd2d3e8, v48\n"
  "v_fmamk_f32 v53, v53, 0xbdd2d3e8, v48\n"
  "s_waitcnt lgkmcnt(2)\n"
  "v_mfma_f32_16x16x32_bf16 v[112:115], a[48:51], v[16:19], v[112:115]\n"
  "ds_read_b128 v[32:35], v1 offset:30720\n"
  "v_mul_f32 v52, v52, v106\n"
  "v_mul_f32 v53, v53, v107\n"
  "v_mfma_f32_16x16x32_bf16 v[116:119], a[52:55], v[16:19], v[116:119]\n"
  "v_exp_f32 v52, v52\n"
  "v_mfma_f32_16x16x32_bf16 v[112:115], a[56:59], v[20:23], v[112:115]\n"
  "ds_read_b128 v[36:39], v1 offset:31744\n"
  "v_exp_f32 v53, v53\n"
  "v_mfma_f32_16x16x32_bf16 v[116:119], a[60:63], v[20:23], v[116:119]\n"
  "v_add_f32 v52, 1.0, v52\n"
  "v_add_f32 v53, 1.0, v53\n"
  "s_waitcnt lgkmcnt(2)\n"
  "v_mfma_f32_16x16x32_bf16 v[112:115], a[64:67], v[24:27], v[112:115]\n"
  "ds_read_b128 v[16:19], v1 offset:32768\n"
  "v_rcp_f32 v52, v52\n"
  "v_mfma_f32_16x16x32_bf16 v[116:119], a[68:71], v[24:27], v[116:119]\n"
  "v_rcp_f32 v53, v53\n"
  "v_mfma_f32_16x16x32_bf16 v[112:115], a[72:75], v[28:31], v[112:115]\n"
  "ds_read_b128 v[20:23], v1 offset:33792\n"
  "v_mul_f32 v106, v106, v52\n"
  "v_mul_f32 v107, v107, v53\n"
  "v_cvt_pk_bf16_f32 v45, v106, v107\n"
  "v_mfma_f32_16x16x32_bf16 v[116:119], a[76:79], v[28:31], v[116:119]\n"
  "v_mul_f32 v52, v108, v108\n"
  "v_mul_f32 v53, v109, v109\n"
  "s_waitcnt lgkmcnt(2)\n"
  "v_mfma_f32_16x16x32_bf16 v[112:115], a[80:83], v[32:35], v[112:115]\n"
  "ds_read_b128 v[24:27], v1 offset:34816\n"
  "v_fmamk_f32 v52, v52, 0xbdd2d3e8, v48\n"
  "v_fmamk_f32 v53, v53, 0xbdd2d3e8, v48\n"
  "v_mfma_f32_16x16x32_bf16 v[116:119], a[84:87], v[32:35], v[116:119]\n"
  "v_mul_f32 v52, v52, v108\n"
  "v_mul_f32 v53, v53, v109\n"
  "v_mfma_f32_16x16x32_bf16 v[112:115], a[88:91], v[36:39], v[112:115]\n"
  "ds_read_b128 v[28:31], v1 offset:35840\n"
  "v_exp_f32 v52, v52\n"
  "v_mfma_f32_16x16x32_bf16 v[116:119], a[92:95], v[36:39], v[116:119]\n"
  "v_exp_f32 v53, v53\n"
  "s_waitcnt lgkmcnt(2)\n"
  "v_mfma_f32_16x16x32_bf16 v[112:115], a[96:99], v[16:19], v[112:115]\n"
  "ds_read_b128 v[32:35], v1 offset:18432\n"
  "v_add_f32 v52, 1.0, v52\n"
  "v_add_f32 v53, 1.0, v53\n"
  "v_mfma_f32_16x16x32_bf16 v[116:119], a[100:103], v[16:19], v[116:119]\n"
  "v_rcp_f32 v52, v52\n"
  "v_mfma_f32_16x16x32_bf16 v[112:115], a[104:107], v[20:23], v[112:115]\n"
  "ds_read_b128 v[36:39], v1 offset:19456\n"
  "v_rcp_f32 v53, v53\n"
  "v_mfma_f32_16x16x32_bf16 v[116:119], a[108:111], v[20:23], v[116:119]\n"
  "v_mul_f32 v108, v108, v52\n"
  "v_mul_f32 v109, v109, v53\n"
  "v_cvt_pk_bf16_f32 v46, v108, v109\n"
  "s_waitcnt lgkmcnt(2)\n"
  "v_mfma_f32_16x16x32_bf16 v[112:115], a[112:115], v[24:27], v[112:115]\n"
  "ds_read_b128 v[16:19], v1 offset:20480\n"
  "v_mul_f32 v52, v110, v110\n"
  "v_mul_f32 v53, v111, v111\n"
  "v_mfma_f32_16x16x32_bf16 v[116:119], a[116:119], v[24:27], v[116:119]\n"
  "v_fmamk_f32 v52, v52, 0xbdd2d3e8, v48\n"
  "v_fmamk_f32 v53, v53, 0xbdd2d3e8, v48\n"
  "v_mfma_f32_16x16x32_bf16 v[112:115], a[120:123], v[28:31], v[112:115]\n"
  "ds_read_b128 v[20:23], v1 offset:21504\n"
  "v_mul_f32 v52, v52, v110\n"
  "v_mul_f32 v53, v53, v111\n"
  "v_mfma_f32_16x16x32_bf16 v[116:119], a[124:127], v[28:31], v[116:119]\n"
  "v_exp_f32 v52, v52\n"
  "s_waitcnt lgkmcnt(2)\n"
  "v_mfma_f32_16x16x32_bf16 v[112:115], a[128:131], v[32:35], v[112:115]\n"
  "ds_read_b128 v[24:27], v1 offset:22528\n"
  "v_exp_f32 v53, v53\n"
  "v_mfma_f32_16x16x32_bf16 v[116:119], a[132:135], v[32:35], v[116:119]\n"
  "v_add_f32 v52, 1.0, v52\n"
  "v_add_f32 v53, 1.0, v53\n"
  "v_mfma_f32_16x16x32_bf16 v[112:115], a[136:139], v[36:39], v[112:115]\n"
  "ds_read_b128 v[28:31], v1 offset:23552\n"
  "v_rcp_f32 v52, v52\n"
  "s_waitcnt vmcnt(1)\n"
  "v_subrev_u32 v6, v3, v4\n"
  "v_cmp_lt_i32 vcc, s70, v6\n"
  "v_mfma_f32_16x16x32_bf16 v[116:119], a[140:143], v[36:39], v[116:119]\n"
  "v_rcp_f32 v53, v53\n"
  "s_cmp_lt_i32 s70, 63\n"
  "s_cselect_b32 s94, -1, 0\n"
  "s_waitcnt lgkmcnt(2)\n"
  "v_mfma_f32_16x16x32_bf16 v[120:123], a[144:147], v[16:19], v[72:75]\n"
  "ds_read_b128 v[32:35], v1 offset:24576\n"
  "v_mul_f32 v110, v110, v52\n"
  "v_mul_f32 v111, v111, v53\n"
  "v_cvt_pk_bf16_f32 v47, v110, v111\n"
  "v_mfma_f32_16x16x32_bf16 v[124:127], a[148:151], v[16:19], v[76:79]\n"
  "buffer_store_dwordx4 v[44:47], v7, s[36:39], s77 offen offset:0 sc1\n"
  "v_mul_f32 v52, v112, v112\n"
  "v_mul_f32 v53, v113, v113\n"
  "s_cmp_eq_u64 vcc, s[94:95]\n"
  "s_cselect_b32 s71, 1, 0\n"
  "s_cselect_b32 s78, s78, 0x80000000\n"
  "v_mfma_f32_16x16x32_bf16 v[120:123], a[152:155], v[20:23], v[120:123]\n"
  "ds_read_b128 v[36:39], v1 offset:25600\n"
  "v_fmamk_f32 v52, v52, 0xbdd2d3e8, v48\n"
  "v_fmamk_f32 v53, v53, 0xbdd2d3e8, v48\n"
  "v_mfma_f32_16x16x32_bf16 v[124:127], a[156:159], v[20:23], v[124:127]\n"
  "v_mul_f32 v52, v52, v112\n"
  "v_mul_f32 v53, v53, v113\n"
  "s_waitcnt lgkmcnt(2)\n"
  "v_mfma_f32_16x16x32_bf16 v[120:123], a[160:163], v[24:27], v[120:123]\n"
  "ds_read_b128 v[16:19], v1 offset:26624\n"
  "v_exp_f32 v52, v52\n"
  "v_mfma_f32_16x16x32_bf16 v[124:127], a[164:167], v[24:27], v[124:127]\n"
  "s_add_u32 m0, s79, 0\n"
  "s_add_u32 s76, s78, 0\n"
  "v_exp_f32 v53, v53\n"
  "v_mfma_f32_16x16x32_bf16 v[120:123], a[168:171], v[28:31], v[120:123]\n"
  "ds_read_b128 v[20:23], v1 offset:27648\n"
  "v_add_f32 v52, 1.0, v52\n"
  "v_add_f32 v53, 1.0, v53\n"
  "v_mfma_f32_16x16x32_bf16 v[124:127], a[172:175], v[28:31], v[124:127]\n"
  "buffer_load_dwordx4 v7, s[36:39], s76 offen sc1 lds\n"
  "v_rcp_f32 v52, v52\n"
  "s_waitcnt lgkmcnt(2)\n"
  "v_mfma_f32_16x16x32_bf16 v[120:123], a[176:179], v[32:35], v[120:123]\n"
  "ds_read_b128 v[24:27], v1 offset:28672\n"
  "v_rcp_f32 v53, v53\n"
  "v_mfma_f32_16x16x32_bf16 v[124:127], a[180:183], v[32:35], v[124:127]\n"
  "v_mul_f32 v112, v112, v52\n"
  "v_mul_f32 v113, v113, v53\n"
  "v_cvt_pk_bf16_f32 v40, v112, v113\n"
  "v_mfma_f32_16x16x32_bf16 v[120:123], a[184:187], v[36:39], v[120:123]\n"
  "ds_read_b128 v[28:31], v1 offset:29696\n"
  "v_mul_f32 v52, v114, v114\n"
  "v_mul_f32 v53, v115, v115\n"
  "v_mfma_f32_16x16x32_bf16 v[124:127], a[188:191], v[36:39], v[124:127]\n"
  "s_add_u32 m0, s79, 4096\n"
  "s_add_u32 s76, s78, 16384\n"
  "v_fmamk_f32 v52, v52, 0xbdd2d3e8, v48\n"
  "v_fmamk_f32 v53, v53, 0xbdd2d3e8, v48\n"
  "s_waitcnt lgkmcnt(2)\n"
  "v_mfma_f32_16x16x32_bf16 v[120:123], a[192:195], v[16:19], v[120:123]\n"
  "ds_read_b128 v[32:35], v1 offset:30720\n"
  "v_mul_f32 v52, v52, v114\n"
  "v_mul_f32 v53, v53, v115\n"
  "v_mfma_f32_16x16x32_bf16 v[124:127], a[196:199], v[16:19], v[124:127]\n"
  "buffer_load_dwordx4 v7, s[36:39], s76 offen sc1 lds\n"
  "v_exp_f32 v52, v52\n"
  "v_mfma_f32_16x16x32_bf16 v[120:123], a[200:203], v[20:23], v[120:123]\n"
  "ds_read_b128 v[36:39], v1 offset:31744\n"
  "v_exp_f32 v53, v53\n"
  "v_mfma_f32_16x16x32_bf16 v[124:127], a[204:207], v[20:23], v[124:127]\n"
  "v_add_f32 v52, 1.0, v52\n"
  "v_add_f32 v53, 1.0, v53\n"
  "s_waitcnt lgkmcnt(2)\n"
  "v_mfma_f32_16x16x32_bf16 v[120:123], a[208:211], v[24:27], v[120:123]\n"
  "ds_read_b128 v[16:19], v1 offset:32768\n"
  "v_rcp_f32 v52, v52\n"
  "v_mfma_f32_16x16x32_bf16 v[124:127], a[212:215], v[24:27], v[124:127]\n"
  "s_add_u32 m0, s79, 8192\n"
  "s_add_u32 s76, s78, 32768\n"
  "v_rcp_f32 v53, v53\n"
  "v_mfma_f32_16x16x32_bf16 v[120:123], a[216:219], v[28:31], v[120:123]\n"
  "ds_read_b128 v[20:23], v1 offset:33792\n"
  "v_mul_f32 v114, v114, v52\n"
  "v_mul_f32 v115, v115, v53\n"
  "v_cvt_pk_bf16_f32 v41, v114, v115\n"
  "v_mfma_f32_16x16x32_bf16 v[124:127], a[220:223], v[28:31], v[124:127]\n"
  "buffer_load_dwordx4 v7, s[36:39], s76 offen sc1 lds\n"
  "v_mul_f32 v52, v116, v116\n"
  "v_mul_f32 v53, v117, v117\n"
  "s_waitcnt lgkmcnt(2)\n"
  "v_mfma_f32_16x16x32_bf16 v[120:123], a[224:227], v[32:35], v[120:123]\n"
  "ds_read_b128 v[24:27], v1 offset:34816\n"
  "v_fmamk_f32 v52, v52, 0xbdd2d3e8, v48\n"
  "v_fmamk_f32 v53, v53, 0xbdd2d3e8, v48\n"
  "v_mfma_f32_16x16x32_bf16 v[124:127], a[228:231], v[32:35], v[124:127]\n"
  "v_mul_f32 v52, v52, v116\n"
  "v_mul_f32 v53, v53, v117\n"
  "v_mfma_f32_16x16x32_bf16 v[120:123], a[232:235], v[36:39], v[120:123]\n"
  "ds_read_b128 v[28:31], v1 offset:35840\n"
  "v_exp_f32 v52, v52\n"
  "v_mfma_f32_16x16x32_bf16 v[124:127], a[236:239], v[36:39], v[124:127]\n"
  "s_add_u32 m0, s79, 12288\n"
  "s_add_u32 s76, s78, 49152\n"
  "v_exp_f32 v53, v53\n"
  "s_waitcnt lgkmcnt(2)\n"
  "v_mfma_f32_16x16x32_bf16 v[120:123], a[240:243], v[16:19], v[120:123]\n"
  "ds_read_b128 v[32:35], v1 offset:18432\n"
  "v_add_f32 v52, 1.0, v52\n"
  "v_add_f32 v53, 1.0, v53\n"
  "v_mfma_f32_16x16x32_bf16 v[124:127], a[244:247], v[16:19], v[124:127]\n"
  "buffer_load_dwordx4 v7, s[36:39], s76 offen sc1 lds\n"
  "v_rcp_f32 v52, v52\n"
  "v_mfma_f32_16x16x32_bf16 v[120:123], a[248:251], v[20:23], v[120:123]\n"
  "ds_read_b128 v[36:39], v1 offset:19456\n"
  "v_rcp_f32 v53, v53\n"
  "v_mfma_f32_16x16x32_bf16 v[124:127], a[252:255], v[20:23], v[124:127]\n"
  "v_mul_f32 v116, v116, v52\n"
  "v_mul_f32 v117, v117, v53\n"
  "v_cvt_pk_bf16_f32 v42, v116, v117\n"
  "s_waitcnt lgkmcnt(2)\n"
  "v_mfma_f32_16x16x32_bf16 v[120:123], v[224:227], v[24:27], v[120:123]\n"
  "v_mul_f32 v52, v118, v118\n"
  "v_mul_f32 v53, v119, v119\n"
  "v_mfma_f32_16x16x32_bf16 v[124:127], v[228:231], v[24:27], v[124:127]\n"
  "s_add_u32 m0, s79, 16384\n"
  "s_add_u32 s76, s78, s56\n"
  "v_fmamk_f32 v52, v52, 0xbdd2d3e8, v48\n"
  "v_fmamk_f32 v53, v53, 0xbdd2d3e8, v48\n"
  "v_mfma_f32_16x16x32_bf16 v[120:123], v[232:235], v[28:31], v[120:123]\n"
  "v_mul_f32 v52, v52, v118\n"
  "v_mul_f32 v53, v53, v119\n"
  "v_mfma_f32_16x16x32_bf16 v[124:127], v[236:239], v[28:31], v[124:127]\n"
  "buffer_load_dwordx4 v7, s[36:39], s76 offen sc1 lds\n"
  "v_exp_f32 v52, v52\n"
  "s_waitcnt lgkmcnt(0)\n"
  "v_mfma_f32_16x16x32_bf16 v[120:123], v[240:243], v[32:35], v[120:123]\n"
  "v_exp_f32 v53, v53\n"
  "v_mfma_f32_16x16x32_bf16 v[124:127], v[244:247], v[32:35], v[124:127]\n"
  "v_add_f32 v52, 1.0, v52\n"
  "v_add_f32 v53, 1.0, v53\n"
  "v_mfma_f32_16x16x32_bf16 v[120:123], v[248:251], v[36:39], v[120:123]\n"
  "v_rcp_f32 v52, v52\n"
  "v_mfma_f32_16x16x32_bf16 v[124:127], v[252:255], v[36:39], v[124:127]\n"
  "buffer_load_dword v4, v2, s[36:39], 0 offen sc1\n"
  "v_rcp_f32 v53, v53\n"
  "v_mul_f32 v118, v118, v52\n"
  "v_mul_f32 v119, v119, v53\n"
  "v_cvt_pk_bf16_f32 v43, v118, v119\n"
  "buffer_store_dwordx4 v[40:43], v7, s[36:39], s80 offen offset:256 sc1\n"
  "s_nop 1\n"
  "v_pk_mul_f32 v[52:53], v[120:121], v[120:121]\n"
  "v_pk_mul_f32 v[54:55], v[122:123], v[122:123]\n"
  "v_pk_mul_f32 v[56:57], v[124:125], v[124:125]\n"
  "v_pk_mul_f32 v[58:59], v[126:127], v[126:127]\n"
  "v_pk_fma_f32 v[52:53], v[52:53], s[90:91], v[48:49] op_sel_hi:[1,0,0]\n"
  "v_pk_fma_f32 v[54:55], v[54:55], s[90:91], v[48:49] op_sel_hi:[1,0,0]\n"
  "v_pk_fma_f32 v[56:57], v[56:57], s[90:91], v[48:49] op_sel_hi:[1,0,0]\n"
  "v_pk_fma_f32 v[58:59], v[58:59], s[90:91], v[48:49] op_sel_hi:[1,0,0]\n"
  "v_pk_mul_f32 v[52:53], v[52:53], v[120:121]\n"
  "v_pk_mul_f32 v[54:55], v[54:55], v[122:123]\n"
  "v_pk_mul_f32 v[56:57], v[56:57], v[124:125]\n"
  "v_pk_mul_f32 v[58:59], v[58:59], v[126:127]\n"
  "v_exp_f32 v52, v52\n"
  "v_exp_f32 v53, v53\n"
  "v_exp_f32 v54, v54\n"
  "v_exp_f32 v55, v55\n"
  "v_exp_f32 v56, v56\n"
  "v_exp_f32 v57, v57\n"
  "v_exp_f32 v58, v58\n"
  "v_exp_f32 v59, v59\n"
  "v_pk_add_f32 v[52:53], v[52:53], 1.0 op_sel_hi:[1,0]\n"
  "v_pk_add_f32 v[54:55], v[54:55], 1.0 op_sel_hi:[1,0]\n"
  "v_pk_add_f32 v[56:57], v[56:57], 1.0 op_sel_hi:[1,0]\n"
  "v_pk_add_f32 v[58:59], v[58:59], 1.0 op_sel_hi:[1,0]\n"
  "v_rcp_f32 v52, v52\n"
  "v_rcp_f32 v53, v53\n"
  "v_rcp_f32 v54, v54\n"
  "v_rcp_f32 v55, v55\n"
  "v_rcp_f32 v56, v56\n"
  "v_rcp_f32 v57, v57\n"
  "v_rcp_f32 v58, v58\n"
  "v_rcp_f32 v59, v59\n"
  "v_pk_mul_f32 v[120:121], v[120:121], v[52:53]\n"
  "v_pk_mul_f32 v[122:123], v[122:123], v[54:55]\n"
  "v_pk_mul_f32 v[124:125], v[124:125], v[56:57]\n"
  "v_pk_mul_f32 v[126:127], v[126:127], v[58:59]\n"
  "v_cvt_pk_bf16_f32 v44, v120, v121\n"
  "v_cvt_pk_bf16_f32 v45, v122, v123\n"
  "v_cvt_pk_bf16_f32 v46, v124, v125\n"
  "v_cvt_pk_bf16_f32 v47, v126, v127\n"
  "buffer_store_dwordx4 v[44:47], v7, s[36:39], s77 offen offset:256 sc1\n"
  "s_xor_b32 s81, s81, 65536\n"
  "v_xor_b32 v1, 65536, v1\n"
  "s_add_u32 s70, s70, 1\n"
  "s_mov_b32 s82, s71\n"
  "s_cmp_lg_u32 s71, 0\n"
  "s_cbranch_scc1 L_pf_%=\n"
  "s_waitcnt vmcnt(2)\n"
  "v_subrev_u32 v6, v3, v4\n"
  "s_add_u32 s74, s70, 0\n"
  "v_cmp_le_i32 vcc, s74, v6\n"
  "s_cmp_eq_u64 vcc, exec\n"
  "s_cbranch_scc0 L_idle_%=\n"
  "s_cmp_eq_u32 s70, 64\n"
  "s_cbranch_scc1 L_idle_%=\n"
  "s_add_u32 s78, s70, 0\n"
  "s_and_b32 s78, s78, s61\n"
  "s_mul_i32 s78, s78, s60\n"
  "s_lshr_b32 s76, s78, 8\n"
  "s_and_b32 s76, s76, 8192\n"
  "s_add_u32 s78, s78, s59\n"
  "s_xor_b32 s78, s78, s76\n"
  "s_add_u32 s79, s81, s57\n"
  "s_add_u32 m0, s79, 0\n"
  "s_add_u32 s76, s78, 0\n"
  "buffer_load_dwordx4 v7, s[36:39], s76 offen sc1 lds\n"
  "s_add_u32 m0, s79, 4096\n"
  "s_add_u32 s76, s78, 16384\n"
  "buffer_load_dwordx4 v7, s[36:39], s76 offen sc1 lds\n"
  "s_add_u32 m0, s79, 8192\n"
  "s_add_u32 s76, s78, 32768\n"
  "buffer_load_dwordx4 v7, s[36:39], s76 offen sc1 lds\n"
  "s_add_u32 m0, s79, 12288\n"
  "s_add_u32 s76, s78, 49152\n"
  "buffer_load_dwordx4 v7, s[36:39], s76 offen sc1 lds\n"
  "s_add_u32 m0, s79, 16384\n"
  "s_add_u32 s76, s78, s56\n"
  "buffer_load_dwordx4 v7, s[36:39], s76 offen sc1 lds\n"
  "s_waitcnt vmcnt(5)\n"
  "s_add_u32 s75, s70, -1\n"
  "s_lshl_b32 s74, s75, 2\n"
  "s_add_u32 s74, s74, 163584\n"
  "v_mov_b32 v16, s74\n"
  "v_mov_b32 v6, 1\n"
  "s_mov_b64 exec, 1\n"
  "ds_add_rtn_u32 v16, v16, v6\n"
  "s_mov_b64 exec, -1\n"
  "v_mov_b32 v6, s75\n"
  "s_waitcnt lgkmcnt(0)\n"
  "v_readfirstlane_b32 s74, v16\n"
  "s_cmp_eq_u32 s74, 3\n"
  "s_cselect_b32 s74, s58, 0x80000000\n"
  "buffer_store_dword v6, v5, s[36:39], s74 offen sc1\n"
  "s_waitcnt vmcnt(3)\n"
  "s_branch L_go_%=\n"
  "L_idle_%=:\n"
  "buffer_load_dword v4, v2, s[36:39], 0 offen sc1\n"
  "s_waitcnt vmcnt(1)\n"
  "s_add_u32 s75, s70, -1\n"
  "s_lshl_b32 s74, s75, 2\n"
  "s_add_u32 s74, s74, 163584\n"
  "v_mov_b32 v16, s74\n"
  "v_mov_b32 v6, 1\n"
  "s_mov_b64 exec, 1\n"
  "ds_add_rtn_u32 v16, v16, v6\n"
  "s_mov_b64 exec, -1\n"
  "v_mov_b32 v6, s75\n"
  "s_waitcnt lgkmcnt(0)\n"
  "v_readfirstlane_b32 s74, v16\n"
  "s_cmp_eq_u32 s74, 3\n"
  "s_cselect_b32 s74, s58, 0x80000000\n"
  "buffer_store_dword v6, v5, s[36:39], s74 offen sc1\n"
  "s_waitcnt vmcnt(1)\n"
  "s_cmp_eq_u32 s70, 64\n"
  "s_cbranch_scc1 L_fin_%=\n"
  "v_subrev_u32 v6, v3, v4\n"
  "s_add_u32 s74, s70, 0\n"
  "v_cmp_le_i32 vcc, s74, v6\n"
  "s_cmp_eq_u64 vcc, exec\n"
  "s_cbranch_scc1 L_poll_x_%=\n"
  "L_first_%=:\n"
  "s_cmp_lg_u32 s72, 0\n"
  "s_cselect_b32 s73, 0, 32768\n"
  "v_add_u32 v6, s70, v3\n"
  "L_poll_%=:\n"
  "buffer_load_dword v4, v2, s[36:39], 0 offen sc1\n"
  "s_sub_u32 s73, s73, 1\n"
  "s_cselect_b32 s72, 1, s72\n"
  "s_cselect_b64 s[2:3], 0, -1\n"
  "s_waitcnt vmcnt(0)\n"
  "v_cmp_gt_i32 vcc, v6, v4\n"
  "s_and_b64 vcc, vcc, s[2:3]\n"
  "s_cbranch_scc1 L_poll_%=\n"
  "L_poll_x_%=:\n"
  "s_add_u32 s78, s70, 0\n"
  "s_and_b32 s78, s78, s61\n"
  "s_mul_i32 s78, s78, s60\n"
  "s_lshr_b32 s76, s78, 8\n"
  "s_and_b32 s76, s76, 8192\n"
  "s_add_u32 s78, s78, s59\n"
  "s_xor_b32 s78, s78, s76\n"
  "s_add_u32 s79, s81, s57\n"
  "s_add_u32 m0, s79, 0\n"
  "s_add_u32 s76, s78, 0\n"
  "buffer_load_dwordx4 v7, s[36:39], s76 offen sc1 lds\n"
  "s_add_u32 m0, s79, 4096\n"
  "s_add_u32 s76, s78, 16384\n"
  "buffer_load_dwordx4 v7, s[36:39], s76 offen sc1 lds\n"
  "s_add_u32 m0, s79, 8192\n"
  "s_add_u32 s76, s78, 32768\n"
  "buffer_load_dwordx4 v7, s[36:39], s76 offen sc1 lds\n"
  "s_add_u32 m0, s79, 12288\n"
  "s_add_u32 s76, s78, 49152\n"
  "buffer_load_dwordx4 v7, s[36:39], s76 offen sc1 lds\n"
  "s_add_u32 m0, s79, 16384\n"
  "s_add_u32 s76, s78, s56\n"
  "buffer_load_dwordx4 v7, s[36:39], s76 offen sc1 lds\n"
  "s_waitcnt vmcnt(2)\n"
  "s_branch L_go_%=\n"
  "L_bn_%=:\n"
  "s_waitcnt vmcnt(3)\n"
  "s_branch L_bm_%=\n"
  "L_fin_%=:\n"
  "s_waitcnt vmcnt(0)\n"
  "L_c1_%=:\n"
  "s_endpgm\n"
      :
      : "{v0}"(ln * 16u), "{v1}"(ln * 16u), "{v2}"(ln < 4 ? c.flag(F_R1 + c.lane) : OOB), "{v3}"(ln < 4 ? 0 : NEVER),
        "{v7}"(a.ab), "{v8}"(a.voff), "{v9}"(a.ra), "{v[64:79]}"(bias), "{s[40:43]}"(mkrsrc(p.w1, 576 * 4608 * 2)), COMMON
      : OWNED);
}
#define L_OPERANDS                                                                                                                   \
  "{v0}"(ln * 16u), "{v1}"(g * 512u + t * 16u), "{v7}"(TR(lpart + (24u * j + 4u * w + g) * 512u) + t * 16u),                        \
  "{v8}"((row0 + 4u * w + q) * 1152u + g * 64u + (ln & 3u) * 16u + 384u * j),                                                         \
  "{v9}"((row0 + 8u * w + (ln >> 3)) * 1152u + (ln & 7u) * 16u + 384u * j + 256u),                                                    \
  "{v10}"(g * 2048u + w * 256u + q * 64u + 16u * (ln & 3u)), "{v11}"(8192u + g * 1024u + q * 128u + w * 32u + 8u * (ln & 3u)),       \
  "{v12}"(TR(lpart + (24u * j + 16u + 2u * w + (g >> 1)) * 512u) + (g & 1u) * 256u + t * 16u), COMMON
DI void role_o(Ctx& c, const Params& p, unsigned i, unsigned j) {
  const unsigned ln = (unsigned)c.lane, w = (unsigned)c.wave, g = ln >> 4, t = ln & 15u, q = (ln >> 2) & 3u;
  const unsigned row0 = 1024u * i, lpart = L_PO + i * (40 * KB);
  const unsigned fword = c.flag(F_O + 3 * i + j);
  const unsigned srcb = c.slab0(0), srcm = 4 * MB, srck = RING - 1, dstb = c.slab1(0);
  asm volatile(
  "s_getpc_b64 s[0:1]\n"
  "L_c0_%=:\n"
  "v_mbcnt_lo_u32_b32 v5, -1, 0\n"
  "v_mbcnt_hi_u32_b32 v5, -1, v5\n"
  "v_lshlrev_b32 v5, 4, v5\n"
  "v_add_lshl_u32 v5, v5, s57, 2\n"
  "v_min_u32 v5, L_c1_%=-L_c0_%=, v5\n"
  "global_load_dword v5, v5, s[0:1]\n"
  "s_nop 4\n"
  "s_add_u32 m0, s57, 0\n"
  "s_mov_b32 s74, 0\n"
  "buffer_load_dwordx4 v8, s[40:43], s74 offen lds\n"
  "s_add_u32 m0, s57, 4096\n"
  "s_mov_b32 s74, 18432\n"
  "buffer_load_dwordx4 v8, s[40:43], s74 offen lds\n"
  "s_add_u32 m0, s57, 8192\n"
  "s_mov_b32 s74, 0\n"
  "buffer_load_dwordx4 v9, s[40:43], s74 offen lds\n"
  "s_add_u32 m0, s57, 12288\n"
  "s_mov_b32 s74, 36864\n"
  "buffer_load_dwordx4 v8, s[40:43], s74 offen lds\n"
  "s_add_u32 m0, s57, 16384\n"
  "s_mov_b32 s74, 55296\n"
  "buffer_load_dwordx4 v8, s[40:43], s74 offen lds\n"
  "s_add_u32 m0, s57, 20480\n"
  "s_mov_b32 s74, 36864\n"
  "buffer_load_dwordx4 v9, s[40:43], s74 offen lds\n"
  "s_add_u32 m0, s57, 24576\n"
  "s_mov_b32 s74, 73728\n"
  "buffer_load_dwordx4 v8, s[40:43], s74 offen lds\n"
  "s_add_u32 m0, s57, 28672\n"
  "s_mov_b32 s74, 92160\n"
  "buffer_load_dwordx4 v8, s[40:43], s74 offen lds\n"
  "s_add_u32 m0, s57, 32768\n"
  "s_mov_b32 s74, 73728\n"
  "buffer_load_dwordx4 v9, s[40:43], s74 offen lds\n"
  "s_add_u32 m0, s57, 36864\n"
  "s_mov_b32 s74, 110592\n"
  "buffer_load_dwordx4 v8, s[40:43], s74 offen lds\n"
  "s_add_u32 m0, s57, 40960\n"
  "s_mov_b32 s74, 129024\n"
  "buffer_load_dwordx4 v8, s[40:43], s74 offen lds\n"
  "s_add_u32 m0, s57, 45056\n"
  "s_mov_b32 s74, 110592\n"
  "buffer_load_dwordx4 v9, s[40:43], s74 offen lds\n"
  "s_add_u32 m0, s57, 49152\n"
  "s_mov_b32 s74, 147456\n"
  "buffer_load_dwordx4 v8, s[40:43], s74 offen lds\n"
  "s_add_u32 m0, s57, 53248\n"
  "s_mov_b32 s74, 165888\n"
  "buffer_load_dwordx4 v8, s[40:43], s74 offen lds\n"
  "s_add_u32 m0, s57, 57344\n"
  "s_mov_b32 s74, 147456\n"
  "buffer_load_dwordx4 v9, s[40:43], s74 offen lds\n"
  "s_add_u32 m0, s57, 61440\n"
  "s_mov_b32 s74, 184320\n"
  "buffer_load_dwordx4 v8, s[40:43], s74 offen lds\n"
  "s_add_u32 m0, s57, 65536\n"
  "s_mov_b32 s74, 202752\n"
  "buffer_load_dwordx4 v8, s[40:43], s74 offen lds\n"
  "s_add_u32 m0, s57, 69632\n"
  "s_mov_b32 s74, 184320\n"
  "buffer_load_dwordx4 v9, s[40:43], s74 offen lds\n"
  "s_add_u32 m0, s57, 73728\n"
  "s_mov_b32 s74, 221184\n"
  "buffer_load_dwordx4 v8, s[40:43], s74 offen lds\n"
  "s_add_u32 m0, s57, 77824\n"
  "s_mov_b32 s74, 239616\n"
  "buffer_load_dwordx4 v8, s[40:43], s74 offen lds\n"
  "s_add_u32 m0, s57, 81920\n"
  "s_mov_b32 s74, 221184\n"
  "buffer_load_dwordx4 v9, s[40:43], s74 offen lds\n"
  "s_waitcnt vmcnt(18) lgkmcnt(0)\n"
  "s_barrier\n"
  "s_add_u32 m0, s57, 86016\n"
  "s_mov_b32 s74, 258048\n"
  "buffer_load_dwordx4 v8, s[40:43], s74 offen lds\n"
  "s_add_u32 m0, s57, 90112\n"
  "s_mov_b32 s74, 276480\n"
  "buffer_load_dwordx4 v8, s[40:43], s74 offen lds\n"
  "s_add_u32 m0, s57, 94208\n"
  "s_mov_b32 s74, 258048\n"
  "buffer_load_dwordx4 v9, s[40:43], s74 offen lds\n"
  "v_add_u32 v4, 0, v10\n"
  "v_add_u32 v6, 0, v11\n"
  "ds_read_b64_tr_b16 a[0:1], v4 offset:0\n"
  "ds_read_b64_tr_b16 a[2:3], v4 offset:1024\n"
  "ds_read_b64_tr_b16 a[4:5], v4 offset:8\n"
  "ds_read_b64_tr_b16 a[6:7], v4 offset:1032\n"
  "ds_read_b64_tr_b16 a[8:9], v6 offset:0\n"
  "ds_read_b64_tr_b16 a[10:11], v6 offset:512\n"
  "s_waitcnt vmcnt(18) lgkmcnt(0)\n"
  "s_barrier\n"
  "s_add_u32 m0, s57, 0\n"
  "s_mov_b32 s74, 294912\n"
  "buffer_load_dwordx4 v8, s[40:43], s74 offen lds\n"
  "s_add_u32 m0, s57, 4096\n"
  "s_mov_b32 s74, 313344\n"
  "buffer_load_dwordx4 v8, s[40:43], s74 offen lds\n"
  "s_add_u32 m0, s57, 8192\n"
  "s_mov_b32 s74, 294912\n"
  "buffer_load_dwordx4 v9, s[40:43], s74 offen lds\n"
  "v_add_u32 v4, 12288, v10\n"
  "v_add_u32 v6, 12288, v11\n"
  "ds_read_b64_tr_b16 a[12:13], v4 offset:0\n"
  "ds_read_b64_tr_b16 a[14:15], v4 offset:1024\n"
  "ds_read_b64_tr_b16 a[16:17], v4 offset:8\n"
  "ds_read_b64_tr_b16 a[18:19], v4 offset:1032\n"
  "ds_read_b64_tr_b16 a[20:21], v6 offset:0\n"
  "ds_read_b64_tr_b16 a[22:23], v6 offset:512\n"
  "s_waitcnt vmcnt(18) lgkmcnt(0)\n"
  "s_barrier\n"
  "s_add_u32 m0, s57, 12288\n"
  "s_mov_b32 s74, 331776\n"
  "buffer_load_dwordx4 v8, s[40:43], s74 offen lds\n"
  "s_add_u32 m0, s57, 16384\n"
  "s_mov_b32 s74, 350208\n"
  "buffer_load_dwordx4 v8, s[40:43], s74 offen lds\n"
  "s_add_u32 m0, s57, 20480\n"
  "s_mov_b32 s74, 331776\n"
  "buffer_load_dwordx4 v9, s[40:43], s74 offen lds\n"
  "v_add_u32 v4, 24576, v10\n"
  "v_add_u32 v6, 24576, v11\n"
  "ds_read_b64_tr_b16 a[24:25], v4 offset:0\n"
  "ds_read_b64_tr_b16 a[26:27], v4 offset:1024\n"
  "ds_read_b64_tr_b16 a[28:29], v4 offset:8\n"
  "ds_read_b64_tr_b16 a[30:31], v4 offset:1032\n"
  "ds_read_b64_tr_b16 a[32:33], v6 offset:0\n"
  "ds_read_b64_tr_b16 a[34:35], v6 offset:512\n"
  "s_waitcnt vmcnt(18) lgkmcnt(0)\n"
  "s_barrier\n"
  "s_add_u32 m0, s57, 24576\n"
  "s_mov_b32 s74, 368640\n"
  "buffer_load_dwordx4 v8, s[40:43], s74 offen lds\n"
  "s_add_u32 m0, s57, 28672\n"
  "s_mov_b32 s74, 387072\n"
  "buffer_load_dwordx4 v8, s[40:43], s74 offen lds\n"
  "s_add_u32 m0, s57, 32768\n"
  "s_mov_b32 s74, 368640\n"
  "buffer_load_dwordx4 v9, s[40:43], s74 offen lds\n"
  "v_add_u32 v4, 36864, v10\n"
  "v_add_u32 v6, 36864, v11\n"
  "ds_read_b64_tr_b16 a[36:37], v4 offset:0\n"
  "ds_read_b64_tr_b16 a[38:39], v4 offset:1024\n"
  "ds_read_b64_tr_b16 a[40:41], v4 offset:8\n"
  "ds_read_b64_tr_b16 a[42:43], v4 offset:1032\n"
  "ds_read_b64_tr_b16 a[44:45], v6 offset:0\n"
  "ds_read_b64_tr_b16 a[46:47], v6 offset:512\n"
  "s_waitcnt vmcnt(18) lgkmcnt(0)\n"
  "s_barrier\n"
  "s_add_u32 m0, s57, 36864\n"
  "s_mov_b32 s74, 405504\n"
  "buffer_load_dwordx4 v8, s[40:43], s74 offen lds\n"
  "s_add_u32 m0, s57, 40960\n"
  "s_mov_b32 s74, 423936\n"
  "buffer_load_dwordx4 v8, s[40:43], s74 offen lds\n"
  "s_add_u32 m0, s57, 45056\n"
  "s_mov_b32 s74, 405504\n"
  "buffer_load_dwordx4 v9, s[40:43], s74 offen lds\n"
  "v_add_u32 v4, 49152, v10\n"
  "v_add_u32 v6, 49152, v11\n"
  "ds_read_b64_tr_b16 a[48:49], v4 offset:0\n"
  "ds_read_b64_tr_b16 a[50:51], v4 offset:1024\n"
  "ds_read_b64_tr_b16 a[52:53], v4 offset:8\n"
  "ds_read_b64_tr_b16 a[54:55], v4 offset:1032\n"
  "ds_read_b64_tr_b16 a[56:57], v6 offset:0\n"
  "ds_read_b64_tr_b16 a[58:59], v6 offset:512\n"
  "s_waitcnt vmcnt(18) lgkmcnt(0)\n"
  "s_barrier\n"
  "s_add_u32 m0, s57, 49152\n"
  "s_mov_b32 s74, 442368\n"
  "buffer_load_dwordx4 v8, s[40:43], s74 offen lds\n"
  "s_add_u32 m0, s57, 53248\n"
  "s_mov_b32 s74, 460800\n"
  "buffer_load_dwordx4 v8, s[40:43], s74 offen lds\n"
  "s_add_u32 m0, s57, 57344\n"
  "s_mov_b32 s74, 442368\n"
  "buffer_load_dwordx4 v9, s[40:43], s74 offen lds\n"
  "v_add_u32 v4, 61440, v10\n"
  "v_add_u32 v6, 61440, v11\n"
  "ds_read_b64_tr_b16 a[60:61], v4 offset:0\n"
  "ds_read_b64_tr_b16 a[62:63], v4 offset:1024\n"
  "ds_read_b64_tr_b16 a[64:65], v4 offset:8\n"
  "ds_read_b64_tr_b16 a[66:67], v4 offset:1032\n"
  "ds_read_b64_tr_b16 a[68:69], v6 offset:0\n"
  "ds_read_b64_tr_b16 a[70:71], v6 offset:512\n"
  "s_waitcnt vmcnt(18) lgkmcnt(0)\n"
  "s_barrier\n"
  "s_add_u32 m0, s57, 61440\n"
  "s_mov_b32 s74, 479232\n"
  "buffer_load_dwordx4 v8, s[40:43], s74 offen lds\n"
  "s_add_u32 m0, s57, 65536\n"
  "s_mov_b32 s74, 497664\n"
  "buffer_load_dwordx4 v8, s[40:43], s74 offen lds\n"
  "s_add_u32 m0, s57, 69632\n"
  "s_mov_b32 s74, 479232\n"
  "buffer_load_dwordx4 v9, s[40:43], s74 offen lds\n"
  "v_add_u32 v4, 73728, v10\n"
  "v_add_u32 v6, 73728, v11\n"
  "ds_read_b64_tr_b16 a[72:73], v4 offset:0\n"
  "ds_read_b64_tr_b16 a[74:75], v4 offset:1024\n"
  "ds_read_b64_tr_b16 a[76:77], v4 offset:8\n"
  "ds_read_b64_tr_b16 a[78:79], v4 offset:1032\n"
  "ds_read_b64_tr_b16 a[80:81], v6 offset:0\n"
  "ds_read_b64_tr_b16 a[82:83], v6 offset:512\n"
  "s_waitcnt vmcnt(18) lgkmcnt(0)\n"
  "s_barrier\n"
  "s_add_u32 m0, s57, 73728\n"
  "s_mov_b32 s74, 516096\n"
  "buffer_load_dwordx4 v8, s[40:43], s74 offen lds\n"
  "s_add_u32 m0, s57, 77824\n"
  "s_mov_b32 s74, 534528\n"
  "buffer_load_dwordx4 v8, s[40:43], s74 offen lds\n"
  "s_add_u32 m0, s57, 81920\n"
  "s_mov_b32 s74, 516096\n"
  "buffer_load_dwordx4 v9, s[40:43], s74 offen lds\n"
  "v_add_u32 v4, 86016, v10\n"
  "v_add_u32 v6, 86016, v11\n"
  "ds_read_b64_tr_b16 a[84:85], v4 offset:0\n"
  "ds_read_b64_tr_b16 a[86:87], v4 offset:1024\n"
  "ds_read_b64_tr_b16 a[88:89], v4 offset:8\n"
  "ds_read_b64_tr_b16 a[90:91], v4 offset:1032\n"
  "ds_read_b64_tr_b16 a[92:93], v6 offset:0\n"
  "ds_read_b64_tr_b16 a[94:95], v6 offset:512\n"
  "s_waitcnt vmcnt(18) lgkmcnt(0)\n"
  "s_barrier\n"
  "s_add_u32 m0, s57, 86016\n"
  "s_mov_b32 s74, 552960\n"
  "buffer_load_dwordx4 v8, s[40:43], s74 offen lds\n"
  "s_add_u32 m0, s57, 90112\n"
  "s_mov_b32 s74, 571392\n"
  "buffer_load_dwordx4 v8, s[40:43], s74 offen lds\n"
  "s_add_u32 m0, s57, 94208\n"
  "s_mov_b32 s74, 552960\n"
  "buffer_load_dwordx4 v9, s[40:43], s74 offen lds\n"
  "v_add_u32 v4, 0, v10\n"
  "v_add_u32 v6, 0, v11\n"
  "ds_read_b64_tr_b16 a[96:97], v4 offset:0\n"
  "ds_read_b64_tr_b16 a[98:99], v4 offset:1024\n"
  "ds_read_b64_tr_b16 a[100:101], v4 offset:8\n"
  "ds_read_b64_tr_b16 a[102:103], v4 offset:1032\n"
  "ds_read_b64_tr_b16 a[104:105], v6 offset:0\n"
  "ds_read_b64_tr_b16 a[106:107], v6 offset:512\n"
  "s_waitcnt vmcnt(18) lgkmcnt(0)\n"
  "s_barrier\n"
  "s_add_u32 m0, s57, 0\n"
  "s_mov_b32 s74, 589824\n"
  "buffer_load_dwordx4 v8, s[40:43], s74 offen lds\n"
  "s_add_u32 m0, s57, 4096\n"
  "s_mov_b32 s74, 608256\n"
  "buffer_load_dwordx4 v8, s[40:43], s74 offen lds\n"
  "s_add_u32 m0, s57, 8192\n"
  "s_mov_b32 s74, 589824\n"
  "buffer_load_dwordx4 v9, s[40:43], s74 offen lds\n"
  "v_add_u32 v4, 12288, v10\n"
  "v_add_u32 v6, 12288, v11\n"
  "ds_read_b64_tr_b16 a[108:109], v4 offset:0\n"
  "ds_read_b64_tr_b16 a[110:111], v4 offset:1024\n"
  "ds_read_b64_tr_b16 a[112:113], v4 offset:8\n"
  "ds_read_b64_tr_b16 a[114:115], v4 offset:1032\n"
  "ds_read_b64_tr_b16 a[116:117], v6 offset:0\n"
  "ds_read_b64_tr_b16 a[118:119], v6 offset:512\n"
  "s_waitcnt vmcnt(18) lgkmcnt(0)\n"
  "s_barrier\n"
  "s_add_u32 m0, s57, 12288\n"
  "s_mov_b32 s74, 626688\n"
  "buffer_load_dwordx4 v8, s[40:43], s74 offen lds\n"
  "s_add_u32 m0, s57, 16384\n"
  "s_mov_b32 s74, 645120\n"
  "buffer_load_dwordx4 v8, s[40:43], s74 offen lds\n"
  "s_add_u32 m0, s57, 20480\n"
  "s_mov_b32 s74, 626688\n"
  "buffer_load_dwordx4 v9, s[40:43], s74 offen lds\n"
  "v_add_u32 v4, 24576, v10\n"
  "v_add_u32 v6, 24576, v11\n"
  "ds_read_b64_tr_b16 a[120:121], v4 offset:0\n"
  "ds_read_b64_tr_b16 a[122:123], v4 offset:1024\n"
  "ds_read_b64_tr_b16 a[124:125], v4 offset:8\n"
  "ds_read_b64_tr_b16 a[126:127], v4 offset:1032\n"
  "ds_read_b64_tr_b16 a[128:129], v6 offset:0\n"
  "ds_read_b64_tr_b16 a[130:131], v6 offset:512\n"
  "s_waitcnt vmcnt(18) lgkmcnt(0)\n"
  "s_barrier\n"
  "s_add_u32 m0, s57, 24576\n"
  "s_mov_b32 s74, 663552\n"
  "buffer_load_dwordx4 v8, s[40:43], s74 offen lds\n"
  "s_add_u32 m0, s57, 28672\n"
  "s_mov_b32 s74, 681984\n"
  "buffer_load_dwordx4 v8, s[40:43], s74 offen lds\n"
  "s_add_u32 m0, s57, 32768\n"
  "s_mov_b32 s74, 663552\n"
  "buffer_load_dwordx4 v9, s[40:43], s74 offen lds\n"
  "v_add_u32 v4, 36864, v10\n"
  "v_add_u32 v6, 36864, v11\n"
  "ds_read_b64_tr_b16 a[132:133], v4 offset:0\n"
  "ds_read_b64_tr_b16 a[134:135], v4 offset:1024\n"
  "ds_read_b64_tr_b16 a[136:137], v4 offset:8\n"
  "ds_read_b64_tr_b16 a[138:139], v4 offset:1032\n"
  "ds_read_b64_tr_b16 a[140:141], v6 offset:0\n"
  "ds_read_b64_tr_b16 a[142:143], v6 offset:512\n"
  "s_waitcnt vmcnt(18) lgkmcnt(0)\n"
  "s_barrier\n"
  "s_add_u32 m0, s57, 36864\n"
  "s_mov_b32 s74, 700416\n"
  "buffer_load_dwordx4 v8, s[40:43], s74 offen lds\n"
  "s_add_u32 m0, s57, 40960\n"
  "s_mov_b32 s74, 718848\n"
  "buffer_load_dwordx4 v8, s[40:43], s74 offen lds\n"
  "s_add_u32 m0, s57, 45056\n"
  "s_mov_b32 s74, 700416\n"
  "buffer_load_dwordx4 v9, s[40:43], s74 offen lds\n"
  "v_add_u32 v4, 49152, v10\n"
  "v_add_u32 v6, 49152, v11\n"
  "ds_read_b64_tr_b16 a[144:145], v4 offset:0\n"
  "ds_read_b64_tr_b16 a[146:147], v4 offset:1024\n"
  "ds_read_b64_tr_b16 a[148:149], v4 offset:8\n"
  "ds_read_b64_tr_b16 a[150:151], v4 offset:1032\n"
  "ds_read_b64_tr_b16 a[152:153], v6 offset:0\n"
  "ds_read_b64_tr_b16 a[154:155], v6 offset:512\n"
  "s_waitcnt vmcnt(18) lgkmcnt(0)\n"
  "s_barrier\n"
  "s_add_u32 m0, s57, 49152\n"
  "s_mov_b32 s74, 737280\n"
  "buffer_load_dwordx4 v8, s[40:43], s74 offen lds\n"
  "s_add_u32 m0, s57, 53248\n"
  "s_mov_b32 s74, 755712\n"
  "buffer_load_dwordx4 v8, s[40:43], s74 offen lds\n"
  "s_add_u32 m0, s57, 57344\n"
  "s_mov_b32 s74, 737280\n"
  "buffer_load_dwordx4 v9, s[40:43], s74 offen lds\n"
  "v_add_u32 v4, 61440, v10\n"
  "v_add_u32 v6, 61440, v11\n"
  "ds_read_b64_tr_b16 a[156:157], v4 offset:0\n"
  "ds_read_b64_tr_b16 a[158:159], v4 offset:1024\n"
  "ds_read_b64_tr_b16 a[160:161], v4 offset:8\n"
  "ds_read_b64_tr_b16 a[162:163], v4 offset:1032\n"
  "ds_read_b64_tr_b16 a[164:165], v6 offset:0\n"
  "ds_read_b64_tr_b16 a[166:167], v6 offset:512\n"
  "s_waitcnt vmcnt(18) lgkmcnt(0)\n"
  "s_barrier\n"
  "s_add_u32 m0, s57, 61440\n"
  "s_mov_b32 s74, 774144\n"
  "buffer_load_dwordx4 v8, s[40:43], s74 offen lds\n"
  "s_add_u32 m0, s57, 65536\n"
  "s_mov_b32 s74, 792576\n"
  "buffer_load_dwordx4 v8, s[40:43], s74 offen lds\n"
  "s_add_u32 m0, s57, 69632\n"
  "s_mov_b32 s74, 774144\n"
  "buffer_load_dwordx4 v9, s[40:43], s74 offen lds\n"
  "v_add_u32 v4, 73728, v10\n"
  "v_add_u32 v6, 73728, v11\n"
  "ds_read_b64_tr_b16 a[168:169], v4 offset:0\n"
  "ds_read_b64_tr_b16 a[170:171], v4 offset:1024\n"
  "ds_read_b64_tr_b16 a[172:173], v4 offset:8\n"
  "ds_read_b64_tr_b16 a[174:175], v4 offset:1032\n"
  "ds_read_b64_tr_b16 a[176:177], v6 offset:0\n"
  "ds_read_b64_tr_b16 a[178:179], v6 offset:512\n"
  "s_waitcnt vmcnt(18) lgkmcnt(0)\n"
  "s_barrier\n"
  "s_add_u32 m0, s57, 73728\n"
  "s_mov_b32 s74, 811008\n"
  "buffer_load_dwordx4 v8, s[40:43], s74 offen lds\n"
  "s_add_u32 m0, s57, 77824\n"
  "s_mov_b32 s74, 829440\n"
  "buffer_load_dwordx4 v8, s[40:43], s74 offen lds\n"
  "s_add_u32 m0, s57, 81920\n"
  "s_mov_b32 s74, 811008\n"
  "buffer_load_dwordx4 v9, s[40:43], s74 offen lds\n"
  "v_add_u32 v4, 86016, v10\n"
  "v_add_u32 v6, 86016, v11\n"
  "ds_read_b64_tr_b16 a[180:181], v4 offset:0\n"
  "ds_read_b64_tr_b16 a[182:183], v4 offset:1024\n"
  "ds_read_b64_tr_b16 a[184:185], v4 offset:8\n"
  "ds_read_b64_tr_b16 a[186:187], v4 offset:1032\n"
  "ds_read_b64_tr_b16 a[188:189], v6 offset:0\n"
  "ds_read_b64_tr_b16 a[190:191], v6 offset:512\n"
  "s_waitcnt vmcnt(18) lgkmcnt(0)\n"
  "s_barrier\n"
  "s_add_u32 m0, s57, 86016\n"
  "s_mov_b32 s74, 847872\n"
  "buffer_load_dwordx4 v8, s[40:43], s74 offen lds\n"
  "s_add_u32 m0, s57, 90112\n"
  "s_mov_b32 s74, 866304\n"
  "buffer_load_dwordx4 v8, s[40:43], s74 offen lds\n"
  "s_add_u32 m0, s57, 94208\n"
  "s_mov_b32 s74, 847872\n"
  "buffer_load_dwordx4 v9, s[40:43], s74 offen lds\n"
  "v_add_u32 v4, 0, v10\n"
  "v_add_u32 v6, 0, v11\n"
  "ds_read_b64_tr_b16 a[192:193], v4 offset:0\n"
  "ds_read_b64_tr_b16 a[194:195], v4 offset:1024\n"
  "ds_read_b64_tr_b16 a[196:197], v4 offset:8\n"
  "ds_read_b64_tr_b16 a[198:199], v4 offset:1032\n"
  "ds_read_b64_tr_b16 a[200:201], v6 offset:0\n"
  "ds_read_b64_tr_b16 a[202:203], v6 offset:512\n"
  "s_waitcnt vmcnt(18) lgkmcnt(0)\n"
  "s_barrier\n"
  "s_add_u32 m0, s57, 0\n"
  "s_mov_b32 s74, 884736\n"
  "buffer_load_dwordx4 v8, s[40:43], s74 offen lds\n"
  "s_add_u32 m0, s57, 4096\n"
  "s_mov_b32 s74, 903168\n"
  "buffer_load_dwordx4 v8, s[40:43], s74 offen lds\n"
  "s_add_u32 m0, s57, 8192\n"
  "s_mov_b32 s74, 884736\n"
  "buffer_load_dwordx4 v9, s[40:43], s74 offen lds\n"
  "v_add_u32 v4, 12288, v10\n"
  "v_add_u32 v6, 12288, v11\n"
  "ds_read_b64_tr_b16 a[204:205], v4 offset:0\n"
  "ds_read_b64_tr_b16 a[206:207], v4 offset:1024\n"
  "ds_read_b64_tr_b16 a[208:209], v4 offset:8\n"
  "ds_read_b64_tr_b16 a[210:211], v4 offset:1032\n"
  "ds_read_b64_tr_b16 a[212:213], v6 offset:0\n"
  "ds_read_b64_tr_b16 a[214:215], v6 offset:512\n"
  "s_waitcnt vmcnt(18) lgkmcnt(0)\n"
  "s_barrier\n"
  "s_add_u32 m0, s57, 12288\n"
  "s_mov_b32 s74, 921600\n"
  "buffer_load_dwordx4 v8, s[40:43], s74 offen lds\n"
  "s_add_u32 m0, s57, 16384\n"
  "s_mov_b32 s74, 940032\n"
  "buffer_load_dwordx4 v8, s[40:43], s74 offen lds\n"
  "s_add_u32 m0, s57, 20480\n"
  "s_mov_b32 s74, 921600\n"
  "buffer_load_dwordx4 v9, s[40:43], s74 offen lds\n"
  "v_add_u32 v4, 24576, v10\n"
  "v_add_u32 v6, 24576, v11\n"
  "ds_read_b64_tr_b16 a[216:217], v4 offset:0\n"
  "ds_read_b64_tr_b16 a[218:219], v4 offset:1024\n"
  "ds_read_b64_tr_b16 a[220:221], v4 offset:8\n"
  "ds_read_b64_tr_b16 a[222:223], v4 offset:1032\n"
  "ds_read_b64_tr_b16 a[224:225], v6 offset:0\n"
  "ds_read_b64_tr_b16 a[226:227], v6 offset:512\n"
  "s_waitcnt vmcnt(18) lgkmcnt(0)\n"
  "s_barrier\n"
  "s_add_u32 m0, s57, 24576\n"
  "s_mov_b32 s74, 958464\n"
  "buffer_load_dwordx4 v8, s[40:43], s74 offen lds\n"
  "s_add_u32 m0, s57, 28672\n"
  "s_mov_b32 s74, 976896\n"
  "buffer_load_dwordx4 v8, s[40:43], s74 offen lds\n"
  "s_add_u32 m0, s57, 32768\n"
  "s_mov_b32 s74, 958464\n"
  "buffer_load_dwordx4 v9, s[40:43], s74 offen lds\n"
  "v_add_u32 v4, 36864, v10\n"
  "v_add_u32 v6, 36864, v11\n"
  "ds_read_b64_tr_b16 a[228:229], v4 offset:0\n"
  "ds_read_b64_tr_b16 a[230:231], v4 offset:1024\n"
  "ds_read_b64_tr_b16 a[232:233], v4 offset:8\n"
  "ds_read_b64_tr_b16 a[234:235], v4 offset:1032\n"
  "ds_read_b64_tr_b16 a[236:237], v6 offset:0\n"
  "ds_read_b64_tr_b16 a[238:239], v6 offset:512\n"
  "s_waitcnt vmcnt(18) lgkmcnt(0)\n"
  "s_barrier\n"
  "s_add_u32 m0, s57, 36864\n"
  "s_mov_b32 s74, 995328\n"
  "buffer_load_dwordx4 v8, s[40:43], s74 offen lds\n"
  "s_add_u32 m0, s57, 40960\n"
  "s_mov_b32 s74, 1013760\n"
  "buffer_load_dwordx4 v8, s[40:43], s74 offen lds\n"
  "s_add_u32 m0, s57, 45056\n"
  "s_mov_b32 s74, 995328\n"
  "buffer_load_dwordx4 v9, s[40:43], s74 offen lds\n"
  "v_add_u32 v4, 49152, v10\n"
  "v_add_u32 v6, 49152, v11\n"
  "ds_read_b64_tr_b16 a[240:241], v4 offset:0\n"
  "ds_read_b64_tr_b16 a[242:243], v4 offset:1024\n"
  "ds_read_b64_tr_b16 a[244:245], v4 offset:8\n"
  "ds_read_b64_tr_b16 a[246:247], v4 offset:1032\n"
  "ds_read_b64_tr_b16 a[248:249], v6 offset:0\n"
  "ds_read_b64_tr_b16 a[250:251], v6 offset:512\n"
  "s_waitcnt vmcnt(18) lgkmcnt(0)\n"
  "s_barrier\n"
  "s_add_u32 m0, s57, 49152\n"
  "s_mov_b32 s74, 1032192\n"
  "buffer_load_dwordx4 v8, s[40:43], s74 offen lds\n"
  "s_add_u32 m0, s57, 53248\n"
  "s_mov_b32 s74, 1050624\n"
  "buffer_load_dwordx4 v8, s[40:43], s74 offen lds\n"
  "s_add_u32 m0, s57, 57344\n"
  "s_mov_b32 s74, 1032192\n"
  "buffer_load_dwordx4 v9, s[40:43], s74 offen lds\n"
  "v_add_u32 v4, 61440, v10\n"
  "v_add_u32 v6, 61440, v11\n"
  "ds_read_b64_tr_b16 a[252:253], v4 offset:0\n"
  "ds_read_b64_tr_b16 a[254:255], v4 offset:1024\n"
  "ds_read_b64_tr_b16 v[128:129], v4 offset:8\n"
  "ds_read_b64_tr_b16 v[130:131], v4 offset:1032\n"
  "ds_read_b64_tr_b16 v[132:133], v6 offset:0\n"
  "ds_read_b64_tr_b16 v[134:135], v6 offset:512\n"
  "s_waitcnt vmcnt(18) lgkmcnt(0)\n"
  "s_barrier\n"
  "s_add_u32 m0, s57, 61440\n"
  "s_mov_b32 s74, 1069056\n"
  "buffer_load_dwordx4 v8, s[40:43], s74 offen lds\n"
  "s_add_u32 m0, s57, 65536\n"
  "s_mov_b32 s74, 1087488\n"
  "buffer_load_dwordx4 v8, s[40:43], s74 offen lds\n"
  "s_add_u32 m0, s57, 69632\n"
  "s_mov_b32 s74, 1069056\n"
  "buffer_load_dwordx4 v9, s[40:43], s74 offen lds\n"
  "v_add_u32 v4, 73728, v10\n"
  "v_add_u32 v6, 73728, v11\n"
  "ds_read_b64_tr_b16 v[136:137], v4 offset:0\n"
  "ds_read_b64_tr_b16 v[138:139], v4 offset:1024\n"
  "ds_read_b64_tr_b16 v[140:141], v4 offset:8\n"
  "ds_read_b64_tr_b16 v[142:143], v4 offset:1032\n"
  "ds_read_b64_tr_b16 v[144:145], v6 offset:0\n"
  "ds_read_b64_tr_b16 v[146:147], v6 offset:512\n"
  "s_waitcnt vmcnt(18) lgkmcnt(0)\n"
  "s_barrier\n"
  "s_add_u32 m0, s57, 73728\n"
  "s_mov_b32 s74, 1105920\n"
  "buffer_load_dwordx4 v8, s[40:43], s74 offen lds\n"
  "s_add_u32 m0, s57, 77824\n"
  "s_mov_b32 s74, 1124352\n"
  "buffer_load_dwordx4 v8, s[40:43], s74 offen lds\n"
  "s_add_u32 m0, s57, 81920\n"
  "s_mov_b32 s74, 1105920\n"
  "buffer_load_dwordx4 v9, s[40:43], s74 offen lds\n"
  "v_add_u32 v4, 86016, v10\n"
  "v_add_u32 v6, 86016, v11\n"
  "ds_read_b64_tr_b16 v[148:149], v4 offset:0\n"
  "ds_read_b64_tr_b16 v[150:151], v4 offset:1024\n"
  "ds_read_b64_tr_b16 v[152:153], v4 offset:8\n"
  "ds_read_b64_tr_b16 v[154:155], v4 offset:1032\n"
  "ds_read_b64_tr_b16 v[156:157], v6 offset:0\n"
  "ds_read_b64_tr_b16 v[158:159], v6 offset:512\n"
  "s_waitcnt vmcnt(18) lgkmcnt(0)\n"
  "s_barrier\n"
  "s_add_u32 m0, s57, 86016\n"
  "s_mov_b32 s74, 1142784\n"
  "buffer_load_dwordx4 v8, s[40:43], s74 offen lds\n"
  "s_add_u32 m0, s57, 90112\n"
  "s_mov_b32 s74, 1161216\n"
  "buffer_load_dwordx4 v8, s[40:43], s74 offen lds\n"
  "s_add_u32 m0, s57, 94208\n"
  "s_mov_b32 s74, 1142784\n"
  "buffer_load_dwordx4 v9, s[40:43], s74 offen lds\n"
  "v_add_u32 v4, 0, v10\n"
  "v_add_u32 v6, 0, v11\n"
  "ds_read_b64_tr_b16 v[160:161], v4 offset:0\n"
  "ds_read_b64_tr_b16 v[162:163], v4 offset:1024\n"
  "ds_read_b64_tr_b16 v[164:165], v4 offset:8\n"
  "ds_read_b64_tr_b16 v[166:167], v4 offset:1032\n"
  "ds_read_b64_tr_b16 v[168:169], v6 offset:0\n"
  "ds_read_b64_tr_b16 v[170:171], v6 offset:512\n"
  "s_waitcnt vmcnt(18) lgkmcnt(0)\n"
  "s_barrier\n"
  "v_add_u32 v4, 12288, v10\n"
  "v_add_u32 v6, 12288, v11\n"
  "ds_read_b64_tr_b16 v[172:173], v4 offset:0\n"
  "ds_read_b64_tr_b16 v[174:175], v4 offset:1024\n"
  "ds_read_b64_tr_b16 v[176:177], v4 offset:8\n"
  "ds_read_b64_tr_b16 v[178:179], v4 offset:1032\n"
  "ds_read_b64_tr_b16 v[180:181], v6 offset:0\n"
  "ds_read_b64_tr_b16 v[182:183], v6 offset:512\n"
  "s_waitcnt vmcnt(15) lgkmcnt(0)\n"
  "s_barrier\n"
  "v_add_u32 v4, 24576, v10\n"
  "v_add_u32 v6, 24576, v11\n"
  "ds_read_b64_tr_b16 v[184:185], v4 offset:0\n"
  "ds_read_b64_tr_b16 v[186:187], v4 offset:1024\n"
  "ds_read_b64_tr_b16 v[188:189], v4 offset:8\n"
  "ds_read_b64_tr_b16 v[190:191], v4 offset:1032\n"
  "ds_read_b64_tr_b16 v[192:193], v6 offset:0\n"
  "ds_read_b64_tr_b16 v[194:195], v6 offset:512\n"
  "s_waitcnt vmcnt(12) lgkmcnt(0)\n"
  "s_barrier\n"
  "v_add_u32 v4, 36864, v10\n"
  "v_add_u32 v6, 36864, v11\n"
  "ds_read_b64_tr_b16 v[196:197], v4 offset:0\n"
  "ds_read_b64_tr_b16 v[198:199], v4 offset:1024\n"
  "ds_read_b64_tr_b16 v[200:201], v4 offset:8\n"
  "ds_read_b64_tr_b16 v[202:203], v4 offset:1032\n"
  "ds_read_b64_tr_b16 v[204:205], v6 offset:0\n"
  "ds_read_b64_tr_b16 v[206:207], v6 offset:512\n"
  "s_waitcnt vmcnt(9) lgkmcnt(0)\n"
  "s_barrier\n"
  "v_add_u32 v4, 49152, v10\n"
  "v_add_u32 v6, 49152, v11\n"
  "ds_read_b64_tr_b16 v[208:209], v4 offset:0\n"
  "ds_read_b64_tr_b16 v[210:211], v4 offset:1024\n"
  "ds_read_b64_tr_b16 v[212:213], v4 offset:8\n"
  "ds_read_b64_tr_b16 v[214:215], v4 offset:1032\n"
  "ds_read_b64_tr_b16 v[216:217], v6 offset:0\n"
  "ds_read_b64_tr_b16 v[218:219], v6 offset:512\n"
  "s_waitcnt vmcnt(6) lgkmcnt(0)\n"
  "s_barrier\n"
  "v_add_u32 v4, 61440, v10\n"
  "v_add_u32 v6, 61440, v11\n"
  "ds_read_b64_tr_b16 v[220:221], v4 offset:0\n"
  "ds_read_b64_tr_b16 v[222:223], v4 offset:1024\n"
  "ds_read_b64_tr_b16 v[224:225], v4 offset:8\n"
  "ds_read_b64_tr_b16 v[226:227], v4 offset:1032\n"
  "ds_read_b64_tr_b16 v[228:229], v6 offset:0\n"
  "ds_read_b64_tr_b16 v[230:231], v6 offset:512\n"
  "s_waitcnt vmcnt(3) lgkmcnt(0)\n"
  "s_barrier\n"
  "v_add_u32 v4, 73728, v10\n"
  "v_add_u32 v6, 73728, v11\n"
  "ds_read_b64_tr_b16 v[232:233], v4 offset:0\n"
  "ds_read_b64_tr_b16 v[234:235], v4 offset:1024\n"
  "ds_read_b64_tr_b16 v[236:237], v4 offset:8\n"
  "ds_read_b64_tr_b16 v[238:239], v4 offset:1032\n"
  "ds_read_b64_tr_b16 v[240:241], v6 offset:0\n"
  "ds_read_b64_tr_b16 v[242:243], v6 offset:512\n"
  "s_waitcnt vmcnt(0) lgkmcnt(0)\n"
  "s_barrier\n"
  "v_add_u32 v4, 86016, v10\n"
  "v_add_u32 v6, 86016, v11\n"
  "ds_read_b64_tr_b16 v[244:245], v4 offset:0\n"
  "ds_read_b64_tr_b16 v[246:247], v4 offset:1024\n"
  "ds_read_b64_tr_b16 v[248:249], v4 offset:8\n"
  "ds_read_b64_tr_b16 v[250:251], v4 offset:1032\n"
  "ds_read_b64_tr_b16 v[252:253], v6 offset:0\n"
  "ds_read_b64_tr_b16 v[254:255], v6 offset:512\n"
  "s_waitcnt lgkmcnt(0)\n"
  "s_lshr_b32 s75, s57, 10\n"
  "s_add_u32 s74, s75, 0\n"
  "s_min_u32 s74, s74, 63\n"
  "s_add_u32 s74, s74, s64\n"
  "s_lshr_b32 s76, s74, 3\n"
  "s_lshl_b32 s76, s76, 14\n"
  "s_and_b32 s74, s74, 7\n"
  "s_lshl_b32 s74, s74, 10\n"
  "s_or_b32 s4, s74, s76\n"
  "s_add_u32 s74, s75, 4\n"
  "s_min_u32 s74, s74, 63\n"
  "s_add_u32 s74, s74, s64\n"
  "s_lshr_b32 s76, s74, 3\n"
  "s_lshl_b32 s76, s76, 14\n"
  "s_and_b32 s74, s74, 7\n"
  "s_lshl_b32 s74, s74, 10\n"
  "s_or_b32 s5, s74, s76\n"
  "s_add_u32 s74, s75, 8\n"
  "s_min_u32 s74, s74, 63\n"
  "s_add_u32 s74, s74, s64\n"
  "s_lshr_b32 s76, s74, 3\n"
  "s_lshl_b32 s76, s76, 14\n"
  "s_and_b32 s74, s74, 7\n"
  "s_lshl_b32 s74, s74, 10\n"
  "s_or_b32 s6, s74, s76\n"
  "s_add_u32 s74, s75, 12\n"
  "s_min_u32 s74, s74, 63\n"
  "s_add_u32 s74, s74, s64\n"
  "s_lshr_b32 s76, s74, 3\n"
  "s_lshl_b32 s76, s76, 14\n"
  "s_and_b32 s74, s74, 7\n"
  "s_lshl_b32 s74, s74, 10\n"
  "s_or_b32 s7, s74, s76\n"
  "s_add_u32 s74, s75, 16\n"
  "s_min_u32 s74, s74, 63\n"
  "s_add_u32 s74, s74, s64\n"
  "s_lshr_b32 s76, s74, 3\n"
  "s_lshl_b32 s76, s76, 14\n"
  "s_and_b32 s74, s74, 7\n"
  "s_lshl_b32 s74, s74, 10\n"
  "s_or_b32 s8, s74, s76\n"
  "s_add_u32 s74, s75, 20\n"
  "s_min_u32 s74, s74, 63\n"
  "s_add_u32 s74, s74, s64\n"
  "s_lshr_b32 s76, s74, 3\n"
  "s_lshl_b32 s76, s76, 14\n"
  "s_and_b32 s74, s74, 7\n"
  "s_lshl_b32 s74, s74, 10\n"
  "s_or_b32 s9, s74, s76\n"
  "s_add_u32 s74, s75, 24\n"
  "s_min_u32 s74, s74, 63\n"
  "s_add_u32 s74, s74, s64\n"
  "s_lshr_b32 s76, s74, 3\n"
  "s_lshl_b32 s76, s76, 14\n"
  "s_and_b32 s74, s74, 7\n"
  "s_lshl_b32 s74, s74, 10\n"
  "s_or_b32 s10, s74, s76\n"
  "s_add_u32 s74, s75, 28\n"
  "s_min_u32 s74, s74, 63\n"
  "s_add_u32 s74, s74, s64\n"
  "s_lshr_b32 s76, s74, 3\n"
  "s_lshl_b32 s76, s76, 14\n"
  "s_and_b32 s74, s74, 7\n"
  "s_lshl_b32 s74, s74, 10\n"
  "s_or_b32 s11, s74, s76\n"
  "s_add_u32 s74, s75, 32\n"
  "s_min_u32 s74, s74, 63\n"
  "s_add_u32 s74, s74, s64\n"
  "s_lshr_b32 s76, s74, 3\n"
  "s_lshl_b32 s76, s76, 14\n"
  "s_and_b32 s74, s74, 7\n"
  "s_lshl_b32 s74, s74, 10\n"
  "s_or_b32 s12, s74, s76\n"
  "s_add_u32 s74, s75, 36\n"
  "s_min_u32 s74, s74, 63\n"
  "s_add_u32 s74, s74, s64\n"
  "s_lshr_b32 s76, s74, 3\n"
  "s_lshl_b32 s76, s76, 14\n"
  "s_and_b32 s74, s74, 7\n"
  "s_lshl_b32 s74, s74, 10\n"
  "s_or_b32 s13, s74, s76\n"
  "s_add_u32 s74, s75, 40\n"
  "s_min_u32 s74, s74, 63\n"
  "s_add_u32 s74, s74, s64\n"
  "s_lshr_b32 s76, s74, 3\n"
  "s_lshl_b32 s76, s76, 14\n"
  "s_and_b32 s74, s74, 7\n"
  "s_lshl_b32 s74, s74, 10\n"
  "s_or_b32 s14, s74, s76\n"
  "s_add_u32 s74, s75, 44\n"
  "s_min_u32 s74, s74, 63\n"
  "s_add_u32 s74, s74, s64\n"
  "s_lshr_b32 s76, s74, 3\n"
  "s_lshl_b32 s76, s76, 14\n"
  "s_and_b32 s74, s74, 7\n"
  "s_lshl_b32 s74, s74, 10\n"
  "s_or_b32 s15, s74, s76\n"
  "s_add_u32 s74, s75, 48\n"
  "s_min_u32 s74, s74, 63\n"
  "s_add_u32 s74, s74, s64\n"
  "s_lshr_b32 s76, s74, 3\n"
  "s_lshl_b32 s76, s76, 14\n"
  "s_and_b32 s74, s74, 7\n"
  "s_lshl_b32 s74, s74, 10\n"
  "s_or_b32 s16, s74, s76\n"
  "s_add_u32 s74, s75, 52\n"
  "s_min_u32 s74, s74, 63\n"
  "s_add_u32 s74, s74, s64\n"
  "s_lshr_b32 s76, s74, 3\n"
  "s_lshl_b32 s76, s76, 14\n"
  "s_and_b32 s74, s74, 7\n"
  "s_lshl_b32 s74, s74, 10\n"
  "s_or_b32 s17, s74, s76\n"
  "s_add_u32 s74, s75, 56\n"
  "s_min_u32 s74, s74, 63\n"
  "s_add_u32 s74, s74, s64\n"
  "s_lshr_b32 s76, s74, 3\n"
  "s_lshl_b32 s76, s76, 14\n"
  "s_and_b32 s74, s74, 7\n"
  "s_lshl_b32 s74, s74, 10\n"
  "s_or_b32 s18, s74, s76\n"
  "s_add_u32 s74, s75, 60\n"
  "s_min_u32 s74, s74, 63\n"
  "s_add_u32 s74, s74, s64\n"
  "s_lshr_b32 s76, s74, 3\n"
  "s_lshl_b32 s76, s76, 14\n"
  "s_and_b32 s74, s74, 7\n"
  "s_lshl_b32 s74, s74, 10\n"
  "s_or_b32 s19, s74, s76\n"
  "s_barrier\n"
  "v_lshrrev_b32 v4, 2, v0\n"
  "v_add_u32 v4, 163584, v4\n"
  "v_mov_b32 v6, 0\n"
  "ds_write_b32 v4, v6\n"
  "v_min_u32 v5, 1, v0\n"
  "v_lshlrev_b32 v5, 30, v5\n"
  "s_mov_b32 s70, 0\n"
  "s_mov_b32 s71, 0\n"
  "s_mov_b32 s72, 0\n"
  "s_mov_b32 s81, 0\n"
  "s_mov_b32 s82, 0\n"
  "s_mov_b32 s83, 0\n"
  "s_mov_b32 s95, -1\n"
  "s_cmp_eq_u32 s57, 0\n"
  "s_cselect_b32 s92, s58, 0x80000000\n"
  "s_waitcnt lgkmcnt(0)\n"
  "s_barrier\n"
  "s_branch L_first_%=\n"
  "L_pf_%=:\n"
  "s_waitcnt vmcnt(9)\n"
  "L_go_%=:\n"
  "s_barrier\n"
  "ds_read_b128 v[16:19], v1 offset:0\n"
  "ds_read_b128 v[20:23], v1 offset:256\n"
  "ds_read_b128 v[24:27], v1 offset:2048\n"
  "ds_read_b128 v[28:31], v1 offset:2304\n"
  "s_waitcnt lgkmcnt(3)\n"
  "v_mfma_f32_16x16x32_bf16 v[40:43], a[0:3], v[16:19], 0\n"
  "ds_read_b128 v[32:35], v1 offset:4096\n"
  "s_waitcnt lgkmcnt(3)\n"
  "v_mfma_f32_16x16x32_bf16 v[52:55], a[0:3], v[20:23], 0\n"
  "ds_read_b128 v[36:39], v1 offset:4352\n"
  "v_mfma_f32_16x16x32_bf16 v[44:47], a[4:7], v[16:19], 0\n"
  "v_mfma_f32_16x16x32_bf16 v[56:59], a[4:7], v[20:23], 0\n"
  "v_mfma_f32_16x16x32_bf16 v[48:51], a[8:11], v[16:19], 0\n"
  "v_mfma_f32_16x16x32_bf16 v[60:63], a[8:11], v[20:23], 0\n"
  "s_waitcnt lgkmcnt(2)\n"
  "v_mfma_f32_16x16x32_bf16 v[40:43], a[12:15], v[24:27], v[40:43]\n"
  "ds_read_b128 v[16:19], v1 offset:6144\n"
  "v_mfma_f32_16x16x32_bf16 v[52:55], a[12:15], v[28:31], v[52:55]\n"
  "ds_read_b128 v[20:23], v1 offset:6400\n"
  "v_mfma_f32_16x16x32_bf16 v[44:47], a[16:19], v[24:27], v[44:47]\n"
  "s_add_u32 m0, s79, 36864\n"
  "s_add_u32 s76, s78, s13\n"
  "v_mfma_f32_16x16x32_bf16 v[56:59], a[16:19], v[28:31], v[56:59]\n"
  "buffer_load_dwordx4 v0, s[36:39], s76 offen sc1 lds\n"
  "v_mfma_f32_16x16x32_bf16 v[48:51], a[20:23], v[24:27], v[48:51]\n"
  "v_mfma_f32_16x16x32_bf16 v[60:63], a[20:23], v[28:31], v[60:63]\n"
  "s_waitcnt lgkmcnt(2)\n"
  "v_mfma_f32_16x16x32_bf16 v[40:43], a[24:27], v[32:35], v[40:43]\n"
  "ds_read_b128 v[24:27], v1 offset:8192\n"
  "v_mfma_f32_16x16x32_bf16 v[52:55], a[24:27], v[36:39], v[52:55]\n"
  "ds_read_b128 v[28:31], v1 offset:8448\n"
  "s_add_u32 m0, s79, 40960\n"
  "s_add_u32 s76, s78, s14\n"
  "v_mfma_f32_16x16x32_bf16 v[44:47], a[28:31], v[32:35], v[44:47]\n"
  "buffer_load_dwordx4 v0, s[36:39], s76 offen sc1 lds\n"
  "v_mfma_f32_16x16x32_bf16 v[56:59], a[28:31], v[36:39], v[56:59]\n"
  "v_mfma_f32_16x16x32_bf16 v[48:51], a[32:35], v[32:35], v[48:51]\n"
  "v_mfma_f32_16x16x32_bf16 v[60:63], a[32:35], v[36:39], v[60:63]\n"
  "s_waitcnt lgkmcnt(2)\n"
  "v_mfma_f32_16x16x32_bf16 v[40:43], a[36:39], v[16:19], v[40:43]\n"
  "ds_read_b128 v[32:35], v1 offset:10240\n"
  "s_add_u32 m0, s79, 45056\n"
  "s_add_u32 s76, s78, s15\n"
  "v_mfma_f32_16x16x32_bf16 v[52:55], a[36:39], v[20:23], v[52:55]\n"
  "ds_read_b128 v[36:39], v1 offset:10496\n"
  "buffer_load_dwordx4 v0, s[36:39], s76 offen sc1 lds\n"
  "v_mfma_f32_16x16x32_bf16 v[44:47], a[40:43], v[16:19], v[44:47]\n"
  "v_mfma_f32_16x16x32_bf16 v[56:59], a[40:43], v[20:23], v[56:59]\n"
  "v_mfma_f32_16x16x32_bf16 v[48:51], a[44:47], v[16:19], v[48:51]\n"
  "v_mfma_f32_16x16x32_bf16 v[60:63], a[44:47], v[20:23], v[60:63]\n"
  "s_add_u32 m0, s79, 49152\n"
  "s_add_u32 s76, s78, s16\n"
  "s_waitcnt lgkmcnt(2)\n"
  "v_mfma_f32_16x16x32_bf16 v[40:43], a[48:51], v[24:27], v[40:43]\n"
  "ds_read_b128 v[16:19], v1 offset:12288\n"
  "buffer_load_dwordx4 v0, s[36:39], s76 offen sc1 lds\n"
  "v_mfma_f32_16x16x32_bf16 v[52:55], a[48:51], v[28:31], v[52:55]\n"
  "ds_read_b128 v[20:23], v1 offset:12544\n"
  "v_mfma_f32_16x16x32_bf16 v[44:47], a[52:55], v[24:27], v[44:47]\n"
  "v_mfma_f32_16x16x32_bf16 v[56:59], a[52:55], v[28:31], v[56:59]\n"
  "v_mfma_f32_16x16x32_bf16 v[48:51], a[56:59], v[24:27], v[48:51]\n"
  "s_add_u32 m0, s79, 53248\n"
  "s_add_u32 s76, s78, s17\n"
  "v_mfma_f32_16x16x32_bf16 v[60:63], a[56:59], v[28:31], v[60:63]\n"
  "buffer_load_dwordx4 v0, s[36:39], s76 offen sc1 lds\n"
  "s_waitcnt lgkmcnt(2)\n"
  "v_mfma_f32_16x16x32_bf16 v[40:43], a[60:63], v[32:35], v[40:43]\n"
  "ds_read_b128 v[24:27], v1 offset:14336\n"
  "v_mfma_f32_16x16x32_bf16 v[52:55], a[60:63], v[36:39], v[52:55]\n"
  "ds_read_b128 v[28:31], v1 offset:14592\n"
  "v_mfma_f32_16x16x32_bf16 v[44:47], a[64:67], v[32:35], v[44:47]\n"
  "s_add_u32 s75, s70, -1\n"
  "v_mov_b32 v6, s75\n"
  "v_mfma_f32_16x16x32_bf16 v[56:59], a[64:67], v[36:39], v[56:59]\n"
  "s_add_u32 m0, s79, 57344\n"
  "s_add_u32 s76, s78, s18\n"
  "v_mfma_f32_16x16x32_bf16 v[48:51], a[68:71], v[32:35], v[48:51]\n"
  "buffer_load_dwordx4 v0, s[36:39], s76 offen sc1 lds\n"
  "v_mfma_f32_16x16x32_bf16 v[60:63], a[68:71], v[36:39], v[60:63]\n"
  "s_waitcnt vmcnt(6)\n"
  "s_barrier\n"
  "s_waitcnt lgkmcnt(2)\n"
  "v_mfma_f32_16x16x32_bf16 v[40:43], a[72:75], v[16:19], v[40:43]\n"
  "ds_read_b128 v[32:35], v1 offset:16384\n"
  "v_mfma_f32_16x16x32_bf16 v[52:55], a[72:75], v[20:23], v[52:55]\n"
  "ds_read_b128 v[36:39], v1 offset:16640\n"
  "v_mfma_f32_16x16x32_bf16 v[44:47], a[76:79], v[16:19], v[44:47]\n"
  "buffer_store_dword v6, v5, s[36:39], s92 offen sc1\n"
  "v_mfma_f32_16x16x32_bf16 v[56:59], a[76:79], v[20:23], v[56:59]\n"
  "v_mfma_f32_16x16x32_bf16 v[48:51], a[80:83], v[16:19], v[48:51]\n"
  "v_mfma_f32_16x16x32_bf16 v[60:63], a[80:83], v[20:23], v[60:63]\n"
  "s_add_u32 m0, s79, 61440\n"
  "s_add_u32 s76, s78, s19\n"
  "s_waitcnt lgkmcnt(2)\n"
  "v_mfma_f32_16x16x32_bf16 v[40:43], a[84:87], v[24:27], v[40:43]\n"
  "ds_read_b128 v[16:19], v1 offset:18432\n"
  "buffer_load_dwordx4 v0, s[36:39], s76 offen sc1 lds\n"
  "v_mfma_f32_16x16x32_bf16 v[52:55], a[84:87], v[28:31], v[52:55]\n"
  "ds_read_b128 v[20:23], v1 offset:18688\n"
  "v_mfma_f32_16x16x32_bf16 v[44:47], a[88:91], v[24:27], v[44:47]\n"
  "v_mfma_f32_16x16x32_bf16 v[56:59], a[88:91], v[28:31], v[56:59]\n"
  "v_mfma_f32_16x16x32_bf16 v[48:51], a[92:95], v[24:27], v[48:51]\n"
  "v_mfma_f32_16x16x32_bf16 v[60:63], a[92:95], v[28:31], v[60:63]\n"
  "s_waitcnt lgkmcnt(2)\n"
  "v_mfma_f32_16x16x32_bf16 v[40:43], a[96:99], v[32:35], v[40:43]\n"
  "ds_read_b128 v[24:27], v1 offset:20480\n"
  "v_mfma_f32_16x16x32_bf16 v[52:55], a[96:99], v[36:39], v[52:55]\n"
  "ds_read_b128 v[28:31], v1 offset:20736\n"
  "v_mfma_f32_16x16x32_bf16 v[44:47], a[100:103], v[32:35], v[44:47]\n"
  "v_mfma_f32_16x16x32_bf16 v[56:59], a[100:103], v[36:39], v[56:59]\n"
  "v_mfma_f32_16x16x32_bf16 v[48:51], a[104:107], v[32:35], v[48:51]\n"
  "v_mfma_f32_16x16x32_bf16 v[60:63], a[104:107], v[36:39], v[60:63]\n"
  "s_waitcnt lgkmcnt(2)\n"
  "v_mfma_f32_16x16x32_bf16 v[40:43], a[108:111], v[16:19], v[40:43]\n"
  "ds_read_b128 v[32:35], v1 offset:22528\n"
  "v_mfma_f32_16x16x32_bf16 v[52:55], a[108:111], v[20:23], v[52:55]\n"
  "ds_read_b128 v[36:39], v1 offset:22784\n"
  "v_mfma_f32_16x16x32_bf16 v[44:47], a[112:115], v[16:19], v[44:47]\n"
  "s_add_u32 s78, s70, 1\n"
  "s_and_b32 s78, s78, s61\n"
  "v_mfma_f32_16x16x32_bf16 v[56:59], a[112:115], v[20:23], v[56:59]\n"
  "v_mfma_f32_16x16x32_bf16 v[48:51], a[116:119], v[16:19], v[48:51]\n"
  "s_mul_i32 s78, s78, s60\n"
  "s_add_u32 s78, s78, s59\n"
  "v_mfma_f32_16x16x32_bf16 v[60:63], a[116:119], v[20:23], v[60:63]\n"
  "s_xor_b32 s79, s81, 65536\n"
  "s_add_u32 s79, s79, s57\n"
  "s_waitcnt lgkmcnt(2)\n"
  "v_mfma_f32_16x16x32_bf16 v[40:43], a[120:123], v[24:27], v[40:43]\n"
  "ds_read_b128 v[16:19], v1 offset:24576\n"
  "v_mfma_f32_16x16x32_bf16 v[52:55], a[120:123], v[28:31], v[52:55]\n"
  "ds_read_b128 v[20:23], v1 offset:24832\n"
  "s_and_b32 s80, s70, 15\n"
  "s_lshl_b32 s80, s80, 22\n"
  "v_mfma_f32_16x16x32_bf16 v[44:47], a[124:127], v[24:27], v[44:47]\n"
  "s_add_u32 s80, s80, s62\n"
  "v_mfma_f32_16x16x32_bf16 v[56:59], a[124:127], v[28:31], v[56:59]\n"
  "v_mfma_f32_16x16x32_bf16 v[48:51], a[128:131], v[24:27], v[48:51]\n"
  "v_mfma_f32_16x16x32_bf16 v[60:63], a[128:131], v[28:31], v[60:63]\n"
  "s_waitcnt lgkmcnt(2)\n"
  "v_mfma_f32_16x16x32_bf16 v[40:43], a[132:135], v[32:35], v[40:43]\n"
  "ds_read_b128 v[24:27], v1 offset:26624\n"
  "v_mfma_f32_16x16x32_bf16 v[52:55], a[132:135], v[36:39], v[52:55]\n"
  "ds_read_b128 v[28:31], v1 offset:26880\n"
  "v_mfma_f32_16x16x32_bf16 v[44:47], a[136:139], v[32:35], v[44:47]\n"
  "v_mfma_f32_16x16x32_bf16 v[56:59], a[136:139], v[36:39], v[56:59]\n"
  "v_mfma_f32_16x16x32_bf16 v[48:51], a[140:143], v[32:35], v[48:51]\n"
  "v_mfma_f32_16x16x32_bf16 v[60:63], a[140:143], v[36:39], v[60:63]\n"
  "s_waitcnt lgkmcnt(2)\n"
  "v_mfma_f32_16x16x32_bf16 v[40:43], a[144:147], v[16:19], v[40:43]\n"
  "ds_read_b128 v[32:35], v1 offset:28672\n"
  "v_mfma_f32_16x16x32_bf16 v[52:55], a[144:147], v[20:23], v[52:55]\n"
  "ds_read_b128 v[36:39], v1 offset:28928\n"
  "v_mfma_f32_16x16x32_bf16 v[44:47], a[148:151], v[16:19], v[44:47]\n"
  "v_mfma_f32_16x16x32_bf16 v[56:59], a[148:151], v[20:23], v[56:59]\n"
  "v_mfma_f32_16x16x32_bf16 v[48:51], a[152:155], v[16:19], v[48:51]\n"
  "v_mfma_f32_16x16x32_bf16 v[60:63], a[152:155], v[20:23], v[60:63]\n"
  "s_waitcnt lgkmcnt(2)\n"
  "v_mfma_f32_16x16x32_bf16 v[40:43], a[156:159], v[24:27], v[40:43]\n"
  "ds_read_b128 v[16:19], v1 offset:30720\n"
  "v_mfma_f32_16x16x32_bf16 v[52:55], a[156:159], v[28:31], v[52:55]\n"
  "ds_read_b128 v[20:23], v1 offset:30976\n"
  "v_mfma_f32_16x16x32_bf16 v[44:47], a[160:163], v[24:27], v[44:47]\n"
  "v_mfma_f32_16x16x32_bf16 v[56:59], a[160:163], v[28:31], v[56:59]\n"
  "v_mfma_f32_16x16x32_bf16 v[48:51], a[164:167], v[24:27], v[48:51]\n"
  "v_mfma_f32_16x16x32_bf16 v[60:63], a[164:167], v[28:31], v[60:63]\n"
  "s_waitcnt lgkmcnt(2)\n"
  "v_mfma_f32_16x16x32_bf16 v[40:43], a[168:171], v[32:35], v[40:43]\n"
  "ds_read_b128 v[24:27], v1 offset:32768\n"
  "v_mfma_f32_16x16x32_bf16 v[52:55], a[168:171], v[36:39], v[52:55]\n"
  "ds_read_b128 v[28:31], v1 offset:33024\n"
  "v_mfma_f32_16x16x32_bf16 v[44:47], a[172:175], v[32:35], v[44:47]\n"
  "v_mfma_f32_16x16x32_bf16 v[56:59], a[172:175], v[36:39], v[56:59]\n"
  "v_mfma_f32_16x16x32_bf16 v[48:51], a[176:179], v[32:35], v[48:51]\n"
  "v_mfma_f32_16x16x32_bf16 v[60:63], a[176:179], v[36:39], v[60:63]\n"
  "s_waitcnt lgkmcnt(2)\n"
  "v_mfma_f32_16x16x32_bf16 v[40:43], a[180:183], v[16:19], v[40:43]\n"
  "ds_read_b128 v[32:35], v1 offset:34816\n"
  "v_mfma_f32_16x16x32_bf16 v[52:55], a[180:183], v[20:23], v[52:55]\n"
  "ds_read_b128 v[36:39], v1 offset:35072\n"
  "v_mfma_f32_16x16x32_bf16 v[44:47], a[184:187], v[16:19], v[44:47]\n"
  "v_mfma_f32_16x16x32_bf16 v[56:59], a[184:187], v[20:23], v[56:59]\n"
  "v_mfma_f32_16x16x32_bf16 v[48:51], a[188:191], v[16:19], v[48:51]\n"
  "buffer_load_dword v4, v2, s[36:39], 0 offen sc1\n"
  "v_mfma_f32_16x16x32_bf16 v[60:63], a[188:191], v[20:23], v[60:63]\n"
  "s_waitcnt vmcnt(1)\n"
  "s_barrier\n"
  "s_waitcnt lgkmcnt(2)\n"
  "v_mfma_f32_16x16x32_bf16 v[40:43], a[192:195], v[24:27], v[40:43]\n"
  "ds_read_b128 v[16:19], v1 offset:36864\n"
  "v_mfma_f32_16x16x32_bf16 v[52:55], a[192:195], v[28:31], v[52:55]\n"
  "ds_read_b128 v[20:23], v1 offset:37120\n"
  "v_mfma_f32_16x16x32_bf16 v[44:47], a[196:199], v[24:27], v[44:47]\n"
  "v_mfma_f32_16x16x32_bf16 v[56:59], a[196:199], v[28:31], v[56:59]\n"
  "v_mfma_f32_16x16x32_bf16 v[48:51], a[200:203], v[24:27], v[48:51]\n"
  "v_mfma_f32_16x16x32_bf16 v[60:63], a[200:203], v[28:31], v[60:63]\n"
  "s_waitcnt lgkmcnt(2)\n"
  "v_mfma_f32_16x16x32_bf16 v[40:43], a[204:207], v[32:35], v[40:43]\n"
  "ds_read_b128 v[24:27], v1 offset:38912\n"
  "v_mfma_f32_16x16x32_bf16 v[52:55], a[204:207], v[36:39], v[52:55]\n"
  "ds_read_b128 v[28:31], v1 offset:39168\n"
  "v_mfma_f32_16x16x32_bf16 v[44:47], a[208:211], v[32:35], v[44:47]\n"
  "v_mfma_f32_16x16x32_bf16 v[56:59], a[208:211], v[36:39], v[56:59]\n"
  "v_mfma_f32_16x16x32_bf16 v[48:51], a[212:215], v[32:35], v[48:51]\n"
  "v_mfma_f32_16x16x32_bf16 v[60:63], a[212:215], v[36:39], v[60:63]\n"
  "s_waitcnt lgkmcnt(2)\n"
  "v_mfma_f32_16x16x32_bf16 v[40:43], a[216:219], v[16:19], v[40:43]\n"
  "ds_read_b128 v[32:35], v1 offset:40960\n"
  "v_mfma_f32_16x16x32_bf16 v[52:55], a[216:219], v[20:23], v[52:55]\n"
  "ds_read_b128 v[36:39], v1 offset:41216\n"
  "v_mfma_f32_16x16x32_bf16 v[44:47], a[220:223], v[16:19], v[44:47]\n"
  "v_mfma_f32_16x16x32_bf16 v[56:59], a[220:223], v[20:23], v[56:59]\n"
  "v_mfma_f32_16x16x32_bf16 v[48:51], a[224:227], v[16:19], v[48:51]\n"
  "v_mfma_f32_16x16x32_bf16 v[60:63], a[224:227], v[20:23], v[60:63]\n"
  "s_waitcnt lgkmcnt(2)\n"
  "v_mfma_f32_16x16x32_bf16 v[40:43], a[228:231], v[24:27], v[40:43]\n"
  "ds_read_b128 v[16:19], v1 offset:43008\n"
  "v_mfma_f32_16x16x32_bf16 v[52:55], a[228:231], v[28:31], v[52:55]\n"
  "ds_read_b128 v[20:23], v1 offset:43264\n"
  "v_mfma_f32_16x16x32_bf16 v[44:47], a[232:235], v[24:27], v[44:47]\n"
  "v_mfma_f32_16x16x32_bf16 v[56:59], a[232:235], v[28:31], v[56:59]\n"
  "v_mfma_f32_16x16x32_bf16 v[48:51], a[236:239], v[24:27], v[48:51]\n"
  "v_mfma_f32_16x16x32_bf16 v[60:63], a[236:239], v[28:31], v[60:63]\n"
  "s_waitcnt lgkmcnt(2)\n"
  "v_mfma_f32_16x16x32_bf16 v[40:43], a[240:243], v[32:35], v[40:43]\n"
  "ds_read_b128 v[24:27], v1 offset:45056\n"
  "v_mfma_f32_16x16x32_bf16 v[52:55], a[240:243], v[36:39], v[52:55]\n"
  "ds_read_b128 v[28:31], v1 offset:45312\n"
  "v_mfma_f32_16x16x32_bf16 v[44:47], a[244:247], v[32:35], v[44:47]\n"
  "v_mfma_f32_16x16x32_bf16 v[56:59], a[244:247], v[36:39], v[56:59]\n"
  "v_mfma_f32_16x16x32_bf16 v[48:51], a[248:251], v[32:35], v[48:51]\n"
  "v_mfma_f32_16x16x32_bf16 v[60:63], a[248:251], v[36:39], v[60:63]\n"
  "s_waitcnt lgkmcnt(2)\n"
  "v_mfma_f32_16x16x32_bf16 v[40:43], a[252:255], v[16:19], v[40:43]\n"
  "ds_read_b128 v[32:35], v1 offset:47104\n"
  "v_mfma_f32_16x16x32_bf16 v[52:55], a[252:255], v[20:23], v[52:55]\n"
  "ds_read_b128 v[36:39], v1 offset:47360\n"
  "v_mfma_f32_16x16x32_bf16 v[44:47], v[128:131], v[16:19], v[44:47]\n"
  "v_mfma_f32_16x16x32_bf16 v[56:59], v[128:131], v[20:23], v[56:59]\n"
  "v_mfma_f32_16x16x32_bf16 v[48:51], v[132:135], v[16:19], v[48:51]\n"
  "v_mfma_f32_16x16x32_bf16 v[60:63], v[132:135], v[20:23], v[60:63]\n"
  "s_waitcnt lgkmcnt(2)\n"
  "v_mfma_f32_16x16x32_bf16 v[40:43], v[136:139], v[24:27], v[40:43]\n"
  "ds_read_b128 v[16:19], v1 offset:49152\n"
  "v_mfma_f32_16x16x32_bf16 v[52:55], v[136:139], v[28:31], v[52:55]\n"
  "ds_read_b128 v[20:23], v1 offset:49408\n"
  "s_waitcnt vmcnt(0)\n"
  "v_subrev_u32 v6, v3, v4\n"
  "v_cmp_lt_i32 vcc, s70, v6\n"
  "v_mfma_f32_16x16x32_bf16 v[44:47], v[140:143], v[24:27], v[44:47]\n"
  "s_cmp_lt_i32 s70, 63\n"
  "s_cselect_b32 s94, -1, 0\n"
  "v_mfma_f32_16x16x32_bf16 v[56:59], v[140:143], v[28:31], v[56:59]\n"
  "v_mfma_f32_16x16x32_bf16 v[48:51], v[144:147], v[24:27], v[48:51]\n"
  "s_cmp_eq_u64 vcc, s[94:95]\n"
  "s_cselect_b32 s71, 1, 0\n"
  "s_cselect_b32 s78, s78, 0x80000000\n"
  "v_mfma_f32_16x16x32_bf16 v[60:63], v[144:147], v[28:31], v[60:63]\n"
  "s_waitcnt lgkmcnt(2)\n"
  "v_mfma_f32_16x16x32_bf16 v[40:43], v[148:151], v[32:35], v[40:43]\n"
  "ds_read_b128 v[24:27], v1 offset:51200\n"
  "s_add_u32 m0, s79, 0\n"
  "s_add_u32 s76, s78, s4\n"
  "v_mfma_f32_16x16x32_bf16 v[52:55], v[148:151], v[36:39], v[52:55]\n"
  "ds_read_b128 v[28:31], v1 offset:51456\n"
  "buffer_load_dwordx4 v0, s[36:39], s76 offen sc1 lds\n"
  "v_mfma_f32_16x16x32_bf16 v[44:47], v[152:155], v[32:35], v[44:47]\n"
  "v_mfma_f32_16x16x32_bf16 v[56:59], v[152:155], v[36:39], v[56:59]\n"
  "v_mfma_f32_16x16x32_bf16 v[48:51], v[156:159], v[32:35], v[48:51]\n"
  "v_mfma_f32_16x16x32_bf16 v[60:63], v[156:159], v[36:39], v[60:63]\n"
  "s_add_u32 m0, s79, 4096\n"
  "s_add_u32 s76, s78, s5\n"
  "s_waitcnt lgkmcnt(2)\n"
  "v_mfma_f32_16x16x32_bf16 v[40:43], v[160:163], v[16:19], v[40:43]\n"
  "ds_read_b128 v[32:35], v1 offset:53248\n"
  "buffer_load_dwordx4 v0, s[36:39], s76 offen sc1 lds\n"
  "v_mfma_f32_16x16x32_bf16 v[52:55], v[160:163], v[20:23], v[52:55]\n"
  "ds_read_b128 v[36:39], v1 offset:53504\n"
  "v_mfma_f32_16x16x32_bf16 v[44:47], v[164:167], v[16:19], v[44:47]\n"
  "v_mfma_f32_16x16x32_bf16 v[56:59], v[164:167], v[20:23], v[56:59]\n"
  "v_mfma_f32_16x16x32_bf16 v[48:51], v[168:171], v[16:19], v[48:51]\n"
  "s_add_u32 m0, s79, 8192\n"
  "s_add_u32 s76, s78, s6\n"
  "v_mfma_f32_16x16x32_bf16 v[60:63], v[168:171], v[20:23], v[60:63]\n"
  "buffer_load_dwordx4 v0, s[36:39], s76 offen sc1 lds\n"
  "s_waitcnt lgkmcnt(2)\n"
  "v_mfma_f32_16x16x32_bf16 v[40:43], v[172:175], v[24:27], v[40:43]\n"
  "ds_read_b128 v[16:19], v1 offset:55296\n"
  "v_mfma_f32_16x16x32_bf16 v[52:55], v[172:175], v[28:31], v[52:55]\n"
  "ds_read_b128 v[20:23], v1 offset:55552\n"
  "v_mfma_f32_16x16x32_bf16 v[44:47], v[176:179], v[24:27], v[44:47]\n"
  "v_mfma_f32_16x16x32_bf16 v[56:59], v[176:179], v[28:31], v[56:59]\n"
  "s_add_u32 m0, s79, 12288\n"
  "s_add_u32 s76, s78, s7\n"
  "v_mfma_f32_16x16x32_bf16 v[48:51], v[180:183], v[24:27], v[48:51]\n"
  "buffer_load_dwordx4 v0, s[36:39], s76 offen sc1 lds\n"
  "v_mfma_f32_16x16x32_bf16 v[60:63], v[180:183], v[28:31], v[60:63]\n"
  "s_waitcnt lgkmcnt(2)\n"
  "v_mfma_f32_16x16x32_bf16 v[40:43], v[184:187], v[32:35], v[40:43]\n"
  "ds_read_b128 v[24:27], v1 offset:57344\n"
  "v_mfma_f32_16x16x32_bf16 v[52:55], v[184:187], v[36:39], v[52:55]\n"
  "ds_read_b128 v[28:31], v1 offset:57600\n"
  "v_mfma_f32_16x16x32_bf16 v[44:47], v[188:191], v[32:35], v[44:47]\n"
  "s_add_u32 m0, s79, 16384\n"
  "s_add_u32 s76, s78, s8\n"
  "v_mfma_f32_16x16x32_bf16 v[56:59], v[188:191], v[36:39], v[56:59]\n"
  "buffer_load_dwordx4 v0, s[36:39], s76 offen sc1 lds\n"
  "v_mfma_f32_16x16x32_bf16 v[48:51], v[192:195], v[32:35], v[48:51]\n"
  "v_mfma_f32_16x16x32_bf16 v[60:63], v[192:195], v[36:39], v[60:63]\n"
  "s_waitcnt lgkmcnt(2)\n"
  "v_mfma_f32_16x16x32_bf16 v[40:43], v[196:199], v[16:19], v[40:43]\n"
  "ds_read_b128 v[32:35], v1 offset:59392\n"
  "v_mfma_f32_16x16x32_bf16 v[52:55], v[196:199], v[20:23], v[52:55]\n"
  "ds_read_b128 v[36:39], v1 offset:59648\n"
  "s_add_u32 m0, s79, 20480\n"
  "s_add_u32 s76, s78, s9\n"
  "v_mfma_f32_16x16x32_bf16 v[44:47], v[200:203], v[16:19], v[44:47]\n"
  "buffer_load_dwordx4 v0, s[36:39], s76 offen sc1 lds\n"
  "v_mfma_f32_16x16x32_bf16 v[56:59], v[200:203], v[20:23], v[56:59]\n"
  "v_mfma_f32_16x16x32_bf16 v[48:51], v[204:207], v[16:19], v[48:51]\n"
  "v_mfma_f32_16x16x32_bf16 v[60:63], v[204:207], v[20:23], v[60:63]\n"
  "s_waitcnt lgkmcnt(2)\n"
  "v_mfma_f32_16x16x32_bf16 v[40:43], v[208:211], v[24:27], v[40:43]\n"
  "ds_read_b128 v[16:19], v1 offset:61440\n"
  "s_add_u32 m0, s79, 24576\n"
  "s_add_u32 s76, s78, s10\n"
  "v_mfma_f32_16x16x32_bf16 v[52:55], v[208:211], v[28:31], v[52:55]\n"
  "ds_read_b128 v[20:23], v1 offset:61696\n"
  "buffer_load_dwordx4 v0, s[36:39], s76 offen sc1 lds\n"
  "v_mfma_f32_16x16x32_bf16 v[44:47], v[212:215], v[24:27], v[44:47]\n"
  "v_mfma_f32_16x16x32_bf16 v[56:59], v[212:215], v[28:31], v[56:59]\n"
  "v_mfma_f32_16x16x32_bf16 v[48:51], v[216:219], v[24:27], v[48:51]\n"
  "v_mfma_f32_16x16x32_bf16 v[60:63], v[216:219], v[28:31], v[60:63]\n"
  "s_add_u32 m0, s79, 28672\n"
  "s_add_u32 s76, s78, s11\n"
  "s_waitcnt lgkmcnt(2)\n"
  "v_mfma_f32_16x16x32_bf16 v[40:43], v[220:223], v[32:35], v[40:43]\n"
  "ds_read_b128 v[24:27], v1 offset:63488\n"
  "buffer_load_dwordx4 v0, s[36:39], s76 offen sc1 lds\n"
  "v_mfma_f32_16x16x32_bf16 v[52:55], v[220:223], v[36:39], v[52:55]\n"
  "ds_read_b128 v[28:31], v1 offset:63744\n"
  "v_mfma_f32_16x16x32_bf16 v[44:47], v[224:227], v[32:35], v[44:47]\n"
  "v_mfma_f32_16x16x32_bf16 v[56:59], v[224:227], v[36:39], v[56:59]\n"
  "v_mfma_f32_16x16x32_bf16 v[48:51], v[228:231], v[32:35], v[48:51]\n"
  "s_add_u32 m0, s79, 32768\n"
  "s_add_u32 s76, s78, s12\n"
  "v_mfma_f32_16x16x32_bf16 v[60:63], v[228:231], v[36:39], v[60:63]\n"
  "buffer_load_dwordx4 v0, s[36:39], s76 offen sc1 lds\n"
  "s_waitcnt lgkmcnt(2)\n"
  "v_mfma_f32_16x16x32_bf16 v[40:43], v[232:235], v[16:19], v[40:43]\n"
  "v_mfma_f32_16x16x32_bf16 v[52:55], v[232:235], v[20:23], v[52:55]\n"
  "v_mfma_f32_16x16x32_bf16 v[44:47], v[236:239], v[16:19], v[44:47]\n"
  "v_mfma_f32_16x16x32_bf16 v[56:59], v[236:239], v[20:23], v[56:59]\n"
  "v_mfma_f32_16x16x32_bf16 v[48:51], v[240:243], v[16:19], v[48:51]\n"
  "buffer_load_dword v4, v2, s[36:39], 0 offen sc1\n"
  "v_mfma_f32_16x16x32_bf16 v[60:63], v[240:243], v[20:23], v[60:63]\n"
  "s_waitcnt lgkmcnt(0)\n"
  "v_mfma_f32_16x16x32_bf16 v[40:43], v[244:247], v[24:27], v[40:43]\n"
  "v_mfma_f32_16x16x32_bf16 v[44:47], v[248:251], v[24:27], v[44:47]\n"
  "v_mfma_f32_16x16x32_bf16 v[48:51], v[252:255], v[24:27], v[48:51]\n"
  "v_mfma_f32_16x16x32_bf16 v[52:55], v[244:247], v[28:31], v[52:55]\n"
  "v_mfma_f32_16x16x32_bf16 v[56:59], v[248:251], v[28:31], v[56:59]\n"
  "v_mfma_f32_16x16x32_bf16 v[60:63], v[252:255], v[28:31], v[60:63]\n"
  "v_cvt_pk_bf16_f32 v64, v40, v41\n"
  "v_cvt_pk_bf16_f32 v65, v42, v43\n"
  "v_cvt_pk_bf16_f32 v66, v44, v45\n"
  "v_cvt_pk_bf16_f32 v67, v46, v47\n"
  "buffer_store_dwordx4 v[64:67], v7, s[36:39], s80 offen sc1\n"
  "v_cvt_pk_bf16_f32 v72, v48, v49\n"
  "v_cvt_pk_bf16_f32 v73, v50, v51\n"
  "v_cvt_pk_bf16_f32 v68, v52, v53\n"
  "v_cvt_pk_bf16_f32 v69, v54, v55\n"
  "v_cvt_pk_bf16_f32 v70, v56, v57\n"
  "v_cvt_pk_bf16_f32 v71, v58, v59\n"
  "v_cvt_pk_bf16_f32 v74, v60, v61\n"
  "v_cvt_pk_bf16_f32 v75, v62, v63\n"
  "buffer_store_dwordx4 v[68:71], v7, s[36:39], s80 offen offset:256 sc1\n"
  "s_nop 0\n"
  "v_permlane16_swap_b32 v72, v74\n"
  "v_permlane16_swap_b32 v73, v75\n"
  "buffer_store_dwordx4 v[72:75], v12, s[36:39], s80 offen sc1\n"
  "s_xor_b32 s81, s81, 65536\n"
  "v_xor_b32 v1, 65536, v1\n"
  "s_add_u32 s70, s70, 1\n"
  "s_mov_b32 s82, s71\n"
  "s_cmp_lg_u32 s71, 0\n"
  "s_cbranch_scc1 L_pf_%=\n"
  "s_waitcnt vmcnt(3)\n"
  "v_subrev_u32 v6, v3, v4\n"
  "s_add_u32 s74, s70, 0\n"
  "v_cmp_le_i32 vcc, s74, v6\n"
  "s_cmp_eq_u64 vcc, exec\n"
  "s_cbranch_scc0 L_idle_%=\n"
  "s_cmp_eq_u32 s70, 64\n"
  "s_cbranch_scc1 L_idle_%=\n"
  "s_add_u32 s78, s70, 0\n"
  "s_and_b32 s78, s78, s61\n"
  "s_mul_i32 s78, s78, s60\n"
  "s_lshr_b32 s76, s78, 8\n"
  "s_and_b32 s76, s76, 8192\n"
  "s_add_u32 s78, s78, s59\n"
  "s_xor_b32 s78, s78, s76\n"
  "s_add_u32 s79, s81, s57\n"
  "s_add_u32 m0, s79, 0\n"
  "s_add_u32 s76, s78, s4\n"
  "buffer_load_dwordx4 v0, s[36:39], s76 offen sc1 lds\n"
  "s_add_u32 m0, s79, 4096\n"
  "s_add_u32 s76, s78, s5\n"
  "buffer_load_dwordx4 v0, s[36:39], s76 offen sc1 lds\n"
  "s_add_u32 m0, s79, 8192\n"
  "s_add_u32 s76, s78, s6\n"
  "buffer_load_dwordx4 v0, s[36:39], s76 offen sc1 lds\n"
  "s_add_u32 m0, s79, 12288\n"
  "s_add_u32 s76, s78, s7\n"
  "buffer_load_dwordx4 v0, s[36:39], s76 offen sc1 lds\n"
  "s_add_u32 m0, s79, 16384\n"
  "s_add_u32 s76, s78, s8\n"
  "buffer_load_dwordx4 v0, s[36:39], s76 offen sc1 lds\n"
  "s_add_u32 m0, s79, 20480\n"
  "s_add_u32 s76, s78, s9\n"
  "buffer_load_dwordx4 v0, s[36:39], s76 offen sc1 lds\n"
  "s_add_u32 m0, s79, 24576\n"
  "s_add_u32 s76, s78, s10\n"
  "buffer_load_dwordx4 v0, s[36:39], s76 offen sc1 lds\n"
  "s_add_u32 m0, s79, 28672\n"
  "s_add_u32 s76, s78, s11\n"
  "buffer_load_dwordx4 v0, s[36:39], s76 offen sc1 lds\n"
  "s_add_u32 m0, s79, 32768\n"
  "s_add_u32 s76, s78, s12\n"
  "buffer_load_dwordx4 v0, s[36:39], s76 offen sc1 lds\n"
  "s_waitcnt vmcnt(9)\n"
  "s_add_u32 s75, s70, -1\n"
  "s_lshl_b32 s74, s75, 2\n"
  "s_add_u32 s74, s74, 163584\n"
  "v_mov_b32 v16, s74\n"
  "v_mov_b32 v6, 1\n"
  "s_mov_b64 exec, 1\n"
  "ds_add_rtn_u32 v16, v16, v6\n"
  "s_mov_b64 exec, -1\n"
  "v_mov_b32 v6, s75\n"
  "s_waitcnt lgkmcnt(0)\n"
  "v_readfirstlane_b32 s74, v16\n"
  "s_cmp_eq_u32 s74, 3\n"
  "s_cselect_b32 s74, s58, 0x80000000\n"
  "buffer_store_dword v6, v5, s[36:39], s74 offen sc1\n"
  "s_waitcnt vmcnt(6)\n"
  "s_branch L_go_%=\n"
  "L_idle_%=:\n"
  "buffer_load_dword v4, v2, s[36:39], 0 offen sc1\n"
  "s_waitcnt vmcnt(1)\n"
  "s_add_u32 s75, s70, -1\n"
  "s_lshl_b32 s74, s75, 2\n"
  "s_add_u32 s74, s74, 163584\n"
  "v_mov_b32 v16, s74\n"
  "v_mov_b32 v6, 1\n"
  "s_mov_b64 exec, 1\n"
  "ds_add_rtn_u32 v16, v16, v6\n"
  "s_mov_b64 exec, -1\n"
  "v_mov_b32 v6, s75\n"
  "s_waitcnt lgkmcnt(0)\n"
  "v_readfirstlane_b32 s74, v16\n"
  "s_cmp_eq_u32 s74, 3\n"
  "s_cselect_b32 s74, s58, 0x80000000\n"
  "buffer_store_dword v6, v5, s[36:39], s74 offen sc1\n"
  "s_waitcnt vmcnt(1)\n"
  "s_cmp_eq_u32 s70, 64\n"
  "s_cbranch_scc1 L_fin_%=\n"
  "v_subrev_u32 v6, v3, v4\n"
  "s_add_u32 s74, s70, 0\n"
  "v_cmp_le_i32 vcc, s74, v6\n"
  "s_cmp_eq_u64 vcc, exec\n"
  "s_cbranch_scc1 L_poll_x_%=\n"
  "L_first_%=:\n"
  "s_cmp_lg_u32 s72, 0\n"
  "s_cselect_b32 s73, 0, 32768\n"
  "v_add_u32 v6, s70, v3\n"
  "L_poll_%=:\n"
  "buffer_load_dword v4, v2, s[36:39], 0 offen sc1\n"
  "s_sub_u32 s73, s73, 1\n"
  "s_cselect_b32 s72, 1, s72\n"
  "s_cselect_b64 s[2:3], 0, -1\n"
  "s_waitcnt vmcnt(0)\n"
  "v_cmp_gt_i32 vcc, v6, v4\n"
  "s_and_b64 vcc, vcc, s[2:3]\n"
  "s_cbranch_scc1 L_poll_%=\n"
  "L_poll_x_%=:\n"
  "s_add_u32 s78, s70, 0\n"
  "s_and_b32 s78, s78, s61\n"
  "s_mul_i32 s78, s78, s60\n"
  "s_lshr_b32 s76, s78, 8\n"
  "s_and_b32 s76, s76, 8192\n"
  "s_add_u32 s78, s78, s59\n"
  "s_xor_b32 s78, s78, s76\n"
  "s_add_u32 s79, s81, s57\n"
  "s_add_u32 m0, s79, 0\n"
  "s_add_u32 s76, s78, s4\n"
  "buffer_load_dwordx4 v0, s[36:39], s76 offen sc1 lds\n"
  "s_add_u32 m0, s79, 4096\n"
  "s_add_u32 s76, s78, s5\n"
  "buffer_load_dwordx4 v0, s[36:39], s76 offen sc1 lds\n"
  "s_add_u32 m0, s79, 8192\n"
  "s_add_u32 s76, s78, s6\n"
  "buffer_load_dwordx4 v0, s[36:39], s76 offen sc1 lds\n"
  "s_add_u32 m0, s79, 12288\n"
  "s_add_u32 s76, s78, s7\n"
  "buffer_load_dwordx4 v0, s[36:39], s76 offen sc1 lds\n"
  "s_add_u32 m0, s79, 16384\n"
  "s_add_u32 s76, s78, s8\n"
  "buffer_load_dwordx4 v0, s[36:39], s76 offen sc1 lds\n"
  "s_add_u32 m0, s79, 20480\n"
  "s_add_u32 s76, s78, s9\n"
  "buffer_load_dwordx4 v0, s[36:39], s76 offen sc1 lds\n"
  "s_add_u32 m0, s79, 24576\n"
  "s_add_u32 s76, s78, s10\n"
  "buffer_load_dwordx4 v0, s[36:39], s76 offen sc1 lds\n"
  "s_add_u32 m0, s79, 28672\n"
  "s_add_u32 s76, s78, s11\n"
  "buffer_load_dwordx4 v0, s[36:39], s76 offen sc1 lds\n"
  "s_add_u32 m0, s79, 32768\n"
  "s_add_u32 s76, s78, s12\n"
  "buffer_load_dwordx4 v0, s[36:39], s76 offen sc1 lds\n"
  "s_waitcnt vmcnt(5)\n"
  "s_branch L_go_%=\n"
  "L_fin_%=:\n"
  "s_waitcnt vmcnt(0)\n"
  "L_c1_%=:\n"
  "s_endpgm\n"
      :
      : L_OPERANDS, "{v2}"(ln < 8 ? c.flag(F_XO + 8 * i + ln) : OOB), "{v3}"(ln < 8 ? 0 : NEVER), "{s[40:43]}"(mkrsrc(p.Wo, 4096 * 576 * 2)),
        "{s[64:67]}"(uni((v4u){L_OI / 1024 + 64u * i, 0u, 0u, 0u}))
      : OWNED);
}
DI void role_f2(Ctx& c, const Params& p, unsigned i, unsigned j) {
  const unsigned ln = (unsigned)c.lane, w = (unsigned)c.wave, g = ln >> 4, t = ln & 15u, q = (ln >> 2) & 3u;
  const unsigned k0 = i < 4 ? 29u * i : 115u;
  const unsigned row0 = 32u * k0, lpart = L_PF + i * (40 * KB), f0 = k0 >> 3, f1 = (k0 + 28u) >> 3;
  const unsigned fword = c.flag(F_F2 + 3 * i + j);
  const unsigned srcb = c.slab1(0), srcm = 4 * MB, srck = RING - 1, dstb = c.slab1(0);
  asm volatile(
  "s_getpc_b64 s[0:1]\n"
  "L_c0_%=:\n"
  "v_mbcnt_lo_u32_b32 v5, -1, 0\n"
  "v_mbcnt_hi_u32_b32 v5, -1, v5\n"
  "v_lshlrev_b32 v5, 4, v5\n"
  "v_add_lshl_u32 v5, v5, s57, 2\n"
  "v_min_u32 v5, L_c1_%=-L_c0_%=, v5\n"
  "global_load_dword v5, v5, s[0:1]\n"
  "s_nop 4\n"
  "s_add_u32 m0, s57, 0\n"
  "s_mov_b32 s74, 0\n"
  "buffer_load_dwordx4 v8, s[40:43], s74 offen lds\n"
  "s_add_u32 m0, s57, 4096\n"
  "s_mov_b32 s74, 18432\n"
  "buffer_load_dwordx4 v8, s[40:43], s74 offen lds\n"
  "s_add_u32 m0, s57, 8192\n"
  "s_mov_b32 s74, 0\n"
  "buffer_load_dwordx4 v9, s[40:43], s74 offen lds\n"
  "s_add_u32 m0, s57, 12288\n"
  "s_mov_b32 s74, 36864\n"
  "buffer_load_dwordx4 v8, s[40:43], s74 offen lds\n"
  "s_add_u32 m0, s57, 16384\n"
  "s_mov_b32 s74, 55296\n"
  "buffer_load_dwordx4 v8, s[40:43], s74 offen lds\n"
  "s_add_u32 m0, s57, 20480\n"
  "s_mov_b32 s74, 36864\n"
  "buffer_load_dwordx4 v9, s[40:43], s74 offen lds\n"
  "s_add_u32 m0, s57, 24576\n"
  "s_mov_b32 s74, 73728\n"
  "buffer_load_dwordx4 v8, s[40:43], s74 offen lds\n"
  "s_add_u32 m0, s57, 28672\n"
  "s_mov_b32 s74, 92160\n"
  "buffer_load_dwordx4 v8, s[40:43], s74 offen lds\n"
  "s_add_u32 m0, s57, 32768\n"
  "s_mov_b32 s74, 73728\n"
  "buffer_load_dwordx4 v9, s[40:43], s74 offen lds\n"
  "s_add_u32 m0, s57, 36864\n"
  "s_mov_b32 s74, 110592\n"
  "buffer_load_dwordx4 v8, s[40:43], s74 offen lds\n"
  "s_add_u32 m0, s57, 40960\n"
  "s_mov_b32 s74, 129024\n"
  "buffer_load_dwordx4 v8, s[40:43], s74 offen lds\n"
  "s_add_u32 m0, s57, 45056\n"
  "s_mov_b32 s74, 110592\n"
  "buffer_load_dwordx4 v9, s[40:43], s74 offen lds\n"
  "s_add_u32 m0, s57, 49152\n"
  "s_mov_b32 s74, 147456\n"
  "buffer_load_dwordx4 v8, s[40:43], s74 offen lds\n"
  "s_add_u32 m0, s57, 53248\n"
  "s_mov_b32 s74, 165888\n"
  "buffer_load_dwordx4 v8, s[40:43], s74 offen lds\n"
  "s_add_u32 m0, s57, 57344\n"
  "s_mov_b32 s74, 147456\n"
  "buffer_load_dwordx4 v9, s[40:43], s74 offen lds\n"
  "s_add_u32 m0, s57, 61440\n"
  "s_mov_b32 s74, 184320\n"
  "buffer_load_dwordx4 v8, s[40:43], s74 offen lds\n"
  "s_add_u32 m0, s57, 65536\n"
  "s_mov_b32 s74, 202752\n"
  "buffer_load_dwordx4 v8, s[40:43], s74 offen lds\n"
  "s_add_u32 m0, s57, 69632\n"
  "s_mov_b32 s74, 184320\n"
  "buffer_load_dwordx4 v9, s[40:43], s74 offen lds\n"
  "s_add_u32 m0, s57, 73728\n"
  "s_mov_b32 s74, 221184\n"
  "buffer_load_dwordx4 v8, s[40:43], s74 offen lds\n"
  "s_add_u32 m0, s57, 77824\n"
  "s_mov_b32 s74, 239616\n"
  "buffer_load_dwordx4 v8, s[40:43], s74 offen lds\n"
  "s_add_u32 m0, s57, 81920\n"
  "s_mov_b32 s74, 221184\n"
  "buffer_load_dwordx4 v9, s[40:43], s74 offen lds\n"
  "s_waitcnt vmcnt(18) lgkmcnt(0)\n"
  "s_barrier\n"
  "s_add_u32 m0, s57, 86016\n"
  "s_mov_b32 s74, 258048\n"
  "buffer_load_dwordx4 v8, s[40:43], s74 offen lds\n"
  "s_add_u32 m0, s57, 90112\n"
  "s_mov_b32 s74, 276480\n"
  "buffer_load_dwordx4 v8, s[40:43], s74 offen lds\n"
  "s_add_u32 m0, s57, 94208\n"
  "s_mov_b32 s74, 258048\n"
  "buffer_load_dwordx4 v9, s[40:43], s74 offen lds\n"
  "v_add_u32 v4, 0, v10\n"
  "v_add_u32 v6, 0, v11\n"
  "ds_read_b64_tr_b16 a[0:1], v4 offset:0\n"
  "ds_read_b64_tr_b16 a[2:3], v4 offset:1024\n"
  "ds_read_b64_tr_b16 a[4:5], v4 offset:8\n"
  "ds_read_b64_tr_b16 a[6:7], v4 offset:1032\n"
  "ds_read_b64_tr_b16 a[8:9], v6 offset:0\n"
  "ds_read_b64_tr_b16 a[10:11], v6 offset:512\n"
  "s_waitcnt vmcnt(18) lgkmcnt(0)\n"
  "s_barrier\n"
  "s_add_u32 m0, s57, 0\n"
  "s_mov_b32 s74, 294912\n"
  "buffer_load_dwordx4 v8, s[40:43], s74 offen lds\n"
  "s_add_u32 m0, s57, 4096\n"
  "s_mov_b32 s74, 313344\n"
  "buffer_load_dwordx4 v8, s[40:43], s74 offen lds\n"
  "s_add_u32 m0, s57, 8192\n"
  "s_mov_b32 s74, 294912\n"
  "buffer_load_dwordx4 v9, s[40:43], s74 offen lds\n"
  "v_add_u32 v4, 12288, v10\n"
  "v_add_u32 v6, 12288, v11\n"
  "ds_read_b64_tr_b16 a[12:13], v4 offset:0\n"
  "ds_read_b64_tr_b16 a[14:15], v4 offset:1024\n"
  "ds_read_b64_tr_b16 a[16:17], v4 offset:8\n"
  "ds_read_b64_tr_b16 a[18:19], v4 offset:1032\n"
  "ds_read_b64_tr_b16 a[20:21], v6 offset:0\n"
  "ds_read_b64_tr_b16 a[22:23], v6 offset:512\n"
  "s_waitcnt vmcnt(18) lgkmcnt(0)\n"
  "s_barrier\n"
  "s_add_u32 m0, s57, 12288\n"
  "s_mov_b32 s74, 331776\n"
  "buffer_load_dwordx4 v8, s[40:43], s74 offen lds\n"
  "s_add_u32 m0, s57, 16384\n"
  "s_mov_b32 s74, 350208\n"
  "buffer_load_dwordx4 v8, s[40:43], s74 offen lds\n"
  "s_add_u32 m0, s57, 20480\n"
  "s_mov_b32 s74, 331776\n"
  "buffer_load_dwordx4 v9, s[40:43], s74 offen lds\n"
  "v_add_u32 v4, 24576, v10\n"
  "v_add_u32 v6, 24576, v11\n"
  "ds_read_b64_tr_b16 a[24:25], v4 offset:0\n"
  "ds_read_b64_tr_b16 a[26:27], v4 offset:1024\n"
  "ds_read_b64_tr_b16 a[28:29], v4 offset:8\n"
  "ds_read_b64_tr_b16 a[30:31], v4 offset:1032\n"
  "ds_read_b64_tr_b16 a[32:33], v6 offset:0\n"
  "ds_read_b64_tr_b16 a[34:35], v6 offset:512\n"
  "s_waitcnt vmcnt(18) lgkmcnt(0)\n"
  "s_barrier\n"
  "s_add_u32 m0, s57, 24576\n"
  "s_mov_b32 s74, 368640\n"
  "buffer_load_dwordx4 v8, s[40:43], s74 offen lds\n"
  "s_add_u32 m0, s57, 28672\n"
  "s_mov_b32 s74, 387072\n"
  "buffer_load_dwordx4 v8, s[40:43], s74 offen lds\n"
  "s_add_u32 m0, s57, 32768\n"
  "s_mov_b32 s74, 368640\n"
  "buffer_load_dwordx4 v9, s[40:43], s74 offen lds\n"
  "v_add_u32 v4, 36864, v10\n"
  "v_add_u32 v6, 36864, v11\n"
  "ds_read_b64_tr_b16 a[36:37], v4 offset:0\n"
  "ds_read_b64_tr_b16 a[38:39], v4 offset:1024\n"
  "ds_read_b64_tr_b16 a[40:41], v4 offset:8\n"
  "ds_read_b64_tr_b16 a[42:43], v4 offset:1032\n"
  "ds_read_b64_tr_b16 a[44:45], v6 offset:0\n"
  "ds_read_b64_tr_b16 a[46:47], v6 offset:512\n"
  "s_waitcnt vmcnt(18) lgkmcnt(0)\n"
  "s_barrier\n"
  "s_add_u32 m0, s57, 36864\n"
  "s_mov_b32 s74, 405504\n"
  "buffer_load_dwordx4 v8, s[40:43], s74 offen lds\n"
  "s_add_u32 m0, s57, 40960\n"
  "s_mov_b32 s74, 423936\n"
  "buffer_load_dwordx4 v8, s[40:43], s74 offen lds\n"
  "s_add_u32 m0, s57, 45056\n"
  "s_mov_b32 s74, 405504\n"
  "buffer_load_dwordx4 v9, s[40:43], s74 offen lds\n"
  "v_add_u32 v4, 49152, v10\n"
  "v_add_u32 v6, 49152, v11\n"
  "ds_read_b64_tr_b16 a[48:49], v4 offset:0\n"
  "ds_read_b64_tr_b16 a[50:51], v4 offset:1024\n"
  "ds_read_b64_tr_b16 a[52:53], v4 offset:8\n"
  "ds_read_b64_tr_b16 a[54:55], v4 offset:1032\n"
  "ds_read_b64_tr_b16 a[56:57], v6 offset:0\n"
  "ds_read_b64_tr_b16 a[58:59], v6 offset:512\n"
  "s_waitcnt vmcnt(18) lgkmcnt(0)\n"
  "s_barrier\n"
  "s_add_u32 m0, s57, 49152\n"
  "s_mov_b32 s74, 442368\n"
  "buffer_load_dwordx4 v8, s[40:43], s74 offen lds\n"
  "s_add_u32 m0, s57, 53248\n"
  "s_mov_b32 s74, 460800\n"
  "buffer_load_dwordx4 v8, s[40:43], s74 offen lds\n"
  "s_add_u32 m0, s57, 57344\n"
  "s_mov_b32 s74, 442368\n"
  "buffer_load_dwordx4 v9, s[40:43], s74 offen lds\n"
  "v_add_u32 v4, 61440, v10\n"
  "v_add_u32 v6, 61440, v11\n"
  "ds_read_b64_tr_b16 a[60:61], v4 offset:0\n"
  "ds_read_b64_tr_b16 a[62:63], v4 offset:1024\n"
  "ds_read_b64_tr_b16 a[64:65], v4 offset:8\n"
  "ds_read_b64_tr_b16 a[66:67], v4 offset:1032\n"
  "ds_read_b64_tr_b16 a[68:69], v6 offset:0\n"
  "ds_read_b64_tr_b16 a[70:71], v6 offset:512\n"
  "s_waitcnt vmcnt(18) lgkmcnt(0)\n"
  "s_barrier\n"
  "s_add_u32 m0, s57, 61440\n"
  "s_mov_b32 s74, 479232\n"
  "buffer_load_dwordx4 v8, s[40:43], s74 offen lds\n"
  "s_add_u32 m0, s57, 65536\n"
  "s_mov_b32 s74, 497664\n"
  "buffer_load_dwordx4 v8, s[40:43], s74 offen lds\n"
  "s_add_u32 m0, s57, 69632\n"
  "s_mov_b32 s74, 479232\n"
  "buffer_load_dwordx4 v9, s[40:43], s74 offen lds\n"
  "v_add_u32 v4, 73728, v10\n"
  "v_add_u32 v6, 73728, v11\n"
  "ds_read_b64_tr_b16 a[72:73], v4 offset:0\n"
  "ds_read_b64_tr_b16 a[74:75], v4 offset:1024\n"
  "ds_read_b64_tr_b16 a[76:77], v4 offset:8\n"
  "ds_read_b64_tr_b16 a[78:79], v4 offset:1032\n"
  "ds_read_b64_tr_b16 a[80:81], v6 offset:0\n"
  "ds_read_b64_tr_b16 a[82:83], v6 offset:512\n"
  "s_waitcnt vmcnt(18) lgkmcnt(0)\n"
  "s_barrier\n"
  "s_add_u32 m0, s57, 73728\n"
  "s_mov_b32 s74, 516096\n"
  "buffer_load_dwordx4 v8, s[40:43], s74 offen lds\n"
  "s_add_u32 m0, s57, 77824\n"
  "s_mov_b32 s74, 534528\n"
  "buffer_load_dwordx4 v8, s[40:43], s74 offen lds\n"
  "s_add_u32 m0, s57, 81920\n"
  "s_mov_b32 s74, 516096\n"
  "buffer_load_dwordx4 v9, s[40:43], s74 offen lds\n"
  "v_add_u32 v4, 86016, v10\n"
  "v_add_u32 v6, 86016, v11\n"
  "ds_read_b64_tr_b16 a[84:85], v4 offset:0\n"
  "ds_read_b64_tr_b16 a[86:87], v4 offset:1024\n"
  "ds_read_b64_tr_b16 a[88:89], v4 offset:8\n"
  "ds_read_b64_tr_b16 a[90:91], v4 offset:1032\n"
  "ds_read_b64_tr_b16 a[92:93], v6 offset:0\n"
  "ds_read_b64_tr_b16 a[94:95], v6 offset:512\n"
  "s_waitcnt vmcnt(18) lgkmcnt(0)\n"
  "s_barrier\n"
  "s_add_u32 m0, s57, 86016\n"
  "s_mov_b32 s74, 552960\n"
  "buffer_load_dwordx4 v8, s[40:43], s74 offen lds\n"
  "s_add_u32 m0, s57, 90112\n"
  "s_mov_b32 s74, 571392\n"
  "buffer_load_dwordx4 v8, s[40:43], s74 offen lds\n"
  "s_add_u32 m0, s57, 94208\n"
  "s_mov_b32 s74, 552960\n"
  "buffer_load_dwordx4 v9, s[40:43], s74 offen lds\n"
  "v_add_u32 v4, 0, v10\n"
  "v_add_u32 v6, 0, v11\n"
  "ds_read_b64_tr_b16 a[96:97], v4 offset:0\n"
  "ds_read_b64_tr_b16 a[98:99], v4 offset:1024\n"
  "ds_read_b64_tr_b16 a[100:101], v4 offset:8\n"
  "ds_read_b64_tr_b16 a[102:103], v4 offset:1032\n"
  "ds_read_b64_tr_b16 a[104:105], v6 offset:0\n"
  "ds_read_b64_tr_b16 a[106:107], v6 offset:512\n"
  "s_waitcnt vmcnt(18) lgkmcnt(0)\n"
  "s_barrier\n"
  "s_add_u32 m0, s57, 0\n"
  "s_mov_b32 s74, 589824\n"
  "buffer_load_dwordx4 v8, s[40:43], s74 offen lds\n"
  "s_add_u32 m0, s57, 4096\n"
  "s_mov_b32 s74, 608256\n"
  "buffer_load_dwordx4 v8, s[40:43], s74 offen lds\n"
  "s_add_u32 m0, s57, 8192\n"
  "s_mov_b32 s74, 589824\n"
  "buffer_load_dwordx4 v9, s[40:43], s74 offen lds\n"
  "v_add_u32 v4, 12288, v10\n"
  "v_add_u32 v6, 12288, v11\n"
  "ds_read_b64_tr_b16 a[108:109], v4 offset:0\n"
  "ds_read_b64_tr_b16 a[110:111], v4 offset:1024\n"
  "ds_read_b64_tr_b16 a[112:113], v4 offset:8\n"
  "ds_read_b64_tr_b16 a[114:115], v4 offset:1032\n"
  "ds_read_b64_tr_b16 a[116:117], v6 offset:0\n"
  "ds_read_b64_tr_b16 a[118:119], v6 offset:512\n"
  "s_waitcnt vmcnt(18) lgkmcnt(0)\n"
  "s_barrier\n"
  "s_add_u32 m0, s57, 12288\n"
  "s_mov_b32 s74, 626688\n"
  "buffer_load_dwordx4 v8, s[40:43], s74 offen lds\n"
  "s_add_u32 m0, s57, 16384\n"
  "s_mov_b32 s74, 645120\n"
  "buffer_load_dwordx4 v8, s[40:43], s74 offen lds\n"
  "s_add_u32 m0, s57, 20480\n"
  "s_mov_b32 s74, 626688\n"
  "buffer_load_dwordx4 v9, s[40:43], s74 offen lds\n"
  "v_add_u32 v4, 24576, v10\n"
  "v_add_u32 v6, 24576, v11\n"
  "ds_read_b64_tr_b16 a[120:121], v4 offset:0\n"
  "ds_read_b64_tr_b16 a[122:123], v4 offset:1024\n"
  "ds_read_b64_tr_b16 a[124:125], v4 offset:8\n"
  "ds_read_b64_tr_b16 a[126:127], v4 offset:1032\n"
  "ds_read_b64_tr_b16 a[128:129], v6 offset:0\n"
  "ds_read_b64_tr_b16 a[130:131], v6 offset:512\n"
  "s_waitcnt vmcnt(18) lgkmcnt(0)\n"
  "s_barrier\n"
  "s_add_u32 m0, s57, 24576\n"
  "s_mov_b32 s74, 663552\n"
  "buffer_load_dwordx4 v8, s[40:43], s74 offen lds\n"
  "s_add_u32 m0, s57, 28672\n"
  "s_mov_b32 s74, 681984\n"
  "buffer_load_dwordx4 v8, s[40:43], s74 offen lds\n"
  "s_add_u32 m0, s57, 32768\n"
  "s_mov_b32 s74, 663552\n"
  "buffer_load_dwordx4 v9, s[40:43], s74 offen lds\n"
  "v_add_u32 v4, 36864, v10\n"
  "v_add_u32 v6, 36864, v11\n"
  "ds_read_b64_tr_b16 a[132:133], v4 offset:0\n"
  "ds_read_b64_tr_b16 a[134:135], v4 offset:1024\n"
  "ds_read_b64_tr_b16 a[136:137], v4 offset:8\n"
  "ds_read_b64_tr_b16 a[138:139], v4 offset:1032\n"
  "ds_read_b64_tr_b16 a[140:141], v6 offset:0\n"
  "ds_read_b64_tr_b16 a[142:143], v6 offset:512\n"
  "s_waitcnt vmcnt(18) lgkmcnt(0)\n"
  "s_barrier\n"
  "s_add_u32 m0, s57, 36864\n"
  "s_mov_b32 s74, 700416\n"
  "buffer_load_dwordx4 v8, s[40:43], s74 offen lds\n"
  "s_add_u32 m0, s57, 40960\n"
  "s_mov_b32 s74, 718848\n"
  "buffer_load_dwordx4 v8, s[40:43], s74 offen lds\n"
  "s_add_u32 m0, s57, 45056\n"
  "s_mov_b32 s74, 700416\n"
  "buffer_load_dwordx4 v9, s[40:43], s74 offen lds\n"
  "v_add_u32 v4, 49152, v10\n"
  "v_add_u32 v6, 49152, v11\n"
  "ds_read_b64_tr_b16 a[144:145], v4 offset:0\n"
  "ds_read_b64_tr_b16 a[146:147], v4 offset:1024\n"
  "ds_read_b64_tr_b16 a[148:149], v4 offset:8\n"
  "ds_read_b64_tr_b16 a[150:151], v4 offset:1032\n"
  "ds_read_b64_tr_b16 a[152:153], v6 offset:0\n"
  "ds_read_b64_tr_b16 a[154:155], v6 offset:512\n"
  "s_waitcnt vmcnt(18) lgkmcnt(0)\n"
  "s_barrier\n"
  "s_add_u32 m0, s57, 49152\n"
  "s_mov_b32 s74, 737280\n"
  "buffer_load_dwordx4 v8, s[40:43], s74 offen lds\n"
  "s_add_u32 m0, s57, 53248\n"
  "s_mov_b32 s74, 755712\n"
  "buffer_load_dwordx4 v8, s[40:43], s74 offen lds\n"
  "s_add_u32 m0, s57, 57344\n"
  "s_mov_b32 s74, 737280\n"
  "buffer_load_dwordx4 v9, s[40:43], s74 offen lds\n"
  "v_add_u32 v4, 61440, v10\n"
  "v_add_u32 v6, 61440, v11\n"
  "ds_read_b64_tr_b16 a[156:157], v4 offset:0\n"
  "ds_read_b64_tr_b16 a[158:159], v4 offset:1024\n"
  "ds_read_b64_tr_b16 a[160:161], v4 offset:8\n"
  "ds_read_b64_tr_b16 a[162:163], v4 offset:1032\n"
  "ds_read_b64_tr_b16 a[164:165], v6 offset:0\n"
  "ds_read_b64_tr_b16 a[166:167], v6 offset:512\n"
  "s_waitcnt vmcnt(18) lgkmcnt(0)\n"
  "s_barrier\n"
  "s_add_u32 m0, s57, 61440\n"
  "s_mov_b32 s74, 774144\n"
  "buffer_load_dwordx4 v8, s[40:43], s74 offen lds\n"
  "s_add_u32 m0, s57, 65536\n"
  "s_mov_b32 s74, 792576\n"
  "buffer_load_dwordx4 v8, s[40:43], s74 offen lds\n"
  "s_add_u32 m0, s57, 69632\n"
  "s_mov_b32 s74, 774144\n"
  "buffer_load_dwordx4 v9, s[40:43], s74 offen lds\n"
  "v_add_u32 v4, 73728, v10\n"
  "v_add_u32 v6, 73728, v11\n"
  "ds_read_b64_tr_b16 a[168:169], v4 offset:0\n"
  "ds_read_b64_tr_b16 a[170:171], v4 offset:1024\n"
  "ds_read_b64_tr_b16 a[172:173], v4 offset:8\n"
  "ds_read_b64_tr_b16 a[174:175], v4 offset:1032\n"
  "ds_read_b64_tr_b16 a[176:177], v6 offset:0\n"
  "ds_read_b64_tr_b16 a[178:179], v6 offset:512\n"
  "s_waitcnt vmcnt(18) lgkmcnt(0)\n"
  "s_barrier\n"
  "s_add_u32 m0, s57, 73728\n"
  "s_mov_b32 s74, 811008\n"
  "buffer_load_dwordx4 v8, s[40:43], s74 offen lds\n"
  "s_add_u32 m0, s57, 77824\n"
  "s_mov_b32 s74, 829440\n"
  "buffer_load_dwordx4 v8, s[40:43], s74 offen lds\n"
  "s_add_u32 m0, s57, 81920\n"
  "s_mov_b32 s74, 811008\n"
  "buffer_load_dwordx4 v9, s[40:43], s74 offen lds\n"
  "v_add_u32 v4, 86016, v10\n"
  "v_add_u32 v6, 86016, v11\n"
  "ds_read_b64_tr_b16 a[180:181], v4 offset:0\n"
  "ds_read_b64_tr_b16 a[182:183], v4 offset:1024\n"
  "ds_read_b64_tr_b16 a[184:185], v4 offset:8\n"
  "ds_read_b64_tr_b16 a[186:187], v4 offset:1032\n"
  "ds_read_b64_tr_b16 a[188:189], v6 offset:0\n"
  "ds_read_b64_tr_b16 a[190:191], v6 offset:512\n"
  "s_waitcnt vmcnt(18) lgkmcnt(0)\n"
  "s_barrier\n"
  "s_add_u32 m0, s57, 86016\n"
  "s_mov_b32 s74, 847872\n"
  "buffer_load_dwordx4 v8, s[40:43], s74 offen lds\n"
  "s_add_u32 m0, s57, 90112\n"
  "s_mov_b32 s74, 866304\n"
  "buffer_load_dwordx4 v8, s[40:43], s74 offen lds\n"
  "s_add_u32 m0, s57, 94208\n"
  "s_mov_b32 s74, 847872\n"
  "buffer_load_dwordx4 v9, s[40:43], s74 offen lds\n"
  "v_add_u32 v4, 0, v10\n"
  "v_add_u32 v6, 0, v11\n"
  "ds_read_b64_tr_b16 a[192:193], v4 offset:0\n"
  "ds_read_b64_tr_b16 a[194:195], v4 offset:1024\n"
  "ds_read_b64_tr_b16 a[196:197], v4 offset:8\n"
  "ds_read_b64_tr_b16 a[198:199], v4 offset:1032\n"
  "ds_read_b64_tr_b16 a[200:201], v6 offset:0\n"
  "ds_read_b64_tr_b16 a[202:203], v6 offset:512\n"
  "s_waitcnt vmcnt(18) lgkmcnt(0)\n"
  "s_barrier\n"
  "s_add_u32 m0, s57, 0\n"
  "s_mov_b32 s74, 884736\n"
  "buffer_load_dwordx4 v8, s[40:43], s74 offen lds\n"
  "s_add_u32 m0, s57, 4096\n"
  "s_mov_b32 s74, 903168\n"
  "buffer_load_dwordx4 v8, s[40:43], s74 offen lds\n"
  "s_add_u32 m0, s57, 8192\n"
  "s_mov_b32 s74, 884736\n"
  "buffer_load_dwordx4 v9, s[40:43], s74 offen lds\n"
  "v_add_u32 v4, 12288, v10\n"
  "v_add_u32 v6, 12288, v11\n"
  "ds_read_b64_tr_b16 a[204:205], v4 offset:0\n"
  "ds_read_b64_tr_b16 a[206:207], v4 offset:1024\n"
  "ds_read_b64_tr_b16 a[208:209], v4 offset:8\n"
  "ds_read_b64_tr_b16 a[210:211], v4 offset:1032\n"
  "ds_read_b64_tr_b16 a[212:213], v6 offset:0\n"
  "ds_read_b64_tr_b16 a[214:215], v6 offset:512\n"
  "s_waitcnt vmcnt(18) lgkmcnt(0)\n"
  "s_barrier\n"
  "s_add_u32 m0, s57, 12288\n"
  "s_mov_b32 s74, 921600\n"
  "buffer_load_dwordx4 v8, s[40:43], s74 offen lds\n"
  "s_add_u32 m0, s57, 16384\n"
  "s_mov_b32 s74, 940032\n"
  "buffer_load_dwordx4 v8, s[40:43], s74 offen lds\n"
  "s_add_u32 m0, s57, 20480\n"
  "s_mov_b32 s74, 921600\n"
  "buffer_load_dwordx4 v9, s[40:43], s74 offen lds\n"
  "v_add_u32 v4, 24576, v10\n"
  "v_add_u32 v6, 24576, v11\n"
  "ds_read_b64_tr_b16 a[216:217], v4 offset:0\n"
  "ds_read_b64_tr_b16 a[218:219], v4 offset:1024\n"
  "ds_read_b64_tr_b16 a[220:221], v4 offset:8\n"
  "ds_read_b64_tr_b16 a[222:223], v4 offset:1032\n"
  "ds_read_b64_tr_b16 a[224:225], v6 offset:0\n"
  "ds_read_b64_tr_b16 a[226:227], v6 offset:512\n"
  "s_waitcnt vmcnt(18) lgkmcnt(0)\n"
  "s_barrier\n"
  "s_add_u32 m0, s57, 24576\n"
  "s_mov_b32 s74, 958464\n"
  "buffer_load_dwordx4 v8, s[40:43], s74 offen lds\n"
  "s_add_u32 m0, s57, 28672\n"
  "s_mov_b32 s74, 976896\n"
  "buffer_load_dwordx4 v8, s[40:43], s74 offen lds\n"
  "s_add_u32 m0, s57, 32768\n"
  "s_mov_b32 s74, 958464\n"
  "buffer_load_dwordx4 v9, s[40:43], s74 offen lds\n"
  "v_add_u32 v4, 36864, v10\n"
  "v_add_u32 v6, 36864, v11\n"
  "ds_read_b64_tr_b16 a[228:229], v4 offset:0\n"
  "ds_read_b64_tr_b16 a[230:231], v4 offset:1024\n"
  "ds_read_b64_tr_b16 a[232:233], v4 offset:8\n"
  "ds_read_b64_tr_b16 a[234:235], v4 offset:1032\n"
  "ds_read_b64_tr_b16 a[236:237], v6 offset:0\n"
  "ds_read_b64_tr_b16 a[238:239], v6 offset:512\n"
  "s_waitcnt vmcnt(18) lgkmcnt(0)\n"
  "s_barrier\n"
  "s_add_u32 m0, s57, 36864\n"
  "s_mov_b32 s74, 995328\n"
  "buffer_load_dwordx4 v8, s[40:43], s74 offen lds\n"
  "s_add_u32 m0, s57, 40960\n"
  "s_mov_b32 s74, 1013760\n"
  "buffer_load_dwordx4 v8, s[40:43], s74 offen lds\n"
  "s_add_u32 m0, s57, 45056\n"
  "s_mov_b32 s74, 995328\n"
  "buffer_load_dwordx4 v9, s[40:43], s74 offen lds\n"
  "v_add_u32 v4, 49152, v10\n"
  "v_add_u32 v6, 49152, v11\n"
  "ds_read_b64_tr_b16 a[240:241], v4 offset:0\n"
  "ds_read_b64_tr_b16 a[242:243], v4 offset:1024\n"
  "ds_read_b64_tr_b16 a[244:245], v4 offset:8\n"
  "ds_read_b64_tr_b16 a[246:247], v4 offset:1032\n"
  "ds_read_b64_tr_b16 a[248:249], v6 offset:0\n"
  "ds_read_b64_tr_b16 a[250:251], v6 offset:512\n"
  "s_waitcnt vmcnt(18) lgkmcnt(0)\n"
  "s_barrier\n"
  "s_add_u32 m0, s57, 49152\n"
  "s_mov_b32 s74, 1032192\n"
  "buffer_load_dwordx4 v8, s[40:43], s74 offen lds\n"
  "s_add_u32 m0, s57, 53248\n"
  "s_mov_b32 s74, 1050624\n"
  "buffer_load_dwordx4 v8, s[40:43], s74 offen lds\n"
  "s_add_u32 m0, s57, 57344\n"
  "s_mov_b32 s74, 1032192\n"
  "buffer_load_dwordx4 v9, s[40:43], s74 offen lds\n"
  "v_add_u32 v4, 61440, v10\n"
  "v_add_u32 v6, 61440, v11\n"
  "ds_read_b64_tr_b16 a[252:253], v4 offset:0\n"
  "ds_read_b64_tr_b16 a[254:255], v4 offset:1024\n"
  "ds_read_b64_tr_b16 v[128:129], v4 offset:8\n"
  "ds_read_b64_tr_b16 v[130:131], v4 offset:1032\n"
  "ds_read_b64_tr_b16 v[132:133], v6 offset:0\n"
  "ds_read_b64_tr_b16 v[134:135], v6 offset:512\n"
  "s_waitcnt vmcnt(18) lgkmcnt(0)\n"
  "s_barrier\n"
  "v_add_u32 v4, 73728, v10\n"
  "v_add_u32 v6, 73728, v11\n"
  "ds_read_b64_tr_b16 v[136:137], v4 offset:0\n"
  "ds_read_b64_tr_b16 v[138:139], v4 offset:1024\n"
  "ds_read_b64_tr_b16 v[140:141], v4 offset:8\n"
  "ds_read_b64_tr_b16 v[142:143], v4 offset:1032\n"
  "ds_read_b64_tr_b16 v[144:145], v6 offset:0\n"
  "ds_read_b64_tr_b16 v[146:147], v6 offset:512\n"
  "s_waitcnt vmcnt(15) lgkmcnt(0)\n"
  "s_barrier\n"
  "v_add_u32 v4, 86016, v10\n"
  "v_add_u32 v6, 86016, v11\n"
  "ds_read_b64_tr_b16 v[148:149], v4 offset:0\n"
  "ds_read_b64_tr_b16 v[150:151], v4 offset:1024\n"
  "ds_read_b64_tr_b16 v[152:153], v4 offset:8\n"
  "ds_read_b64_tr_b16 v[154:155], v4 offset:1032\n"
  "ds_read_b64_tr_b16 v[156:157], v6 offset:0\n"
  "ds_read_b64_tr_b16 v[158:159], v6 offset:512\n"
  "s_waitcnt vmcnt(12) lgkmcnt(0)\n"
  "s_barrier\n"
  "v_add_u32 v4, 0, v10\n"
  "v_add_u32 v6, 0, v11\n"
  "ds_read_b64_tr_b16 v[160:161], v4 offset:0\n"
  "ds_read_b64_tr_b16 v[162:163], v4 offset:1024\n"
  "ds_read_b64_tr_b16 v[164:165], v4 offset:8\n"
  "ds_read_b64_tr_b16 v[166:167], v4 offset:1032\n"
  "ds_read_b64_tr_b16 v[168:169], v6 offset:0\n"
  "ds_read_b64_tr_b16 v[170:171], v6 offset:512\n"
  "s_waitcnt vmcnt(9) lgkmcnt(0)\n"
  "s_barrier\n"
  "v_add_u32 v4, 12288, v10\n"
  "v_add_u32 v6, 12288, v11\n"
  "ds_read_b64_tr_b16 v[172:173], v4 offset:0\n"
  "ds_read_b64_tr_b16 v[174:175], v4 offset:1024\n"
  "ds_read_b64_tr_b16 v[176:177], v4 offset:8\n"
  "ds_read_b64_tr_b16 v[178:179], v4 offset:1032\n"
  "ds_read_b64_tr_b16 v[180:181], v6 offset:0\n"
  "ds_read_b64_tr_b16 v[182:183], v6 offset:512\n"
  "s_waitcnt vmcnt(6) lgkmcnt(0)\n"
  "s_barrier\n"
  "v_add_u32 v4, 24576, v10\n"
  "v_add_u32 v6, 24576, v11\n"
  "ds_read_b64_tr_b16 v[184:185], v4 offset:0\n"
  "ds_read_b64_tr_b16 v[186:187], v4 offset:1024\n"
  "ds_read_b64_tr_b16 v[188:189], v4 offset:8\n"
  "ds_read_b64_tr_b16 v[190:191], v4 offset:1032\n"
  "ds_read_b64_tr_b16 v[192:193], v6 offset:0\n"
  "ds_read_b64_tr_b16 v[194:195], v6 offset:512\n"
  "s_waitcnt vmcnt(3) lgkmcnt(0)\n"
  "s_barrier\n"
  "v_add_u32 v4, 36864, v10\n"
  "v_add_u32 v6, 36864, v11\n"
  "ds_read_b64_tr_b16 v[196:197], v4 offset:0\n"
  "ds_read_b64_tr_b16 v[198:199], v4 offset:1024\n"
  "ds_read_b64_tr_b16 v[200:201], v4 offset:8\n"
  "ds_read_b64_tr_b16 v[202:203], v4 offset:1032\n"
  "ds_read_b64_tr_b16 v[204:205], v6 offset:0\n"
  "ds_read_b64_tr_b16 v[206:207], v6 offset:512\n"
  "s_waitcnt vmcnt(0) lgkmcnt(0)\n"
  "s_barrier\n"
  "v_add_u32 v4, 49152, v10\n"
  "v_add_u32 v6, 49152, v11\n"
  "ds_read_b64_tr_b16 v[208:209], v4 offset:0\n"
  "ds_read_b64_tr_b16 v[210:211], v4 offset:1024\n"
  "ds_read_b64_tr_b16 v[212:213], v4 offset:8\n"
  "ds_read_b64_tr_b16 v[214:215], v4 offset:1032\n"
  "ds_read_b64_tr_b16 v[216:217], v6 offset:0\n"
  "ds_read_b64_tr_b16 v[218:219], v6 offset:512\n"
  "s_waitcnt lgkmcnt(0)\n"
  "s_cmp_eq_u32 s65, 0\n"
  "s_cbranch_scc1 L_nz_%=\n"
  "v_accvgpr_write_b32 a0, 0\n"
  "v_accvgpr_write_b32 a1, 0\n"
  "v_accvgpr_write_b32 a2, 0\n"
  "v_accvgpr_write_b32 a3, 0\n"
  "v_accvgpr_write_b32 a4, 0\n"
  "v_accvgpr_write_b32 a5, 0\n"
  "v_accvgpr_write_b32 a6, 0\n"
  "v_accvgpr_write_b32 a7, 0\n"
  "v_accvgpr_write_b32 a8, 0\n"
  "v_accvgpr_write_b32 a9, 0\n"
  "v_accvgpr_write_b32 a10, 0\n"
  "v_accvgpr_write_b32 a11, 0\n"
  "L_nz_%=:\n"
  "s_lshr_b32 s75, s57, 10\n"
  "s_add_u32 s74, s75, 0\n"
  "s_min_u32 s74, s74, 57\n"
  "s_add_u32 s74, s74, s64\n"
  "s_lshr_b32 s76, s74, 3\n"
  "s_lshl_b32 s76, s76, 14\n"
  "s_and_b32 s74, s74, 7\n"
  "s_lshl_b32 s74, s74, 10\n"
  "s_or_b32 s4, s74, s76\n"
  "s_add_u32 s74, s75, 4\n"
  "s_min_u32 s74, s74, 57\n"
  "s_add_u32 s74, s74, s64\n"
  "s_lshr_b32 s76, s74, 3\n"
  "s_lshl_b32 s76, s76, 14\n"
  "s_and_b32 s74, s74, 7\n"
  "s_lshl_b32 s74, s74, 10\n"
  "s_or_b32 s5, s74, s76\n"
  "s_add_u32 s74, s75, 8\n"
  "s_min_u32 s74, s74, 57\n"
  "s_add_u32 s74, s74, s64\n"
  "s_lshr_b32 s76, s74, 3\n"
  "s_lshl_b32 s76, s76, 14\n"
  "s_and_b32 s74, s74, 7\n"
  "s_lshl_b32 s74, s74, 10\n"
  "s_or_b32 s6, s74, s76\n"
  "s_add_u32 s74, s75, 12\n"
  "s_min_u32 s74, s74, 57\n"
  "s_add_u32 s74, s74, s64\n"
  "s_lshr_b32 s76, s74, 3\n"
  "s_lshl_b32 s76, s76, 14\n"
  "s_and_b32 s74, s74, 7\n"
  "s_lshl_b32 s74, s74, 10\n"
  "s_or_b32 s7, s74, s76\n"
  "s_add_u32 s74, s75, 16\n"
  "s_min_u32 s74, s74, 57\n"
  "s_add_u32 s74, s74, s64\n"
  "s_lshr_b32 s76, s74, 3\n"
  "s_lshl_b32 s76, s76, 14\n"
  "s_and_b32 s74, s74, 7\n"
  "s_lshl_b32 s74, s74, 10\n"
  "s_or_b32 s8, s74, s76\n"
  "s_add_u32 s74, s75, 20\n"
  "s_min_u32 s74, s74, 57\n"
  "s_add_u32 s74, s74, s64\n"
  "s_lshr_b32 s76, s74, 3\n"
  "s_lshl_b32 s76, s76, 14\n"
  "s_and_b32 s74, s74, 7\n"
  "s_lshl_b32 s74, s74, 10\n"
  "s_or_b32 s9, s74, s76\n"
  "s_add_u32 s74, s75, 24\n"
  "s_min_u32 s74, s74, 57\n"
  "s_add_u32 s74, s74, s64\n"
  "s_lshr_b32 s76, s74, 3\n"
  "s_lshl_b32 s76, s76, 14\n"
  "s_and_b32 s74, s74, 7\n"
  "s_lshl_b32 s74, s74, 10\n"
  "s_or_b32 s10, s74, s76\n"
  "s_add_u32 s74, s75, 28\n"
  "s_min_u32 s74, s74, 57\n"
  "s_add_u32 s74, s74, s64\n"
  "s_lshr_b32 s76, s74, 3\n"
  "s_lshl_b32 s76, s76, 14\n"
  "s_and_b32 s74, s74, 7\n"
  "s_lshl_b32 s74, s74, 10\n"
  "s_or_b32 s11, s74, s76\n"
  "s_add_u32 s74, s75, 32\n"
  "s_min_u32 s74, s74, 57\n"
  "s_add_u32 s74, s74, s64\n"
  "s_lshr_b32 s76, s74, 3\n"
  "s_lshl_b32 s76, s76, 14\n"
  "s_and_b32 s74, s74, 7\n"
  "s_lshl_b32 s74, s74, 10\n"
  "s_or_b32 s12, s74, s76\n"
  "s_add_u32 s74, s75, 36\n"
  "s_min_u32 s74, s74, 57\n"
  "s_add_u32 s74, s74, s64\n"
  "s_lshr_b32 s76, s74, 3\n"
  "s_lshl_b32 s76, s76, 14\n"
  "s_and_b32 s74, s74, 7\n"
  "s_lshl_b32 s74, s74, 10\n"
  "s_or_b32 s13, s74, s76\n"
  "s_add_u32 s74, s75, 40\n"
  "s_min_u32 s74, s74, 57\n"
  "s_add_u32 s74, s74, s64\n"
  "s_lshr_b32 s76, s74, 3\n"
  "s_lshl_b32 s76, s76, 14\n"
  "s_and_b32 s74, s74, 7\n"
  "s_lshl_b32 s74, s74, 10\n"
  "s_or_b32 s14, s74, s76\n"
  "s_add_u32 s74, s75, 44\n"
  "s_min_u32 s74, s74, 57\n"
  "s_add_u32 s74, s74, s64\n"
  "s_lshr_b32 s76, s74, 3\n"
  "s_lshl_b32 s76, s76, 14\n"
  "s_and_b32 s74, s74, 7\n"
  "s_lshl_b32 s74, s74, 10\n"
  "s_or_b32 s15, s74, s76\n"
  "s_add_u32 s74, s75, 48\n"
  "s_min_u32 s74, s74, 57\n"
  "s_add_u32 s74, s74, s64\n"
  "s_lshr_b32 s76, s74, 3\n"
  "s_lshl_b32 s76, s76, 14\n"
  "s_and_b32 s74, s74, 7\n"
  "s_lshl_b32 s74, s74, 10\n"
  "s_or_b32 s16, s74, s76\n"
  "s_add_u32 s74, s75, 52\n"
  "s_min_u32 s74, s74, 57\n"
  "s_add_u32 s74, s74, s64\n"
  "s_lshr_b32 s76, s74, 3\n"
  "s_lshl_b32 s76, s76, 14\n"
  "s_and_b32 s74, s74, 7\n"
  "s_lshl_b32 s74, s74, 10\n"
  "s_or_b32 s17, s74, s76\n"
  "s_add_u32 s74, s75, 56\n"
  "s_min_u32 s74, s74, 57\n"
  "s_add_u32 s74, s74, s64\n"
  "s_lshr_b32 s76, s74, 3\n"
  "s_lshl_b32 s76, s76, 14\n"
  "s_and_b32 s74, s74, 7\n"
  "s_lshl_b32 s74, s74, 10\n"
  "s_or_b32 s18, s74, s76\n"
  "s_barrier\n"
  "v_lshrrev_b32 v4, 2, v0\n"
  "v_add_u32 v4, 163584, v4\n"
  "v_mov_b32 v6, 0\n"
  "ds_write_b32 v4, v6\n"
  "v_min_u32 v5, 1, v0\n"
  "v_lshlrev_b32 v5, 30, v5\n"
  "s_mov_b32 s70, 0\n"
  "s_mov_b32 s71, 0\n"
  "s_mov_b32 s72, 0\n"
  "s_mov_b32 s81, 0\n"
  "s_mov_b32 s82, 0\n"
  "s_mov_b32 s83, 0\n"
  "s_mov_b32 s95, -1\n"
  "s_cmp_eq_u32 s57, 0\n"
  "s_cselect_b32 s92, s58, 0x80000000\n"
  "s_waitcnt lgkmcnt(0)\n"
  "s_barrier\n"
  "s_branch L_first_%=\n"
  "L_pf_%=:\n"
  "s_waitcnt vmcnt(9)\n"
  "L_go_%=:\n"
  "s_barrier\n"
  "ds_read_b128 v[16:19], v1 offset:0\n"
  "ds_read_b128 v[20:23], v1 offset:256\n"
  "ds_read_b128 v[24:27], v1 offset:2048\n"
  "ds_read_b128 v[28:31], v1 offset:2304\n"
  "s_waitcnt lgkmcnt(3)\n"
  "v_mfma_f32_16x16x32_bf16 v[40:43], a[0:3], v[16:19], 0\n"
  "ds_read_b128 v[32:35], v1 offset:4096\n"
  "s_waitcnt lgkmcnt(3)\n"
  "v_mfma_f32_16x16x32_bf16 v[52:55], a[0:3], v[20:23], 0\n"
  "ds_read_b128 v[36:39], v1 offset:4352\n"
  "v_mfma_f32_16x16x32_bf16 v[44:47], a[4:7], v[16:19], 0\n"
  "v_mfma_f32_16x16x32_bf16 v[56:59], a[4:7], v[20:23], 0\n"
  "v_mfma_f32_16x16x32_bf16 v[48:51], a[8:11], v[16:19], 0\n"
  "v_mfma_f32_16x16x32_bf16 v[60:63], a[8:11], v[20:23], 0\n"
  "s_waitcnt lgkmcnt(2)\n"
  "v_mfma_f32_16x16x32_bf16 v[40:43], a[12:15], v[24:27], v[40:43]\n"
  "ds_read_b128 v[16:19], v1 offset:6144\n"
  "v_mfma_f32_16x16x32_bf16 v[52:55], a[12:15], v[28:31], v[52:55]\n"
  "ds_read_b128 v[20:23], v1 offset:6400\n"
  "v_mfma_f32_16x16x32_bf16 v[44:47], a[16:19], v[24:27], v[44:47]\n"
  "s_add_u32 m0, s79, 36864\n"
  "s_add_u32 s76, s78, s13\n"
  "v_mfma_f32_16x16x32_bf16 v[56:59], a[16:19], v[28:31], v[56:59]\n"
  "buffer_load_dwordx4 v0, s[36:39], s76 offen sc1 lds\n"
  "v_mfma_f32_16x16x32_bf16 v[48:51], a[20:23], v[24:27], v[48:51]\n"
  "v_mfma_f32_16x16x32_bf16 v[60:63], a[20:23], v[28:31], v[60:63]\n"
  "s_waitcnt lgkmcnt(2)\n"
  "v_mfma_f32_16x16x32_bf16 v[40:43], a[24:27], v[32:35], v[40:43]\n"
  "ds_read_b128 v[24:27], v1 offset:8192\n"
  "v_mfma_f32_16x16x32_bf16 v[52:55], a[24:27], v[36:39], v[52:55]\n"
  "ds_read_b128 v[28:31], v1 offset:8448\n"
  "s_add_u32 m0, s79, 40960\n"
  "s_add_u32 s76, s78, s14\n"
  "v_mfma_f32_16x16x32_bf16 v[44:47], a[28:31], v[32:35], v[44:47]\n"
  "buffer_load_dwordx4 v0, s[36:39], s76 offen sc1 lds\n"
  "v_mfma_f32_16x16x32_bf16 v[56:59], a[28:31], v[36:39], v[56:59]\n"
  "v_mfma_f32_16x16x32_bf16 v[48:51], a[32:35], v[32:35], v[48:51]\n"
  "v_mfma_f32_16x16x32_bf16 v[60:63], a[32:35], v[36:39], v[60:63]\n"
  "s_waitcnt lgkmcnt(2)\n"
  "v_mfma_f32_16x16x32_bf16 v[40:43], a[36:39], v[16:19], v[40:43]\n"
  "ds_read_b128 v[32:35], v1 offset:10240\n"
  "s_add_u32 m0, s79, 45056\n"
  "s_add_u32 s76, s78, s15\n"
  "v_mfma_f32_16x16x32_bf16 v[52:55], a[36:39], v[20:23], v[52:55]\n"
  "ds_read_b128 v[36:39], v1 offset:10496\n"
  "buffer_load_dwordx4 v0, s[36:39], s76 offen sc1 lds\n"
  "v_mfma_f32_16x16x32_bf16 v[44:47], a[40:43], v[16:19], v[44:47]\n"
  "v_mfma_f32_16x16x32_bf16 v[56:59], a[40:43], v[20:23], v[56:59]\n"
  "v_mfma_f32_16x16x32_bf16 v[48:51], a[44:47], v[16:19], v[48:51]\n"
  "v_mfma_f32_16x16x32_bf16 v[60:63], a[44:47], v[20:23], v[60:63]\n"
  "s_add_u32 m0, s79, 49152\n"
  "s_add_u32 s76, s78, s16\n"
  "s_waitcnt lgkmcnt(2)\n"
  "v_mfma_f32_16x16x32_bf16 v[40:43], a[48:51], v[24:27], v[40:43]\n"
  "ds_read_b128 v[16:19], v1 offset:12288\n"
  "buffer_load_dwordx4 v0, s[36:39], s76 offen sc1 lds\n"
  "v_mfma_f32_16x16x32_bf16 v[52:55], a[48:51], v[28:31], v[52:55]\n"
  "ds_read_b128 v[20:23], v1 offset:12544\n"
  "v_mfma_f32_16x16x32_bf16 v[44:47], a[52:55], v[24:27], v[44:47]\n"
  "v_mfma_f32_16x16x32_bf16 v[56:59], a[52:55], v[28:31], v[56:59]\n"
  "v_mfma_f32_16x16x32_bf16 v[48:51], a[56:59], v[24:27], v[48:51]\n"
  "s_add_u32 m0, s79, 53248\n"
  "s_add_u32 s76, s78, s17\n"
  "v_mfma_f32_16x16x32_bf16 v[60:63], a[56:59], v[28:31], v[60:63]\n"
  "buffer_load_dwordx4 v0, s[36:39], s76 offen sc1 lds\n"
  "s_waitcnt lgkmcnt(2)\n"
  "v_mfma_f32_16x16x32_bf16 v[40:43], a[60:63], v[32:35], v[40:43]\n"
  "ds_read_b128 v[24:27], v1 offset:14336\n"
  "v_mfma_f32_16x16x32_bf16 v[52:55], a[60:63], v[36:39], v[52:55]\n"
  "ds_read_b128 v[28:31], v1 offset:14592\n"
  "v_mfma_f32_16x16x32_bf16 v[44:47], a[64:67], v[32:35], v[44:47]\n"
  "s_add_u32 s75, s70, -1\n"
  "v_mov_b32 v6, s75\n"
  "v_mfma_f32_16x16x32_bf16 v[56:59], a[64:67], v[36:39], v[56:59]\n"
  "s_add_u32 m0, s79, 57344\n"
  "s_add_u32 s76, s78, s18\n"
  "v_mfma_f32_16x16x32_bf16 v[48:51], a[68:71], v[32:35], v[48:51]\n"
  "buffer_load_dwordx4 v0, s[36:39], s76 offen sc1 lds\n"
  "v_mfma_f32_16x16x32_bf16 v[60:63], a[68:71], v[36:39], v[60:63]\n"
  "s_waitcnt vmcnt(6)\n"
  "s_barrier\n"
  "s_waitcnt lgkmcnt(2)\n"
  "v_mfma_f32_16x16x32_bf16 v[40:43], a[72:75], v[16:19], v[40:43]\n"
  "ds_read_b128 v[32:35], v1 offset:16384\n"
  "v_mfma_f32_16x16x32_bf16 v[52:55], a[72:75], v[20:23], v[52:55]\n"
  "ds_read_b128 v[36:39], v1 offset:16640\n"
  "v_mfma_f32_16x16x32_bf16 v[44:47], a[76:79], v[16:19], v[44:47]\n"
  "buffer_store_dword v6, v5, s[36:39], s92 offen sc1\n"
  "v_mfma_f32_16x16x32_bf16 v[56:59], a[76:79], v[20:23], v[56:59]\n"
  "v_mfma_f32_16x16x32_bf16 v[48:51], a[80:83], v[16:19], v[48:51]\n"
  "v_mfma_f32_16x16x32_bf16 v[60:63], a[80:83], v[20:23], v[60:63]\n"
  "s_waitcnt lgkmcnt(2)\n"
  "v_mfma_f32_16x16x32_bf16 v[40:43], a[84:87], v[24:27], v[40:43]\n"
  "ds_read_b128 v[16:19], v1 offset:18432\n"
  "v_mfma_f32_16x16x32_bf16 v[52:55], a[84:87], v[28:31], v[52:55]\n"
  "ds_read_b128 v[20:23], v1 offset:18688\n"
  "v_mfma_f32_16x16x32_bf16 v[44:47], a[88:91], v[24:27], v[44:47]\n"
  "v_mfma_f32_16x16x32_bf16 v[56:59], a[88:91], v[28:31], v[56:59]\n"
  "v_mfma_f32_16x16x32_bf16 v[48:51], a[92:95], v[24:27], v[48:51]\n"
  "v_mfma_f32_16x16x32_bf16 v[60:63], a[92:95], v[28:31], v[60:63]\n"
  "s_waitcnt lgkmcnt(2)\n"
  "v_mfma_f32_16x16x32_bf16 v[40:43], a[96:99], v[32:35], v[40:43]\n"
  "ds_read_b128 v[24:27], v1 offset:20480\n"
  "v_mfma_f32_16x16x32_bf16 v[52:55], a[96:99], v[36:39], v[52:55]\n"
  "ds_read_b128 v[28:31], v1 offset:20736\n"
  "v_mfma_f32_16x16x32_bf16 v[44:47], a[100:103], v[32:35], v[44:47]\n"
  "v_mfma_f32_16x16x32_bf16 v[56:59], a[100:103], v[36:39], v[56:59]\n"
  "v_mfma_f32_16x16x32_bf16 v[48:51], a[104:107], v[32:35], v[48:51]\n"
  "v_mfma_f32_16x16x32_bf16 v[60:63], a[104:107], v[36:39], v[60:63]\n"
  "s_waitcnt lgkmcnt(2)\n"
  "v_mfma_f32_16x16x32_bf16 v[40:43], a[108:111], v[16:19], v[40:43]\n"
  "ds_read_b128 v[32:35], v1 offset:22528\n"
  "v_mfma_f32_16x16x32_bf16 v[52:55], a[108:111], v[20:23], v[52:55]\n"
  "ds_read_b128 v[36:39], v1 offset:22784\n"
  "v_mfma_f32_16x16x32_bf16 v[44:47], a[112:115], v[16:19], v[44:47]\n"
  "s_add_u32 s78, s70, 1\n"
  "s_and_b32 s78, s78, s61\n"
  "v_mfma_f32_16x16x32_bf16 v[56:59], a[112:115], v[20:23], v[56:59]\n"
  "v_mfma_f32_16x16x32_bf16 v[48:51], a[116:119], v[16:19], v[48:51]\n"
  "s_mul_i32 s78, s78, s60\n"
  "s_add_u32 s78, s78, s59\n"
  "v_mfma_f32_16x16x32_bf16 v[60:63], a[116:119], v[20:23], v[60:63]\n"
  "s_xor_b32 s79, s81, 65536\n"
  "s_add_u32 s79, s79, s57\n"
  "s_waitcnt lgkmcnt(2)\n"
  "v_mfma_f32_16x16x32_bf16 v[40:43], a[120:123], v[24:27], v[40:43]\n"
  "ds_read_b128 v[16:19], v1 offset:24576\n"
  "v_mfma_f32_16x16x32_bf16 v[52:55], a[120:123], v[28:31], v[52:55]\n"
  "ds_read_b128 v[20:23], v1 offset:24832\n"
  "s_and_b32 s80, s70, 15\n"
  "s_lshl_b32 s80, s80, 22\n"
  "v_mfma_f32_16x16x32_bf16 v[44:47], a[124:127], v[24:27], v[44:47]\n"
  "s_add_u32 s80, s80, s62\n"
  "v_mfma_f32_16x16x32_bf16 v[56:59], a[124:127], v[28:31], v[56:59]\n"
  "v_mfma_f32_16x16x32_bf16 v[48:51], a[128:131], v[24:27], v[48:51]\n"
  "v_mfma_f32_16x16x32_bf16 v[60:63], a[128:131], v[28:31], v[60:63]\n"
  "s_waitcnt lgkmcnt(2)\n"
  "v_mfma_f32_16x16x32_bf16 v[40:43], a[132:135], v[32:35], v[40:43]\n"
  "ds_read_b128 v[24:27], v1 offset:26624\n"
  "v_mfma_f32_16x16x32_bf16 v[52:55], a[132:135], v[36:39], v[52:55]\n"
  "ds_read_b128 v[28:31], v1 offset:26880\n"
  "v_mfma_f32_16x16x32_bf16 v[44:47], a[136:139], v[32:35], v[44:47]\n"
  "v_mfma_f32_16x16x32_bf16 v[56:59], a[136:139], v[36:39], v[56:59]\n"
  "v_mfma_f32_16x16x32_bf16 v[48:51], a[140:143], v[32:35], v[48:51]\n"
  "v_mfma_f32_16x16x32_bf16 v[60:63], a[140:143], v[36:39], v[60:63]\n"
  "s_waitcnt lgkmcnt(2)\n"
  "v_mfma_f32_16x16x32_bf16 v[40:43], a[144:147], v[16:19], v[40:43]\n"
  "ds_read_b128 v[32:35], v1 offset:28672\n"
  "v_mfma_f32_16x16x32_bf16 v[52:55], a[144:147], v[20:23], v[52:55]\n"
  "ds_read_b128 v[36:39], v1 offset:28928\n"
  "v_mfma_f32_16x16x32_bf16 v[44:47], a[148:151], v[16:19], v[44:47]\n"
  "v_mfma_f32_16x16x32_bf16 v[56:59], a[148:151], v[20:23], v[56:59]\n"
  "v_mfma_f32_16x16x32_bf16 v[48:51], a[152:155], v[16:19], v[48:51]\n"
  "buffer_load_dword v4, v2, s[36:39], 0 offen sc1\n"
  "v_mfma_f32_16x16x32_bf16 v[60:63], a[152:155], v[20:23], v[60:63]\n"
  "s_waitcnt lgkmcnt(2)\n"
  "v_mfma_f32_16x16x32_bf16 v[40:43], a[156:159], v[24:27], v[40:43]\n"
  "ds_read_b128 v[16:19], v1 offset:30720\n"
  "v_mfma_f32_16x16x32_bf16 v[52:55], a[156:159], v[28:31], v[52:55]\n"
  "ds_read_b128 v[20:23], v1 offset:30976\n"
  "v_mfma_f32_16x16x32_bf16 v[44:47], a[160:163], v[24:27], v[44:47]\n"
  "v_mfma_f32_16x16x32_bf16 v[56:59], a[160:163], v[28:31], v[56:59]\n"
  "v_mfma_f32_16x16x32_bf16 v[48:51], a[164:167], v[24:27], v[48:51]\n"
  "v_mfma_f32_16x16x32_bf16 v[60:63], a[164:167], v[28:31], v[60:63]\n"
  "s_waitcnt lgkmcnt(2)\n"
  "v_mfma_f32_16x16x32_bf16 v[40:43], a[168:171], v[32:35], v[40:43]\n"
  "ds_read_b128 v[24:27], v1 offset:32768\n"
  "v_mfma_f32_16x16x32_bf16 v[52:55], a[168:171], v[36:39], v[52:55]\n"
  "ds_read_b128 v[28:31], v1 offset:33024\n"
  "v_mfma_f32_16x16x32_bf16 v[44:47], a[172:175], v[32:35], v[44:47]\n"
  "v_mfma_f32_16x16x32_bf16 v[56:59], a[172:175], v[36:39], v[56:59]\n"
  "v_mfma_f32_16x16x32_bf16 v[48:51], a[176:179], v[32:35], v[48:51]\n"
  "v_mfma_f32_16x16x32_bf16 v[60:63], a[176:179], v[36:39], v[60:63]\n"
  "s_waitcnt lgkmcnt(2)\n"
  "v_mfma_f32_16x16x32_bf16 v[40:43], a[180:183], v[16:19], v[40:43]\n"
  "ds_read_b128 v[32:35], v1 offset:34816\n"
  "v_mfma_f32_16x16x32_bf16 v[52:55], a[180:183], v[20:23], v[52:55]\n"
  "ds_read_b128 v[36:39], v1 offset:35072\n"
  "v_mfma_f32_16x16x32_bf16 v[44:47], a[184:187], v[16:19], v[44:47]\n"
  "v_mfma_f32_16x16x32_bf16 v[56:59], a[184:187], v[20:23], v[56:59]\n"
  "v_mfma_f32_16x16x32_bf16 v[48:51], a[188:191], v[16:19], v[48:51]\n"
  "v_mfma_f32_16x16x32_bf16 v[60:63], a[188:191], v[20:23], v[60:63]\n"
  "s_waitcnt vmcnt(2)\n"
  "s_barrier\n"
  "s_waitcnt lgkmcnt(2)\n"
  "v_mfma_f32_16x16x32_bf16 v[40:43], a[192:195], v[24:27], v[40:43]\n"
  "ds_read_b128 v[16:19], v1 offset:36864\n"
  "v_mfma_f32_16x16x32_bf16 v[52:55], a[192:195], v[28:31], v[52:55]\n"
  "ds_read_b128 v[20:23], v1 offset:37120\n"
  "v_mfma_f32_16x16x32_bf16 v[44:47], a[196:199], v[24:27], v[44:47]\n"
  "v_mfma_f32_16x16x32_bf16 v[56:59], a[196:199], v[28:31], v[56:59]\n"
  "v_mfma_f32_16x16x32_bf16 v[48:51], a[200:203], v[24:27], v[48:51]\n"
  "v_mfma_f32_16x16x32_bf16 v[60:63], a[200:203], v[28:31], v[60:63]\n"
  "s_waitcnt lgkmcnt(2)\n"
  "v_mfma_f32_16x16x32_bf16 v[40:43], a[204:207], v[32:35], v[40:43]\n"
  "ds_read_b128 v[24:27], v1 offset:38912\n"
  "v_mfma_f32_16x16x32_bf16 v[52:55], a[204:207], v[36:39], v[52:55]\n"
  "ds_read_b128 v[28:31], v1 offset:39168\n"
  "v_mfma_f32_16x16x32_bf16 v[44:47], a[208:211], v[32:35], v[44:47]\n"
  "v_mfma_f32_16x16x32_bf16 v[56:59], a[208:211], v[36:39], v[56:59]\n"
  "v_mfma_f32_16x16x32_bf16 v[48:51], a[212:215], v[32:35], v[48:51]\n"
  "v_mfma_f32_16x16x32_bf16 v[60:63], a[212:215], v[36:39], v[60:63]\n"
  "s_waitcnt lgkmcnt(2)\n"
  "v_mfma_f32_16x16x32_bf16 v[40:43], a[216:219], v[16:19], v[40:43]\n"
  "ds_read_b128 v[32:35], v1 offset:40960\n"
  "v_mfma_f32_16x16x32_bf16 v[52:55], a[216:219], v[20:23], v[52:55]\n"
  "ds_read_b128 v[36:39], v1 offset:41216\n"
  "v_mfma_f32_16x16x32_bf16 v[44:47], a[220:223], v[16:19], v[44:47]\n"
  "v_mfma_f32_16x16x32_bf16 v[56:59], a[220:223], v[20:23], v[56:59]\n"
  "v_mfma_f32_16x16x32_bf16 v[48:51], a[224:227], v[16:19], v[48:51]\n"
  "v_mfma_f32_16x16x32_bf16 v[60:63], a[224:227], v[20:23], v[60:63]\n"
  "s_waitcnt lgkmcnt(2)\n"
  "v_mfma_f32_16x16x32_bf16 v[40:43], a[228:231], v[24:27], v[40:43]\n"
  "ds_read_b128 v[16:19], v1 offset:43008\n"
  "v_mfma_f32_16x16x32_bf16 v[52:55], a[228:231], v[28:31], v[52:55]\n"
  "ds_read_b128 v[20:23], v1 offset:43264\n"
  "s_waitcnt vmcnt(0)\n"
  "v_subrev_u32 v6, v3, v4\n"
  "v_cmp_lt_i32 vcc, s70, v6\n"
  "v_mfma_f32_16x16x32_bf16 v[44:47], a[232:235], v[24:27], v[44:47]\n"
  "s_cmp_lt_i32 s70, 63\n"
  "s_cselect_b32 s94, -1, 0\n"
  "v_mfma_f32_16x16x32_bf16 v[56:59], a[232:235], v[28:31], v[56:59]\n"
  "v_mfma_f32_16x16x32_bf16 v[48:51], a[236:239], v[24:27], v[48:51]\n"
  "s_cmp_eq_u64 vcc, s[94:95]\n"
  "s_cselect_b32 s71, 1, 0\n"
  "s_cselect_b32 s78, s78, 0x80000000\n"
  "v_mfma_f32_16x16x32_bf16 v[60:63], a[236:239], v[28:31], v[60:63]\n"
  "s_waitcnt lgkmcnt(2)\n"
  "v_mfma_f32_16x16x32_bf16 v[40:43], a[240:243], v[32:35], v[40:43]\n"
  "ds_read_b128 v[24:27], v1 offset:45056\n"
  "s_add_u32 m0, s79, 0\n"
  "s_add_u32 s76, s78, s4\n"
  "v_mfma_f32_16x16x32_bf16 v[52:55], a[240:243], v[36:39], v[52:55]\n"
  "ds_read_b128 v[28:31], v1 offset:45312\n"
  "buffer_load_dwordx4 v0, s[36:39], s76 offen sc1 lds\n"
  "v_mfma_f32_16x16x32_bf16 v[44:47], a[244:247], v[32:35], v[44:47]\n"
  "v_mfma_f32_16x16x32_bf16 v[56:59], a[244:247], v[36:39], v[56:59]\n"
  "v_mfma_f32_16x16x32_bf16 v[48:51], a[248:251], v[32:35], v[48:51]\n"
  "v_mfma_f32_16x16x32_bf16 v[60:63], a[248:251], v[36:39], v[60:63]\n"
  "s_add_u32 m0, s79, 4096\n"
  "s_add_u32 s76, s78, s5\n"
  "s_waitcnt lgkmcnt(2)\n"
  "v_mfma_f32_16x16x32_bf16 v[40:43], a[252:255], v[16:19], v[40:43]\n"
  "ds_read_b128 v[32:35], v1 offset:47104\n"
  "buffer_load_dwordx4 v0, s[36:39], s76 offen sc1 lds\n"
  "v_mfma_f32_16x16x32_bf16 v[52:55], a[252:255], v[20:23], v[52:55]\n"
  "ds_read_b128 v[36:39], v1 offset:47360\n"
  "v_mfma_f32_16x16x32_bf16 v[44:47], v[128:131], v[16:19], v[44:47]\n"
  "v_mfma_f32_16x16x32_bf16 v[56:59], v[128:131], v[20:23], v[56:59]\n"
  "v_mfma_f32_16x16x32_bf16 v[48:51], v[132:135], v[16:19], v[48:51]\n"
  "s_add_u32 m0, s79, 8192\n"
  "s_add_u32 s76, s78, s6\n"
  "v_mfma_f32_16x16x32_bf16 v[60:63], v[132:135], v[20:23], v[60:63]\n"
  "buffer_load_dwordx4 v0, s[36:39], s76 offen sc1 lds\n"
  "s_waitcnt lgkmcnt(2)\n"
  "v_mfma_f32_16x16x32_bf16 v[40:43], v[136:139], v[24:27], v[40:43]\n"
  "ds_read_b128 v[16:19], v1 offset:49152\n"
  "v_mfma_f32_16x16x32_bf16 v[52:55], v[136:139], v[28:31], v[52:55]\n"
  "ds_read_b128 v[20:23], v1 offset:49408\n"
  "v_mfma_f32_16x16x32_bf16 v[44:47], v[140:143], v[24:27], v[44:47]\n"
  "v_mfma_f32_16x16x32_bf16 v[56:59], v[140:143], v[28:31], v[56:59]\n"
  "s_add_u32 m0, s79, 12288\n"
  "s_add_u32 s76, s78, s7\n"
  "v_mfma_f32_16x16x32_bf16 v[48:51], v[144:147], v[24:27], v[48:51]\n"
  "buffer_load_dwordx4 v0, s[36:39], s76 offen sc1 lds\n"
  "v_mfma_f32_16x16x32_bf16 v[60:63], v[144:147], v[28:31], v[60:63]\n"
  "s_waitcnt lgkmcnt(2)\n"
  "v_mfma_f32_16x16x32_bf16 v[40:43], v[148:151], v[32:35], v[40:43]\n"
  "ds_read_b128 v[24:27], v1 offset:51200\n"
  "v_mfma_f32_16x16x32_bf16 v[52:55], v[148:151], v[36:39], v[52:55]\n"
  "ds_read_b128 v[28:31], v1 offset:51456\n"
  "v_mfma_f32_16x16x32_bf16 v[44:47], v[152:155], v[32:35], v[44:47]\n"
  "s_add_u32 m0, s79, 16384\n"
  "s_add_u32 s76, s78, s8\n"
  "v_mfma_f32_16x16x32_bf16 v[56:59], v[152:155], v[36:39], v[56:59]\n"
  "buffer_load_dwordx4 v0, s[36:39], s76 offen sc1 lds\n"
  "v_mfma_f32_16x16x32_bf16 v[48:51], v[156:159], v[32:35], v[48:51]\n"
  "v_mfma_f32_16x16x32_bf16 v[60:63], v[156:159], v[36:39], v[60:63]\n"
  "s_waitcnt lgkmcnt(2)\n"
  "v_mfma_f32_16x16x32_bf16 v[40:43], v[160:163], v[16:19], v[40:43]\n"
  "ds_read_b128 v[32:35], v1 offset:53248\n"
  "v_mfma_f32_16x16x32_bf16 v[52:55], v[160:163], v[20:23], v[52:55]\n"
  "ds_read_b128 v[36:39], v1 offset:53504\n"
  "s_add_u32 m0, s79, 20480\n"
  "s_add_u32 s76, s78, s9\n"
  "v_mfma_f32_16x16x32_bf16 v[44:47], v[164:167], v[16:19], v[44:47]\n"
  "buffer_load_dwordx4 v0, s[36:39], s76 offen sc1 lds\n"
  "v_mfma_f32_16x16x32_bf16 v[56:59], v[164:167], v[20:23], v[56:59]\n"
  "v_mfma_f32_16x16x32_bf16 v[48:51], v[168:171], v[16:19], v[48:51]\n"
  "v_mfma_f32_16x16x32_bf16 v[60:63], v[168:171], v[20:23], v[60:63]\n"
  "s_waitcnt lgkmcnt(2)\n"
  "v_mfma_f32_16x16x32_bf16 v[40:43], v[172:175], v[24:27], v[40:43]\n"
  "ds_read_b128 v[16:19], v1 offset:55296\n"
  "s_add_u32 m0, s79, 24576\n"
  "s_add_u32 s76, s78, s10\n"
  "v_mfma_f32_16x16x32_bf16 v[52:55], v[172:175], v[28:31], v[52:55]\n"
  "ds_read_b128 v[20:23], v1 offset:55552\n"
  "buffer_load_dwordx4 v0, s[36:39], s76 offen sc1 lds\n"
  "v_mfma_f32_16x16x32_bf16 v[44:47], v[176:179], v[24:27], v[44:47]\n"
  "v_mfma_f32_16x16x32_bf16 v[56:59], v[176:179], v[28:31], v[56:59]\n"
  "v_mfma_f32_16x16x32_bf16 v[48:51], v[180:183], v[24:27], v[48:51]\n"
  "v_mfma_f32_16x16x32_bf16 v[60:63], v[180:183], v[28:31], v[60:63]\n"
  "s_add_u32 m0, s79, 28672\n"
  "s_add_u32 s76, s78, s11\n"
  "s_waitcnt lgkmcnt(2)\n"
  "v_mfma_f32_16x16x32_bf16 v[40:43], v[184:187], v[32:35], v[40:43]\n"
  "ds_read_b128 v[24:27], v1 offset:57344\n"
  "buffer_load_dwordx4 v0, s[36:39], s76 offen sc1 lds\n"
  "v_mfma_f32_16x16x32_bf16 v[52:55], v[184:187], v[36:39], v[52:55]\n"
  "ds_read_b128 v[28:31], v1 offset:57600\n"
  "v_mfma_f32_16x16x32_bf16 v[44:47], v[188:191], v[32:35], v[44:47]\n"
  "v_mfma_f32_16x16x32_bf16 v[56:59], v[188:191], v[36:39], v[56:59]\n"
  "v_mfma_f32_16x16x32_bf16 v[48:51], v[192:195], v[32:35], v[48:51]\n"
  "s_add_u32 m0, s79, 32768\n"
  "s_add_u32 s76, s78, s12\n"
  "v_mfma_f32_16x16x32_bf16 v[60:63], v[192:195], v[36:39], v[60:63]\n"
  "buffer_load_dwordx4 v0, s[36:39], s76 offen sc1 lds\n"
  "s_waitcnt lgkmcnt(2)\n"
  "v_mfma_f32_16x16x32_bf16 v[40:43], v[196:199], v[16:19], v[40:43]\n"
  "v_mfma_f32_16x16x32_bf16 v[52:55], v[196:199], v[20:23], v[52:55]\n"
  "v_mfma_f32_16x16x32_bf16 v[44:47], v[200:203], v[16:19], v[44:47]\n"
  "v_mfma_f32_16x16x32_bf16 v[56:59], v[200:203], v[20:23], v[56:59]\n"
  "v_mfma_f32_16x16x32_bf16 v[48:51], v[204:207], v[16:19], v[48:51]\n"
  "buffer_load_dword v4, v2, s[36:39], 0 offen sc1\n"
  "v_mfma_f32_16x16x32_bf16 v[60:63], v[204:207], v[20:23], v[60:63]\n"
  "s_waitcnt lgkmcnt(0)\n"
  "v_mfma_f32_16x16x32_bf16 v[40:43], v[208:211], v[24:27], v[40:43]\n"
  "v_mfma_f32_16x16x32_bf16 v[44:47], v[212:215], v[24:27], v[44:47]\n"
  "v_mfma_f32_16x16x32_bf16 v[48:51], v[216:219], v[24:27], v[48:51]\n"
  "v_mfma_f32_16x16x32_bf16 v[52:55], v[208:211], v[28:31], v[52:55]\n"
  "v_mfma_f32_16x16x32_bf16 v[56:59], v[212:215], v[28:31], v[56:59]\n"
  "v_mfma_f32_16x16x32_bf16 v[60:63], v[216:219], v[28:31], v[60:63]\n"
  "v_cvt_pk_bf16_f32 v64, v40, v41\n"
  "v_cvt_pk_bf16_f32 v65, v42, v43\n"
  "v_cvt_pk_bf16_f32 v66, v44, v45\n"
  "v_cvt_pk_bf16_f32 v67, v46, v47\n"
  "buffer_store_dwordx4 v[64:67], v7, s[36:39], s80 offen sc1\n"
  "v_cvt_pk_bf16_f32 v72, v48, v49\n"
  "v_cvt_pk_bf16_f32 v73, v50, v51\n"
  "v_cvt_pk_bf16_f32 v68, v52, v53\n"
  "v_cvt_pk_bf16_f32 v69, v54, v55\n"
  "v_cvt_pk_bf16_f32 v70, v56, v57\n"
  "v_cvt_pk_bf16_f32 v71, v58, v59\n"
  "v_cvt_pk_bf16_f32 v74, v60, v61\n"
  "v_cvt_pk_bf16_f32 v75, v62, v63\n"
  "buffer_store_dwordx4 v[68:71], v7, s[36:39], s80 offen offset:256 sc1\n"
  "s_nop 0\n"
  "v_permlane16_swap_b32 v72, v74\n"
  "v_permlane16_swap_b32 v73, v75\n"
  "buffer_store_dwordx4 v[72:75], v12, s[36:39], s80 offen sc1\n"
  "s_xor_b32 s81, s81, 65536\n"
  "v_xor_b32 v1, 65536, v1\n"
  "s_add_u32 s70, s70, 1\n"
  "s_mov_b32 s82, s71\n"
  "s_cmp_lg_u32 s71, 0\n"
  "s_cbranch_scc1 L_pf_%=\n"
  "s_waitcnt vmcnt(3)\n"
  "v_subrev_u32 v6, v3, v4\n"
  "s_add_u32 s74, s70, 0\n"
  "v_cmp_le_i32 vcc, s74, v6\n"
  "s_cmp_eq_u64 vcc, exec\n"
  "s_cbranch_scc0 L_idle_%=\n"
  "s_cmp_eq_u32 s70, 64\n"
  "s_cbranch_scc1 L_idle_%=\n"
  "s_add_u32 s78, s70, 0\n"
  "s_and_b32 s78, s78, s61\n"
  "s_mul_i32 s78, s78, s60\n"
  "s_lshr_b32 s76, s78, 8\n"
  "s_and_b32 s76, s76, 8192\n"
  "s_add_u32 s78, s78, s59\n"
  "s_xor_b32 s78, s78, s76\n"
  "s_add_u32 s79, s81, s57\n"
  "s_add_u32 m0, s79, 0\n"
  "s_add_u32 s76, s78, s4\n"
  "buffer_load_dwordx4 v0, s[36:39], s76 offen sc1 lds\n"
  "s_add_u32 m0, s79, 4096\n"
  "s_add_u32 s76, s78, s5\n"
  "buffer_load_dwordx4 v0, s[36:39], s76 offen sc1 lds\n"
  "s_add_u32 m0, s79, 8192\n"
  "s_add_u32 s76, s78, s6\n"
  "buffer_load_dwordx4 v0, s[36:39], s76 offen sc1 lds\n"
  "s_add_u32 m0, s79, 12288\n"
  "s_add_u32 s76, s78, s7\n"
  "buffer_load_dwordx4 v0, s[36:39], s76 offen sc1 lds\n"
  "s_add_u32 m0, s79, 16384\n"
  "s_add_u32 s76, s78, s8\n"
  "buffer_load_dwordx4 v0, s[36:39], s76 offen sc1 lds\n"
  "s_add_u32 m0, s79, 20480\n"
  "s_add_u32 s76, s78, s9\n"
  "buffer_load_dwordx4 v0, s[36:39], s76 offen sc1 lds\n"
  "s_add_u32 m0, s79, 24576\n"
  "s_add_u32 s76, s78, s10\n"
  "buffer_load_dwordx4 v0, s[36:39], s76 offen sc1 lds\n"
  "s_add_u32 m0, s79, 28672\n"
  "s_add_u32 s76, s78, s11\n"
  "buffer_load_dwordx4 v0, s[36:39], s76 offen sc1 lds\n"
  "s_add_u32 m0, s79, 32768\n"
  "s_add_u32 s76, s78, s12\n"
  "buffer_load_dwordx4 v0, s[36:39], s76 offen sc1 lds\n"
  "s_waitcnt vmcnt(9)\n"
  "s_add_u32 s75, s70, -1\n"
  "s_lshl_b32 s74, s75, 2\n"
  "s_add_u32 s74, s74, 163584\n"
  "v_mov_b32 v16, s74\n"
  "v_mov_b32 v6, 1\n"
  "s_mov_b64 exec, 1\n"
  "ds_add_rtn_u32 v16, v16, v6\n"
  "s_mov_b64 exec, -1\n"
  "v_mov_b32 v6, s75\n"
  "s_waitcnt lgkmcnt(0)\n"
  "v_readfirstlane_b32 s74, v16\n"
  "s_cmp_eq_u32 s74, 3\n"
  "s_cselect_b32 s74, s58, 0x80000000\n"
  "buffer_store_dword v6, v5, s[36:39], s74 offen sc1\n"
  "s_waitcnt vmcnt(6)\n"
  "s_branch L_go_%=\n"
  "L_idle_%=:\n"
  "buffer_load_dword v4, v2, s[36:39], 0 offen sc1\n"
  "s_waitcnt vmcnt(1)\n"
  "s_add_u32 s75, s70, -1\n"
  "s_lshl_b32 s74, s75, 2\n"
  "s_add_u32 s74, s74, 163584\n"
  "v_mov_b32 v16, s74\n"
  "v_mov_b32 v6, 1\n"
  "s_mov_b64 exec, 1\n"
  "ds_add_rtn_u32 v16, v16, v6\n"
  "s_mov_b64 exec, -1\n"
  "v_mov_b32 v6, s75\n"
  "s_waitcnt lgkmcnt(0)\n"
  "v_readfirstlane_b32 s74, v16\n"
  "s_cmp_eq_u32 s74, 3\n"
  "s_cselect_b32 s74, s58, 0x80000000\n"
  "buffer_store_dword v6, v5, s[36:39], s74 offen sc1\n"
  "s_waitcnt vmcnt(1)\n"
  "s_cmp_eq_u32 s70, 64\n"
  "s_cbranch_scc1 L_fin_%=\n"
  "v_subrev_u32 v6, v3, v4\n"
  "s_add_u32 s74, s70, 0\n"
  "v_cmp_le_i32 vcc, s74, v6\n"
  "s_cmp_eq_u64 vcc, exec\n"
  "s_cbranch_scc1 L_poll_x_%=\n"
  "L_first_%=:\n"
  "s_cmp_lg_u32 s72, 0\n"
  "s_cselect_b32 s73, 0, 32768\n"
  "v_add_u32 v6, s70, v3\n"
  "L_poll_%=:\n"
  "buffer_load_dword v4, v2, s[36:39], 0 offen sc1\n"
  "s_sub_u32 s73, s73, 1\n"
  "s_cselect_b32 s72, 1, s72\n"
  "s_cselect_b64 s[2:3], 0, -1\n"
  "s_waitcnt vmcnt(0)\n"
  "v_cmp_gt_i32 vcc, v6, v4\n"
  "s_and_b64 vcc, vcc, s[2:3]\n"
  "s_cbranch_scc1 L_poll_%=\n"
  "L_poll_x_%=:\n"
  "s_add_u32 s78, s70, 0\n"
  "s_and_b32 s78, s78, s61\n"
  "s_mul_i32 s78, s78, s60\n"
  "s_lshr_b32 s76, s78, 8\n"
  "s_and_b32 s76, s76, 8192\n"
  "s_add_u32 s78, s78, s59\n"
  "s_xor_b32 s78, s78, s76\n"
  "s_add_u32 s79, s81, s57\n"
  "s_add_u32 m0, s79, 0\n"
  "s_add_u32 s76, s78, s4\n"
  "buffer_load_dwordx4 v0, s[36:39], s76 offen sc1 lds\n"
  "s_add_u32 m0, s79, 4096\n"
  "s_add_u32 s76, s78, s5\n"
  "buffer_load_dwordx4 v0, s[36:39], s76 offen sc1 lds\n"
  "s_add_u32 m0, s79, 8192\n"
  "s_add_u32 s76, s78, s6\n"
  "buffer_load_dwordx4 v0, s[36:39], s76 offen sc1 lds\n"
  "s_add_u32 m0, s79, 12288\n"
  "s_add_u32 s76, s78, s7\n"
  "buffer_load_dwordx4 v0, s[36:39], s76 offen sc1 lds\n"
  "s_add_u32 m0, s79, 16384\n"
  "s_add_u32 s76, s78, s8\n"
  "buffer_load_dwordx4 v0, s[36:39], s76 offen sc1 lds\n"
  "s_add_u32 m0, s79, 20480\n"
  "s_add_u32 s76, s78, s9\n"
  "buffer_load_dwordx4 v0, s[36:39], s76 offen sc1 lds\n"
  "s_add_u32 m0, s79, 24576\n"
  "s_add_u32 s76, s78, s10\n"
  "buffer_load_dwordx4 v0, s[36:39], s76 offen sc1 lds\n"
  "s_add_u32 m0, s79, 28672\n"
  "s_add_u32 s76, s78, s11\n"
  "buffer_load_dwordx4 v0, s[36:39], s76 offen sc1 lds\n"
  "s_add_u32 m0, s79, 32768\n"
  "s_add_u32 s76, s78, s12\n"
  "buffer_load_dwordx4 v0, s[36:39], s76 offen sc1 lds\n"
  "s_waitcnt vmcnt(5)\n"
  "s_branch L_go_%=\n"
  "L_fin_%=:\n"
  "s_waitcnt vmcnt(0)\n"
  "L_c1_%=:\n"
  "s_endpgm\n"
      :
      : L_OPERANDS, "{v2}"(f0 + ln <= f1 ? c.flag(F_F1 + f0 + ln) : OOB), "{v3}"(f0 + ln <= f1 ? 0 : NEVER), "{s[40:43]}"(mkrsrc(p.w2, 4608 * 576 * 2)),
        "{s[64:67]}"(uni((v4u){L_H / 1024 + 2u * k0, i == 4 ? 1u : 0u, 0u, 0u}))
      : OWNED);
}
DI void role_x(Ctx& c, const Params& p, int h) {
  const unsigned ln = (unsigned)c.lane, w = (unsigned)c.wave, g = ln >> 4, n = ln & 15u, qh = w & 1u, dh = w >> 1, hoff = (unsigned)h * 16384u;
  const W32 a = w32(c, 8192u, (unsigned)(256 * h));
  const unsigned fword = c.flag(F_XK + h);
  const bool xl = ln >= 12 && ln < 20;
  const unsigned srcb = c.xin() + w * 2048u, srcm = 256 * KB, srck = 63;
  const unsigned dstb = c.slab0(0) + TR(L_K) + hoff + w * 2048u;
  asm volatile(
  "s_getpc_b64 s[0:1]\n"
  "L_c0_%=:\n"
  "v_mbcnt_lo_u32_b32 v5, -1, 0\n"
  "v_mbcnt_hi_u32_b32 v5, -1, v5\n"
  "v_lshlrev_b32 v5, 4, v5\n"
  "v_add_lshl_u32 v5, v5, s57, 2\n"
  "v_min_u32 v5, L_c1_%=-L_c0_%=, v5\n"
  "global_load_dword v5, v5, s[0:1]\n"
  "s_nop 4\n"
  "s_add_u32 m0, s57, 0\n"
  "s_mov_b32 s74, 0\n"
  "buffer_load_dwordx4 v8, s[40:43], s74 offen lds\n"
  "s_add_u32 m0, s57, 4096\n"
  "s_mov_b32 s74, 131072\n"
  "buffer_load_dwordx4 v8, s[40:43], s74 offen lds\n"
  "s_add_u32 m0, s57, 8192\n"
  "s_mov_b32 s74, 262144\n"
  "buffer_load_dwordx4 v8, s[40:43], s74 offen lds\n"
  "s_add_u32 m0, s57, 12288\n"
  "s_mov_b32 s74, 393216\n"
  "buffer_load_dwordx4 v8, s[40:43], s74 offen lds\n"
  "s_add_u32 m0, s57, 16384\n"
  "s_mov_b32 s74, 524288\n"
  "buffer_load_dwordx4 v8, s[40:43], s74 offen lds\n"
  "s_add_u32 m0, s57, 20480\n"
  "s_mov_b32 s74, 655360\n"
  "buffer_load_dwordx4 v8, s[40:43], s74 offen lds\n"
  "s_add_u32 m0, s57, 24576\n"
  "s_mov_b32 s74, 786432\n"
  "buffer_load_dwordx4 v8, s[40:43], s74 offen lds\n"
  "s_add_u32 m0, s57, 28672\n"
  "s_mov_b32 s74, 917504\n"
  "buffer_load_dwordx4 v8, s[40:43], s74 offen lds\n"
  "s_add_u32 m0, s57, 32768\n"
  "s_mov_b32 s74, 1048576\n"
  "buffer_load_dwordx4 v8, s[40:43], s74 offen lds\n"
  "s_add_u32 m0, s57, 36864\n"
  "s_mov_b32 s74, 1179648\n"
  "buffer_load_dwordx4 v8, s[40:43], s74 offen lds\n"
  "s_add_u32 m0, s57, 40960\n"
  "s_mov_b32 s74, 1310720\n"
  "buffer_load_dwordx4 v8, s[40:43], s74 offen lds\n"
  "s_add_u32 m0, s57, 45056\n"
  "s_mov_b32 s74, 1441792\n"
  "buffer_load_dwordx4 v8, s[40:43], s74 offen lds\n"
  "s_add_u32 m0, s57, 49152\n"
  "s_mov_b32 s74, 1572864\n"
  "buffer_load_dwordx4 v8, s[40:43], s74 offen lds\n"
  "s_add_u32 m0, s57, 53248\n"
  "s_mov_b32 s74, 1703936\n"
  "buffer_load_dwordx4 v8, s[40:43], s74 offen lds\n"
  "s_add_u32 m0, s57, 57344\n"
  "s_mov_b32 s74, 1835008\n"
  "buffer_load_dwordx4 v8, s[40:43], s74 offen lds\n"
  "s_add_u32 m0, s57, 61440\n"
  "s_mov_b32 s74, 1966080\n"
  "buffer_load_dwordx4 v8, s[40:43], s74 offen lds\n"
  "s_add_u32 m0, s57, 65536\n"
  "s_mov_b32 s74, 2097152\n"
  "buffer_load_dwordx4 v8, s[40:43], s74 offen lds\n"
  "s_add_u32 m0, s57, 69632\n"
  "s_mov_b32 s74, 2228224\n"
  "buffer_load_dwordx4 v8, s[40:43], s74 offen lds\n"
  "s_add_u32 m0, s57, 73728\n"
  "s_mov_b32 s74, 2359296\n"
  "buffer_load_dwordx4 v8, s[40:43], s74 offen lds\n"
  "s_add_u32 m0, s57, 77824\n"
  "s_mov_b32 s74, 2490368\n"
  "buffer_load_dwordx4 v8, s[40:43], s74 offen lds\n"
  "s_add_u32 m0, s57, 81920\n"
  "s_mov_b32 s74, 2621440\n"
  "buffer_load_dwordx4 v8, s[40:43], s74 offen lds\n"
  "s_add_u32 m0, s57, 86016\n"
  "s_mov_b32 s74, 2752512\n"
  "buffer_load_dwordx4 v8, s[40:43], s74 offen lds\n"
  "s_add_u32 m0, s57, 90112\n"
  "s_mov_b32 s74, 2883584\n"
  "buffer_load_dwordx4 v8, s[40:43], s74 offen lds\n"
  "s_add_u32 m0, s57, 94208\n"
  "s_mov_b32 s74, 3014656\n"
  "buffer_load_dwordx4 v8, s[40:43], s74 offen lds\n"
  "s_waitcnt vmcnt(20) lgkmcnt(0)\n"
  "s_barrier\n"
  "s_add_u32 m0, s57, 98304\n"
  "s_mov_b32 s74, 3145728\n"
  "buffer_load_dwordx4 v8, s[40:43], s74 offen lds\n"
  "s_add_u32 m0, s57, 102400\n"
  "s_mov_b32 s74, 3276800\n"
  "buffer_load_dwordx4 v8, s[40:43], s74 offen lds\n"
  "s_add_u32 m0, s57, 106496\n"
  "s_mov_b32 s74, 3407872\n"
  "buffer_load_dwordx4 v8, s[40:43], s74 offen lds\n"
  "s_add_u32 m0, s57, 110592\n"
  "s_mov_b32 s74, 3538944\n"
  "buffer_load_dwordx4 v8, s[40:43], s74 offen lds\n"
  "v_add_u32 v4, 0, v9\n"
  "ds_read_b64_tr_b16 a[0:1], v4 offset:0\n"
  "ds_read_b64_tr_b16 a[2:3], v4 offset:1024\n"
  "ds_read_b64_tr_b16 a[4:5], v4 offset:8\n"
  "ds_read_b64_tr_b16 a[6:7], v4 offset:1032\n"
  "ds_read_b64_tr_b16 a[8:9], v4 offset:8192\n"
  "ds_read_b64_tr_b16 a[10:11], v4 offset:9216\n"
  "ds_read_b64_tr_b16 a[12:13], v4 offset:8200\n"
  "ds_read_b64_tr_b16 a[14:15], v4 offset:9224\n"
  "s_waitcnt vmcnt(20) lgkmcnt(0)\n"
  "s_barrier\n"
  "s_add_u32 m0, s57, 0\n"
  "s_mov_b32 s74, 3670016\n"
  "buffer_load_dwordx4 v8, s[40:43], s74 offen lds\n"
  "s_add_u32 m0, s57, 4096\n"
  "s_mov_b32 s74, 3801088\n"
  "buffer_load_dwordx4 v8, s[40:43], s74 offen lds\n"
  "s_add_u32 m0, s57, 8192\n"
  "s_mov_b32 s74, 3932160\n"
  "buffer_load_dwordx4 v8, s[40:43], s74 offen lds\n"
  "s_add_u32 m0, s57, 12288\n"
  "s_mov_b32 s74, 4063232\n"
  "buffer_load_dwordx4 v8, s[40:43], s74 offen lds\n"
  "v_add_u32 v4, 16384, v9\n"
  "ds_read_b64_tr_b16 a[16:17], v4 offset:0\n"
  "ds_read_b64_tr_b16 a[18:19], v4 offset:1024\n"
  "ds_read_b64_tr_b16 a[20:21], v4 offset:8\n"
  "ds_read_b64_tr_b16 a[22:23], v4 offset:1032\n"
  "ds_read_b64_tr_b16 a[24:25], v4 offset:8192\n"
  "ds_read_b64_tr_b16 a[26:27], v4 offset:9216\n"
  "ds_read_b64_tr_b16 a[28:29], v4 offset:8200\n"
  "ds_read_b64_tr_b16 a[30:31], v4 offset:9224\n"
  "s_waitcnt vmcnt(20) lgkmcnt(0)\n"
  "s_barrier\n"
  "s_add_u32 m0, s57, 16384\n"
  "s_mov_b32 s74, 4194304\n"
  "buffer_load_dwordx4 v8, s[40:43], s74 offen lds\n"
  "s_add_u32 m0, s57, 20480\n"
  "s_mov_b32 s74, 4325376\n"
  "buffer_load_dwordx4 v8, s[40:43], s74 offen lds\n"
  "s_add_u32 m0, s57, 24576\n"
  "s_mov_b32 s74, 4456448\n"
  "buffer_load_dwordx4 v8, s[40:43], s74 offen lds\n"
  "s_add_u32 m0, s57, 28672\n"
  "s_mov_b32 s74, 4587520\n"
  "buffer_load_dwordx4 v8, s[40:43], s74 offen lds\n"
  "v_add_u32 v4, 32768, v9\n"
  "ds_read_b64_tr_b16 a[32:33], v4 offset:0\n"
  "ds_read_b64_tr_b16 a[34:35], v4 offset:1024\n"
  "ds_read_b64_tr_b16 a[36:37], v4 offset:8\n"
  "ds_read_b64_tr_b16 a[38:39], v4 offset:1032\n"
  "ds_read_b64_tr_b16 a[40:41], v4 offset:8192\n"
  "ds_read_b64_tr_b16 a[42:43], v4 offset:9216\n"
  "ds_read_b64_tr_b16 a[44:45], v4 offset:8200\n"
  "ds_read_b64_tr_b16 a[46:47], v4 offset:9224\n"
  "s_waitcnt vmcnt(20) lgkmcnt(0)\n"
  "s_barrier\n"
  "s_add_u32 m0, s57, 32768\n"
  "s_mov_b32 s74, 0\n"
  "buffer_load_dwordx4 v8, s[44:47], s74 offen lds\n"
  "s_add_u32 m0, s57, 36864\n"
  "s_mov_b32 s74, 131072\n"
  "buffer_load_dwordx4 v8, s[44:47], s74 offen lds\n"
  "s_add_u32 m0, s57, 40960\n"
  "s_mov_b32 s74, 262144\n"
  "buffer_load_dwordx4 v8, s[44:47], s74 offen lds\n"
  "s_add_u32 m0, s57, 45056\n"
  "s_mov_b32 s74, 393216\n"
  "buffer_load_dwordx4 v8, s[44:47], s74 offen lds\n"
  "v_add_u32 v4, 49152, v9\n"
  "ds_read_b64_tr_b16 a[48:49], v4 offset:0\n"
  "ds_read_b64_tr_b16 a[50:51], v4 offset:1024\n"
  "ds_read_b64_tr_b16 a[52:53], v4 offset:8\n"
  "ds_read_b64_tr_b16 a[54:55], v4 offset:1032\n"
  "ds_read_b64_tr_b16 a[56:57], v4 offset:8192\n"
  "ds_read_b64_tr_b16 a[58:59], v4 offset:9216\n"
  "ds_read_b64_tr_b16 a[60:61], v4 offset:8200\n"
  "ds_read_b64_tr_b16 a[62:63], v4 offset:9224\n"
  "s_waitcnt vmcnt(20) lgkmcnt(0)\n"
  "s_barrier\n"
  "s_add_u32 m0, s57, 49152\n"
  "s_mov_b32 s74, 524288\n"
  "buffer_load_dwordx4 v8, s[44:47], s74 offen lds\n"
  "s_add_u32 m0, s57, 53248\n"
  "s_mov_b32 s74, 655360\n"
  "buffer_load_dwordx4 v8, s[44:47], s74 offen lds\n"
  "s_add_u32 m0, s57, 57344\n"
  "s_mov_b32 s74, 786432\n"
  "buffer_load_dwordx4 v8, s[44:47], s74 offen lds\n"
  "s_add_u32 m0, s57, 61440\n"
  "s_mov_b32 s74, 917504\n"
  "buffer_load_dwordx4 v8, s[44:47], s74 offen lds\n"
  "v_add_u32 v4, 65536, v9\n"
  "ds_read_b64_tr_b16 a[64:65], v4 offset:0\n"
  "ds_read_b64_tr_b16 a[66:67], v4 offset:1024\n"
  "ds_read_b64_tr_b16 a[68:69], v4 offset:8\n"
  "ds_read_b64_tr_b16 a[70:71], v4 offset:1032\n"
  "ds_read_b64_tr_b16 a[72:73], v4 offset:8192\n"
  "ds_read_b64_tr_b16 a[74:75], v4 offset:9216\n"
  "ds_read_b64_tr_b16 a[76:77], v4 offset:8200\n"
  "ds_read_b64_tr_b16 a[78:79], v4 offset:9224\n"
  "s_waitcnt vmcnt(20) lgkmcnt(0)\n"
  "s_barrier\n"
  "s_add_u32 m0, s57, 65536\n"
  "s_mov_b32 s74, 1048576\n"
  "buffer_load_dwordx4 v8, s[44:47], s74 offen lds\n"
  "s_add_u32 m0, s57, 69632\n"
  "s_mov_b32 s74, 1179648\n"
  "buffer_load_dwordx4 v8, s[44:47], s74 offen lds\n"
  "s_add_u32 m0, s57, 73728\n"
  "s_mov_b32 s74, 1310720\n"
  "buffer_load_dwordx4 v8, s[44:47], s74 offen lds\n"
  "s_add_u32 m0, s57, 77824\n"
  "s_mov_b32 s74, 1441792\n"
  "buffer_load_dwordx4 v8, s[44:47], s74 offen lds\n"
  "v_add_u32 v4, 81920, v9\n"
  "ds_read_b64_tr_b16 a[80:81], v4 offset:0\n"
  "ds_read_b64_tr_b16 a[82:83], v4 offset:1024\n"
  "ds_read_b64_tr_b16 a[84:85], v4 offset:8\n"
  "ds_read_b64_tr_b16 a[86:87], v4 offset:1032\n"
  "ds_read_b64_tr_b16 a[88:89], v4 offset:8192\n"
  "ds_read_b64_tr_b16 a[90:91], v4 offset:9216\n"
  "ds_read_b64_tr_b16 a[92:93], v4 offset:8200\n"
  "ds_read_b64_tr_b16 a[94:95], v4 offset:9224\n"
  "s_waitcnt vmcnt(20) lgkmcnt(0)\n"
  "s_barrier\n"
  "s_add_u32 m0, s57, 81920\n"
  "s_mov_b32 s74, 1572864\n"
  "buffer_load_dwordx4 v8, s[44:47], s74 offen lds\n"
  "s_add_u32 m0, s57, 86016\n"
  "s_mov_b32 s74, 1703936\n"
  "buffer_load_dwordx4 v8, s[44:47], s74 offen lds\n"
  "s_add_u32 m0, s57, 90112\n"
  "s_mov_b32 s74, 1835008\n"
  "buffer_load_dwordx4 v8, s[44:47], s74 offen lds\n"
  "s_add_u32 m0, s57, 94208\n"
  "s_mov_b32 s74, 1966080\n"
  "buffer_load_dwordx4 v8, s[44:47], s74 offen lds\n"
  "v_add_u32 v4, 98304, v9\n"
  "ds_read_b64_tr_b16 a[96:97], v4 offset:0\n"
  "ds_read_b64_tr_b16 a[98:99], v4 offset:1024\n"
  "ds_read_b64_tr_b16 a[100:101], v4 offset:8\n"
  "ds_read_b64_tr_b16 a[102:103], v4 offset:1032\n"
  "ds_read_b64_tr_b16 a[104:105], v4 offset:8192\n"
  "ds_read_b64_tr_b16 a[106:107], v4 offset:9216\n"
  "ds_read_b64_tr_b16 a[108:109], v4 offset:8200\n"
  "ds_read_b64_tr_b16 a[110:111], v4 offset:9224\n"
  "s_waitcnt vmcnt(20) lgkmcnt(0)\n"
  "s_barrier\n"
  "s_add_u32 m0, s57, 98304\n"
  "s_mov_b32 s74, 2097152\n"
  "buffer_load_dwordx4 v8, s[44:47], s74 offen lds\n"
  "s_add_u32 m0, s57, 102400\n"
  "s_mov_b32 s74, 2228224\n"
  "buffer_load_dwordx4 v8, s[44:47], s74 offen lds\n"
  "s_add_u32 m0, s57, 106496\n"
  "s_mov_b32 s74, 2359296\n"
  "buffer_load_dwordx4 v8, s[44:47], s74 offen lds\n"
  "s_add_u32 m0, s57, 110592\n"
  "s_mov_b32 s74, 2490368\n"
  "buffer_load_dwordx4 v8, s[44:47], s74 offen lds\n"
  "v_add_u32 v4, 0, v9\n"
  "ds_read_b64_tr_b16 a[112:113], v4 offset:0\n"
  "ds_read_b64_tr_b16 a[114:115], v4 offset:1024\n"
  "ds_read_b64_tr_b16 a[116:117], v4 offset:8\n"
  "ds_read_b64_tr_b16 a[118:119], v4 offset:1032\n"
  "ds_read_b64_tr_b16 a[120:121], v4 offset:8192\n"
  "ds_read_b64_tr_b16 a[122:123], v4 offset:9216\n"
  "ds_read_b64_tr_b16 a[124:125], v4 offset:8200\n"
  "ds_read_b64_tr_b16 a[126:127], v4 offset:9224\n"
  "s_waitcnt vmcnt(20) lgkmcnt(0)\n"
  "s_barrier\n"
  "s_add_u32 m0, s57, 0\n"
  "s_mov_b32 s74, 2621440\n"
  "buffer_load_dwordx4 v8, s[44:47], s74 offen lds\n"
  "s_add_u32 m0, s57, 4096\n"
  "s_mov_b32 s74, 2752512\n"
  "buffer_load_dwordx4 v8, s[44:47], s74 offen lds\n"
  "s_add_u32 m0, s57, 8192\n"
  "s_mov_b32 s74, 2883584\n"
  "buffer_load_dwordx4 v8, s[44:47], s74 offen lds\n"
  "s_add_u32 m0, s57, 12288\n"
  "s_mov_b32 s74, 3014656\n"
  "buffer_load_dwordx4 v8, s[44:47], s74 offen lds\n"
  "v_add_u32 v4, 16384, v9\n"
  "ds_read_b64_tr_b16 a[128:129], v4 offset:0\n"
  "ds_read_b64_tr_b16 a[130:131], v4 offset:1024\n"
  "ds_read_b64_tr_b16 a[132:133], v4 offset:8\n"
  "ds_read_b64_tr_b16 a[134:135], v4 offset:1032\n"
  "ds_read_b64_tr_b16 a[136:137], v4 offset:8192\n"
  "ds_read_b64_tr_b16 a[138:139], v4 offset:9216\n"
  "ds_read_b64_tr_b16 a[140:141], v4 offset:8200\n"
  "ds_read_b64_tr_b16 a[142:143], v4 offset:9224\n"
  "s_waitcnt vmcnt(20) lgkmcnt(0)\n"
  "s_barrier\n"
  "s_add_u32 m0, s57, 16384\n"
  "s_mov_b32 s74, 3145728\n"
  "buffer_load_dwordx4 v8, s[44:47], s74 offen lds\n"
  "s_add_u32 m0, s57, 20480\n"
  "s_mov_b32 s74, 3276800\n"
  "buffer_load_dwordx4 v8, s[44:47], s74 offen lds\n"
  "s_add_u32 m0, s57, 24576\n"
  "s_mov_b32 s74, 3407872\n"
  "buffer_load_dwordx4 v8, s[44:47], s74 offen lds\n"
  "s_add_u32 m0, s57, 28672\n"
  "s_mov_b32 s74, 3538944\n"
  "buffer_load_dwordx4 v8, s[44:47], s74 offen lds\n"
  "v_add_u32 v4, 32768, v9\n"
  "ds_read_b64_tr_b16 a[144:145], v4 offset:0\n"
  "ds_read_b64_tr_b16 a[146:147], v4 offset:1024\n"
  "ds_read_b64_tr_b16 a[148:149], v4 offset:8\n"
  "ds_read_b64_tr_b16 a[150:151], v4 offset:1032\n"
  "ds_read_b64_tr_b16 a[152:153], v4 offset:8192\n"
  "ds_read_b64_tr_b16 a[154:155], v4 offset:9216\n"
  "ds_read_b64_tr_b16 a[156:157], v4 offset:8200\n"
  "ds_read_b64_tr_b16 a[158:159], v4 offset:9224\n"
  "s_waitcnt vmcnt(20) lgkmcnt(0)\n"
  "s_barrier\n"
  "s_add_u32 m0, s57, 32768\n"
  "s_mov_b32 s74, 3670016\n"
  "buffer_load_dwordx4 v8, s[44:47], s74 offen lds\n"
  "s_add_u32 m0, s57, 36864\n"
  "s_mov_b32 s74, 3801088\n"
  "buffer_load_dwordx4 v8, s[44:47], s74 offen lds\n"
  "s_add_u32 m0, s57, 40960\n"
  "s_mov_b32 s74, 3932160\n"
  "buffer_load_dwordx4 v8, s[44:47], s74 offen lds\n"
  "s_add_u32 m0, s57, 45056\n"
  "s_mov_b32 s74, 4063232\n"
  "buffer_load_dwordx4 v8, s[44:47], s74 offen lds\n"
  "v_add_u32 v4, 49152, v9\n"
  "ds_read_b64_tr_b16 a[160:161], v4 offset:0\n"
  "ds_read_b64_tr_b16 a[162:163], v4 offset:1024\n"
  "ds_read_b64_tr_b16 a[164:165], v4 offset:8\n"
  "ds_read_b64_tr_b16 a[166:167], v4 offset:1032\n"
  "ds_read_b64_tr_b16 a[168:169], v4 offset:8192\n"
  "ds_read_b64_tr_b16 a[170:171], v4 offset:9216\n"
  "ds_read_b64_tr_b16 a[172:173], v4 offset:8200\n"
  "ds_read_b64_tr_b16 a[174:175], v4 offset:9224\n"
  "s_waitcnt vmcnt(20) lgkmcnt(0)\n"
  "s_barrier\n"
  "s_add_u32 m0, s57, 49152\n"
  "s_mov_b32 s74, 4194304\n"
  "buffer_load_dwordx4 v8, s[44:47], s74 offen lds\n"
  "s_add_u32 m0, s57, 53248\n"
  "s_mov_b32 s74, 4325376\n"
  "buffer_load_dwordx4 v8, s[44:47], s74 offen lds\n"
  "s_add_u32 m0, s57, 57344\n"
  "s_mov_b32 s74, 4456448\n"
  "buffer_load_dwordx4 v8, s[44:47], s74 offen lds\n"
  "s_add_u32 m0, s57, 61440\n"
  "s_mov_b32 s74, 4587520\n"
  "buffer_load_dwordx4 v8, s[44:47], s74 offen lds\n"
  "v_add_u32 v4, 65536, v9\n"
  "ds_read_b64_tr_b16 a[176:177], v4 offset:0\n"
  "ds_read_b64_tr_b16 a[178:179], v4 offset:1024\n"
  "ds_read_b64_tr_b16 a[180:181], v4 offset:8\n"
  "ds_read_b64_tr_b16 a[182:183], v4 offset:1032\n"
  "ds_read_b64_tr_b16 a[184:185], v4 offset:8192\n"
  "ds_read_b64_tr_b16 a[186:187], v4 offset:9216\n"
  "ds_read_b64_tr_b16 a[188:189], v4 offset:8200\n"
  "ds_read_b64_tr_b16 a[190:191], v4 offset:9224\n"
  "s_waitcnt vmcnt(20) lgkmcnt(0)\n"
  "s_barrier\n"
  "v_add_u32 v4, 81920, v9\n"
  "ds_read_b64_tr_b16 a[192:193], v4 offset:0\n"
  "ds_read_b64_tr_b16 a[194:195], v4 offset:1024\n"
  "ds_read_b64_tr_b16 a[196:197], v4 offset:8\n"
  "ds_read_b64_tr_b16 a[198:199], v4 offset:1032\n"
  "ds_read_b64_tr_b16 a[200:201], v4 offset:8192\n"
  "ds_read_b64_tr_b16 a[202:203], v4 offset:9216\n"
  "ds_read_b64_tr_b16 a[204:205], v4 offset:8200\n"
  "ds_read_b64_tr_b16 a[206:207], v4 offset:9224\n"
  "s_waitcnt vmcnt(16) lgkmcnt(0)\n"
  "s_barrier\n"
  "v_add_u32 v4, 98304, v9\n"
  "ds_read_b64_tr_b16 a[208:209], v4 offset:0\n"
  "ds_read_b64_tr_b16 a[210:211], v4 offset:1024\n"
  "ds_read_b64_tr_b16 a[212:213], v4 offset:8\n"
  "ds_read_b64_tr_b16 a[214:215], v4 offset:1032\n"
  "ds_read_b64_tr_b16 a[216:217], v4 offset:8192\n"
  "ds_read_b64_tr_b16 a[218:219], v4 offset:9216\n"
  "ds_read_b64_tr_b16 a[220:221], v4 offset:8200\n"
  "ds_read_b64_tr_b16 a[222:223], v4 offset:9224\n"
  "s_waitcnt vmcnt(12) lgkmcnt(0)\n"
  "s_barrier\n"
  "v_add_u32 v4, 0, v9\n"
  "ds_read_b64_tr_b16 a[224:225], v4 offset:0\n"
  "ds_read_b64_tr_b16 a[226:227], v4 offset:1024\n"
  "ds_read_b64_tr_b16 a[228:229], v4 offset:8\n"
  "ds_read_b64_tr_b16 a[230:231], v4 offset:1032\n"
  "ds_read_b64_tr_b16 a[232:233], v4 offset:8192\n"
  "ds_read_b64_tr_b16 a[234:235], v4 offset:9216\n"
  "ds_read_b64_tr_b16 a[236:237], v4 offset:8200\n"
  "ds_read_b64_tr_b16 a[238:239], v4 offset:9224\n"
  "s_waitcnt vmcnt(8) lgkmcnt(0)\n"
  "s_barrier\n"
  "v_add_u32 v4, 16384, v9\n"
  "ds_read_b64_tr_b16 a[240:241], v4 offset:0\n"
  "ds_read_b64_tr_b16 a[242:243], v4 offset:1024\n"
  "ds_read_b64_tr_b16 a[244:245], v4 offset:8\n"
  "ds_read_b64_tr_b16 a[246:247], v4 offset:1032\n"
  "ds_read_b64_tr_b16 a[248:249], v4 offset:8192\n"
  "ds_read_b64_tr_b16 a[250:251], v4 offset:9216\n"
  "ds_read_b64_tr_b16 a[252:253], v4 offset:8200\n"
  "ds_read_b64_tr_b16 a[254:255], v4 offset:9224\n"
  "s_waitcnt vmcnt(4) lgkmcnt(0)\n"
  "s_barrier\n"
  "v_add_u32 v4, 32768, v9\n"
  "ds_read_b64_tr_b16 v[224:225], v4 offset:0\n"
  "ds_read_b64_tr_b16 v[226:227], v4 offset:1024\n"
  "ds_read_b64_tr_b16 v[228:229], v4 offset:8\n"
  "ds_read_b64_tr_b16 v[230:231], v4 offset:1032\n"
  "ds_read_b64_tr_b16 v[232:233], v4 offset:8192\n"
  "ds_read_b64_tr_b16 v[234:235], v4 offset:9216\n"
  "ds_read_b64_tr_b16 v[236:237], v4 offset:8200\n"
  "ds_read_b64_tr_b16 v[238:239], v4 offset:9224\n"
  "s_waitcnt vmcnt(0) lgkmcnt(0)\n"
  "s_barrier\n"
  "v_add_u32 v4, 49152, v9\n"
  "ds_read_b64_tr_b16 v[240:241], v4 offset:0\n"
  "ds_read_b64_tr_b16 v[242:243], v4 offset:1024\n"
  "ds_read_b64_tr_b16 v[244:245], v4 offset:8\n"
  "ds_read_b64_tr_b16 v[246:247], v4 offset:1032\n"
  "ds_read_b64_tr_b16 v[248:249], v4 offset:8192\n"
  "ds_read_b64_tr_b16 v[250:251], v4 offset:9216\n"
  "ds_read_b64_tr_b16 v[252:253], v4 offset:8200\n"
  "ds_read_b64_tr_b16 v[254:255], v4 offset:9224\n"
  "s_waitcnt lgkmcnt(0)\n"
  "s_barrier\n"
  "v_lshrrev_b32 v4, 2, v0\n"
  "v_add_u32 v4, 163584, v4\n"
  "v_mov_b32 v6, 0\n"
  "ds_write_b32 v4, v6\n"
  "v_min_u32 v5, 1, v0\n"
  "v_lshlrev_b32 v5, 30, v5\n"
  "s_mov_b32 s70, 0\n"
  "s_mov_b32 s71, 0\n"
  "s_mov_b32 s72, 0\n"
  "s_mov_b32 s81, 0\n"
  "s_mov_b32 s82, 0\n"
  "s_mov_b32 s83, 0\n"
  "s_mov_b32 s95, -1\n"
  "s_cmp_eq_u32 s57, 0\n"
  "s_cselect_b32 s92, s58, 0x80000000\n"
  "s_waitcnt lgkmcnt(0)\n"
  "s_barrier\n"
  "s_cmp_eq_u32 s57, 0\n"
  "s_cselect_b32 s93, s64, 0x80000000\n"
  "s_mov_b32 s85, -3\n"
  "s_add_u32 s86, s57, 53248\n"
  "v_mov_b32 v72, 0xff800000\n"
  "v_mov_b32 v221, 0\n"
  "v_mov_b32 v222, 0\n"
  "v_cmp_le_i32 s[4:5], 0, v14\n"
  "v_cmp_le_i32 s[12:13], 16, v14\n"
  "v_mov_b32 v84, 0x3f803f80\n"
  "v_cmp_le_i32 s[6:7], 1, v14\n"
  "v_cmp_le_i32 s[14:15], 17, v14\n"
  "v_mov_b32 v85, 0x3f803f80\n"
  "v_cmp_le_i32 s[8:9], 2, v14\n"
  "v_cmp_le_i32 s[16:17], 18, v14\n"
  "v_mov_b32 v86, 0x3f803f80\n"
  "v_cmp_le_i32 s[10:11], 3, v14\n"
  "v_cmp_le_i32 s[18:19], 19, v14\n"
  "v_mov_b32 v87, 0x3f803f80\n"
  "s_branch L_first_%=\n"
  "L_pf_%=:\n"
  "s_waitcnt vmcnt(5)\n"
  "L_go_%=:\n"
  "s_barrier\n"
  "ds_read_b128 v[16:19], v1 offset:0\n"
  "ds_read_b128 v[20:23], v1 offset:1024\n"
  "s_waitcnt lgkmcnt(1)\n"
  "v_mfma_f32_16x16x32_bf16 v[40:43], a[0:3], v[16:19], 0\n"
  "ds_read_b128 v[24:27], v1 offset:2048\n"
  "v_mfma_f32_16x16x32_bf16 v[44:47], a[4:7], v[16:19], 0\n"
  "v_mfma_f32_16x16x32_bf16 v[56:59], a[144:147], v[16:19], 0\n"
  "v_mfma_f32_16x16x32_bf16 v[60:63], a[148:151], v[16:19], 0\n"
  "s_waitcnt lgkmcnt(1)\n"
  "v_mfma_f32_16x16x32_bf16 v[40:43], a[8:11], v[20:23], v[40:43]\n"
  "ds_read_b128 v[28:31], v1 offset:3072\n"
  "v_mfma_f32_16x16x32_bf16 v[44:47], a[12:15], v[20:23], v[44:47]\n"
  "s_add_u32 m0, s79, 20480\n"
  "s_add_u32 s76, s78, 256\n"
  "v_mfma_f32_16x16x32_bf16 v[56:59], a[152:155], v[20:23], v[56:59]\n"
  "buffer_load_dwordx4 v12, s[36:39], s76 offen sc1 lds\n"
  "v_mfma_f32_16x16x32_bf16 v[60:63], a[156:159], v[20:23], v[60:63]\n"
  "s_waitcnt lgkmcnt(1)\n"
  "v_mfma_f32_16x16x32_bf16 v[40:43], a[16:19], v[24:27], v[40:43]\n"
  "ds_read_b128 v[32:35], v1 offset:4096\n"
  "v_mfma_f32_16x16x32_bf16 v[44:47], a[20:23], v[24:27], v[44:47]\n"
  "v_mfma_f32_16x16x32_bf16 v[56:59], a[160:163], v[24:27], v[56:59]\n"
  "s_add_u32 m0, s79, 24576\n"
  "s_add_u32 s76, s78, 16640\n"
  "v_mfma_f32_16x16x32_bf16 v[60:63], a[164:167], v[24:27], v[60:63]\n"
  "buffer_load_dwordx4 v12, s[36:39], s76 offen sc1 lds\n"
  "s_waitcnt lgkmcnt(1)\n"
  "v_mfma_f32_16x16x32_bf16 v[40:43], a[24:27], v[28:31], v[40:43]\n"
  "ds_read_b128 v[36:39], v1 offset:5120\n"
  "v_mfma_f32_16x16x32_bf16 v[44:47], a[28:31], v[28:31], v[44:47]\n"
  "v_mfma_f32_16x16x32_bf16 v[56:59], a[168:171], v[28:31], v[56:59]\n"
  "v_mfma_f32_16x16x32_bf16 v[60:63], a[172:175], v[28:31], v[60:63]\n"
  "s_add_u32 m0, s79, 28672\n"
  "s_add_u32 s76, s78, 33024\n"
  "s_waitcnt lgkmcnt(1)\n"
  "v_mfma_f32_16x16x32_bf16 v[40:43], a[32:35], v[32:35], v[40:43]\n"
  "ds_read_b128 v[16:19], v1 offset:6144\n"
  "buffer_load_dwordx4 v12, s[36:39], s76 offen sc1 lds\n"
  "v_mfma_f32_16x16x32_bf16 v[44:47], a[36:39], v[32:35], v[44:47]\n"
  "v_mfma_f32_16x16x32_bf16 v[56:59], a[176:179], v[32:35], v[56:59]\n"
  "v_mfma_f32_16x16x32_bf16 v[60:63], a[180:183], v[32:35], v[60:63]\n"
  "s_waitcnt lgkmcnt(1)\n"
  "v_mfma_f32_16x16x32_bf16 v[40:43], a[40:43], v[36:39], v[40:43]\n"
  "ds_read_b128 v[20:23], v1 offset:7168\n"
  "s_add_u32 m0, s79, 32768\n"
  "s_add_u32 s76, s78, 49408\n"
  "v_mfma_f32_16x16x32_bf16 v[44:47], a[44:47], v[36:39], v[44:47]\n"
  "buffer_load_dwordx4 v12, s[36:39], s76 offen sc1 lds\n"
  "v_mfma_f32_16x16x32_bf16 v[56:59], a[184:187], v[36:39], v[56:59]\n"
  "v_mfma_f32_16x16x32_bf16 v[60:63], a[188:191], v[36:39], v[60:63]\n"
  "s_waitcnt lgkmcnt(1)\n"
  "v_mfma_f32_16x16x32_bf16 v[40:43], a[48:51], v[16:19], v[40:43]\n"
  "ds_read_b128 v[24:27], v1 offset:8192\n"
  "s_add_u32 s74, s70, 0\n"
  "s_and_b32 s74, s74, 3\n"
  "v_mfma_f32_16x16x32_bf16 v[44:47], a[52:55], v[16:19], v[44:47]\n"
  "s_lshl_b32 s75, s74, 14\n"
  "s_add_u32 s75, s75, 86016\n"
  "v_mfma_f32_16x16x32_bf16 v[56:59], a[192:195], v[16:19], v[56:59]\n"
  "s_cmp_eq_u32 s74, 0\n"
  "s_cselect_b32 s75, 36864, s75\n"
  "s_lshl_b32 s74, s57, 1\n"
  "v_mfma_f32_16x16x32_bf16 v[60:63], a[196:199], v[16:19], v[60:63]\n"
  "s_add_u32 s75, s75, s74\n"
  "v_add_u32 v97, s75, v12\n"
  "s_waitcnt lgkmcnt(1)\n"
  "v_mfma_f32_16x16x32_bf16 v[40:43], a[56:59], v[20:23], v[40:43]\n"
  "ds_read_b128 v[28:31], v1 offset:9216\n"
  "s_add_u32 s80, s70, 0\n"
  "s_and_b32 s80, s80, 15\n"
  "v_mfma_f32_16x16x32_bf16 v[44:47], a[60:63], v[20:23], v[44:47]\n"
  "s_lshl_b32 s80, s80, 22\n"
  "s_add_u32 s80, s80, s62\n"
  "v_mfma_f32_16x16x32_bf16 v[56:59], a[200:203], v[20:23], v[56:59]\n"
  "s_add_u32 s84, s85, 1\n"
  "s_and_b32 s84, s84, 15\n"
  "v_mfma_f32_16x16x32_bf16 v[60:63], a[204:207], v[20:23], v[60:63]\n"
  "s_lshl_b32 s84, s84, 22\n"
  "s_add_u32 s84, s84, s87\n"
  "s_waitcnt lgkmcnt(1)\n"
  "v_mfma_f32_16x16x32_bf16 v[40:43], a[64:67], v[24:27], v[40:43]\n"
  "ds_read_b128 v[32:35], v1 offset:10240\n"
  "s_add_u32 s74, s85, 1\n"
  "s_and_b32 s74, s74, 3\n"
  "v_mfma_f32_16x16x32_bf16 v[44:47], a[68:71], v[24:27], v[44:47]\n"
  "s_lshl_b32 s75, s74, 14\n"
  "s_add_u32 s75, s75, 86016\n"
  "v_mfma_f32_16x16x32_bf16 v[56:59], a[208:211], v[24:27], v[56:59]\n"
  "s_cmp_eq_u32 s74, 0\n"
  "s_cselect_b32 s75, 36864, s75\n"
  "v_add_u32 v98, s75, v12\n"
  "v_mfma_f32_16x16x32_bf16 v[60:63], a[212:215], v[24:27], v[60:63]\n"
  "s_add_u32 s75, s75, s68\n"
  "v_add_u32 v99, s75, v12\n"
  "s_waitcnt lgkmcnt(1)\n"
  "v_mfma_f32_16x16x32_bf16 v[40:43], a[72:75], v[28:31], v[40:43]\n"
  "ds_read_b128 v[36:39], v1 offset:11264\n"
  "v_mfma_f32_16x16x32_bf16 v[44:47], a[76:79], v[28:31], v[44:47]\n"
  "v_mfma_f32_16x16x32_bf16 v[56:59], a[216:219], v[28:31], v[56:59]\n"
  "s_add_u32 s75, s85, 0\n"
  "v_mov_b32 v6, s75\n"
  "v_mfma_f32_16x16x32_bf16 v[60:63], a[220:223], v[28:31], v[60:63]\n"
  "s_waitcnt lgkmcnt(1)\n"
  "v_mfma_f32_16x16x32_bf16 v[40:43], a[80:83], v[32:35], v[40:43]\n"
  "s_waitcnt vmcnt(4)\n"
  "s_barrier\n"
  "ds_read_b128 v[16:19], v1 offset:12288\n"
  "v_mfma_f32_16x16x32_bf16 v[44:47], a[84:87], v[32:35], v[44:47]\n"
  "buffer_store_dword v6, v5, s[36:39], s93 offen sc1\n"
  "v_mfma_f32_16x16x32_bf16 v[56:59], a[224:227], v[32:35], v[56:59]\n"
  "v_mfma_f32_16x16x32_bf16 v[60:63], a[228:231], v[32:35], v[60:63]\n"
  "s_waitcnt lgkmcnt(1)\n"
  "v_mfma_f32_16x16x32_bf16 v[40:43], a[88:91], v[36:39], v[40:43]\n"
  "ds_read_b128 v[20:23], v1 offset:13312\n"
  "v_mfma_f32_16x16x32_bf16 v[44:47], a[92:95], v[36:39], v[44:47]\n"
  "v_mfma_f32_16x16x32_bf16 v[56:59], a[232:235], v[36:39], v[56:59]\n"
  "buffer_load_dword v4, v2, s[36:39], 0 offen sc1\n"
  "v_mfma_f32_16x16x32_bf16 v[60:63], a[236:239], v[36:39], v[60:63]\n"
  "s_waitcnt lgkmcnt(1)\n"
  "v_mfma_f32_16x16x32_bf16 v[40:43], a[96:99], v[16:19], v[40:43]\n"
  "ds_read_b128 v[24:27], v1 offset:14336\n"
  "v_mfma_f32_16x16x32_bf16 v[44:47], a[100:103], v[16:19], v[44:47]\n"
  "v_mfma_f32_16x16x32_bf16 v[56:59], a[240:243], v[16:19], v[56:59]\n"
  "v_mfma_f32_16x16x32_bf16 v[60:63], a[244:247], v[16:19], v[60:63]\n"
  "s_waitcnt lgkmcnt(1)\n"
  "v_mfma_f32_16x16x32_bf16 v[40:43], a[104:107], v[20:23], v[40:43]\n"
  "ds_read_b128 v[28:31], v1 offset:15360\n"
  "v_mfma_f32_16x16x32_bf16 v[44:47], a[108:111], v[20:23], v[44:47]\n"
  "v_mfma_f32_16x16x32_bf16 v[56:59], a[248:251], v[20:23], v[56:59]\n"
  "v_mfma_f32_16x16x32_bf16 v[60:63], a[252:255], v[20:23], v[60:63]\n"
  "s_waitcnt lgkmcnt(1)\n"
  "v_mfma_f32_16x16x32_bf16 v[40:43], a[112:115], v[24:27], v[40:43]\n"
  "ds_read_b128 v[32:35], v1 offset:16384\n"
  "v_mfma_f32_16x16x32_bf16 v[44:47], a[116:119], v[24:27], v[44:47]\n"
  "ds_read_b128 v[36:39], v1 offset:17408\n"
  "v_mfma_f32_16x16x32_bf16 v[56:59], v[224:227], v[24:27], v[56:59]\n"
  "s_waitcnt vmcnt(2)\n"
  "s_barrier\n"
  "ds_read_b128 v[16:19], v1 offset:20480\n"
  "v_mfma_f32_16x16x32_bf16 v[60:63], v[228:231], v[24:27], v[60:63]\n"
  "s_waitcnt lgkmcnt(3)\n"
  "v_mfma_f32_16x16x32_bf16 v[40:43], a[120:123], v[28:31], v[40:43]\n"
  "ds_read_b128 v[20:23], v1 offset:21504\n"
  "v_mfma_f32_16x16x32_bf16 v[44:47], a[124:127], v[28:31], v[44:47]\n"
  "v_mfma_f32_16x16x32_bf16 v[56:59], v[232:235], v[28:31], v[56:59]\n"
  "v_mfma_f32_16x16x32_bf16 v[60:63], v[236:239], v[28:31], v[60:63]\n"
  "s_waitcnt lgkmcnt(3)\n"
  "v_mfma_f32_16x16x32_bf16 v[40:43], a[128:131], v[32:35], v[40:43]\n"
  "ds_read_b128 v[24:27], v1 offset:22528\n"
  "v_mfma_f32_16x16x32_bf16 v[44:47], a[132:135], v[32:35], v[44:47]\n"
  "v_mfma_f32_16x16x32_bf16 v[56:59], v[240:243], v[32:35], v[56:59]\n"
  "v_mfma_f32_16x16x32_bf16 v[60:63], v[244:247], v[32:35], v[60:63]\n"
  "s_waitcnt lgkmcnt(3)\n"
  "v_mfma_f32_16x16x32_bf16 v[40:43], a[136:139], v[36:39], v[40:43]\n"
  "ds_read_b128 v[28:31], v1 offset:23552\n"
  "v_mfma_f32_16x16x32_bf16 v[44:47], a[140:143], v[36:39], v[44:47]\n"
  "v_mfma_f32_16x16x32_bf16 v[56:59], v[248:251], v[36:39], v[56:59]\n"
  "v_mfma_f32_16x16x32_bf16 v[60:63], v[252:255], v[36:39], v[60:63]\n"
  "s_waitcnt lgkmcnt(2)\n"
  "v_mfma_f32_16x16x32_bf16 v[48:51], a[0:3], v[16:19], 0\n"
  "ds_read_b128 v[32:35], v1 offset:24576\n"
  "v_mfma_f32_16x16x32_bf16 v[52:55], a[4:7], v[16:19], 0\n"
  "v_cvt_pk_bf16_f32 v76, v40, v41\n"
  "v_cvt_pk_bf16_f32 v77, v42, v43\n"
  "v_mfma_f32_16x16x32_bf16 v[48:51], a[8:11], v[20:23], v[48:51]\n"
  "ds_read_b128 v[36:39], v1 offset:25600\n"
  "v_cvt_pk_bf16_f32 v78, v44, v45\n"
  "v_cvt_pk_bf16_f32 v79, v46, v47\n"
  "v_mfma_f32_16x16x32_bf16 v[52:55], a[12:15], v[20:23], v[52:55]\n"
  "buffer_store_dwordx4 v[76:79], v12, s[36:39], s80 offen offset:0 sc1\n"
  "s_waitcnt lgkmcnt(2)\n"
  "v_mfma_f32_16x16x32_bf16 v[48:51], a[16:19], v[24:27], v[48:51]\n"
  "ds_read_b128 v[16:19], v1 offset:26624\n"
  "v_mfma_f32_16x16x32_bf16 v[52:55], a[20:23], v[24:27], v[52:55]\n"
  "ds_write_b128 v97, v[76:79] offset:8192\n"
  "v_mfma_f32_16x16x32_bf16 v[48:51], a[24:27], v[28:31], v[48:51]\n"
  "ds_read_b128 v[20:23], v1 offset:27648\n"
  "v_mfma_f32_16x16x32_bf16 v[52:55], a[28:31], v[28:31], v[52:55]\n"
  "v_cvt_pk_bf16_f32 v80, v56, v57\n"
  "v_cvt_pk_bf16_f32 v81, v58, v59\n"
  "s_waitcnt lgkmcnt(3)\n"
  "v_mfma_f32_16x16x32_bf16 v[48:51], a[32:35], v[32:35], v[48:51]\n"
  "ds_read_b128 v[24:27], v1 offset:28672\n"
  "v_cvt_pk_bf16_f32 v82, v60, v61\n"
  "v_cvt_pk_bf16_f32 v83, v62, v63\n"
  "v_mfma_f32_16x16x32_bf16 v[52:55], a[36:39], v[32:35], v[52:55]\n"
  "v_mfma_f32_16x16x32_bf16 v[48:51], a[40:43], v[36:39], v[48:51]\n"
  "ds_read_b128 v[28:31], v1 offset:29696\n"
  "v_mfma_f32_16x16x32_bf16 v[52:55], a[44:47], v[36:39], v[52:55]\n"
  "ds_write_b128 v97, v[80:83] offset:0\n"
  "s_waitcnt lgkmcnt(3)\n"
  "v_mfma_f32_16x16x32_bf16 v[48:51], a[48:51], v[16:19], v[48:51]\n"
  "ds_read_b128 v[32:35], v1 offset:30720\n"
  "s_waitcnt vmcnt(1)\n"
  "v_subrev_u32 v6, v11, v4\n"
  "v_cmp_lt_i32 vcc, s85, v6\n"
  "v_mfma_f32_16x16x32_bf16 v[52:55], a[52:55], v[16:19], v[52:55]\n"
  "s_cmp_lt_i32 s85, 63\n"
  "s_cselect_b32 s94, -1, 0\n"
  "v_mfma_f32_16x16x32_bf16 v[48:51], a[56:59], v[20:23], v[48:51]\n"
  "ds_read_b128 v[36:39], v1 offset:31744\n"
  "v_mfma_f32_16x16x32_bf16 v[52:55], a[60:63], v[20:23], v[52:55]\n"
  "s_cmp_eq_u64 vcc, s[94:95]\n"
  "s_cselect_b32 s83, 1, 0\n"
  "s_cselect_b32 s84, s84, 0x80000000\n"
  "s_waitcnt lgkmcnt(3)\n"
  "v_mfma_f32_16x16x32_bf16 v[48:51], a[64:67], v[24:27], v[48:51]\n"
  "ds_read_b128 v[16:19], v1 offset:32768\n"
  "s_cmp_ge_i32 s85, -1\n"
  "s_cselect_b32 s83, s83, 0\n"
  "s_cselect_b32 s84, s84, 0x80000000\n"
  "v_mfma_f32_16x16x32_bf16 v[52:55], a[68:71], v[24:27], v[52:55]\n"
  "v_mfma_f32_16x16x32_bf16 v[48:51], a[72:75], v[28:31], v[48:51]\n"
  "ds_read_b128 v[20:23], v1 offset:33792\n"
  "s_mov_b32 m0, s86\n"
  "s_add_u32 s76, s84, s65\n"
  "v_mfma_f32_16x16x32_bf16 v[52:55], a[76:79], v[28:31], v[52:55]\n"
  "buffer_load_dwordx4 v7, s[36:39], s76 offen sc1 lds\n"
  "s_waitcnt lgkmcnt(2)\n"
  "v_mfma_f32_16x16x32_bf16 v[48:51], a[80:83], v[32:35], v[48:51]\n"
  "ds_read_b128 v[24:27], v1 offset:34816\n"
  "v_mfma_f32_16x16x32_bf16 v[52:55], a[84:87], v[32:35], v[52:55]\n"
  "v_mfma_f32_16x16x32_bf16 v[48:51], a[88:91], v[36:39], v[48:51]\n"
  "ds_read_b128 v[28:31], v1 offset:35840\n"
  "v_mfma_f32_16x16x32_bf16 v[52:55], a[92:95], v[36:39], v[52:55]\n"
  "s_add_u32 s76, s84, s69\n"
  "s_waitcnt lgkmcnt(2)\n"
  "v_mfma_f32_16x16x32_bf16 v[48:51], a[96:99], v[16:19], v[48:51]\n"
  "ds_read_b128 v[32:35], v1 offset:18432\n"
  "buffer_load_dwordx2 v[180:181], v96, s[36:39], s76 offen offset:0 sc1\n"
  "v_mfma_f32_16x16x32_bf16 v[52:55], a[100:103], v[16:19], v[52:55]\n"
  "v_mfma_f32_16x16x32_bf16 v[48:51], a[104:107], v[20:23], v[48:51]\n"
  "ds_read_b128 v[36:39], v1 offset:19456\n"
  "v_mfma_f32_16x16x32_bf16 v[52:55], a[108:111], v[20:23], v[52:55]\n"
  "s_waitcnt lgkmcnt(2)\n"
  "v_mfma_f32_16x16x32_bf16 v[48:51], a[112:115], v[24:27], v[48:51]\n"
  "ds_read_b128 v[16:19], v1 offset:20480\n"
  "s_add_u32 s76, s84, s69\n"
  "v_mfma_f32_16x16x32_bf16 v[52:55], a[116:119], v[24:27], v[52:55]\n"
  "buffer_load_dwordx2 v[182:183], v96, s[36:39], s76 offen offset:512 sc1\n"
  "v_mfma_f32_16x16x32_bf16 v[48:51], a[120:123], v[28:31], v[48:51]\n"
  "ds_read_b128 v[20:23], v1 offset:21504\n"
  "v_mfma_f32_16x16x32_bf16 v[52:55], a[124:127], v[28:31], v[52:55]\n"
  "s_waitcnt lgkmcnt(2)\n"
  "v_mfma_f32_16x16x32_bf16 v[48:51], a[128:131], v[32:35], v[48:51]\n"
  "ds_read_b128 v[24:27], v1 offset:22528\n"
  "v_mfma_f32_16x16x32_bf16 v[52:55], a[132:135], v[32:35], v[52:55]\n"
  "s_add_u32 s76, s84, s66\n"
  "v_mfma_f32_16x16x32_bf16 v[48:51], a[136:139], v[36:39], v[48:51]\n"
  "ds_read_b128 v[28:31], v1 offset:23552\n"
  "buffer_load_dwordx4 v[164:167], v0, s[36:39], s76 offen offset:0 sc1\n"
  "v_mfma_f32_16x16x32_bf16 v[52:55], a[140:143], v[36:39], v[52:55]\n"
  "s_waitcnt lgkmcnt(2)\n"
  "v_mfma_f32_16x16x32_bf16 v[64:67], a[144:147], v[16:19], 0\n"
  "ds_read_b128 v[32:35], v1 offset:24576\n"
  "v_mfma_f32_16x16x32_bf16 v[68:71], a[148:151], v[16:19], 0\n"
  "v_cvt_pk_bf16_f32 v76, v48, v49\n"
  "v_cvt_pk_bf16_f32 v77, v50, v51\n"
  "v_mfma_f32_16x16x32_bf16 v[64:67], a[152:155], v[20:23], v[64:67]\n"
  "ds_read_b128 v[36:39], v1 offset:25600\n"
  "v_cvt_pk_bf16_f32 v78, v52, v53\n"
  "v_cvt_pk_bf16_f32 v79, v54, v55\n"
  "v_mfma_f32_16x16x32_bf16 v[68:71], a[156:159], v[20:23], v[68:71]\n"
  "buffer_store_dwordx4 v[76:79], v12, s[36:39], s80 offen offset:256 sc1\n"
  "s_waitcnt lgkmcnt(2)\n"
  "v_mfma_f32_16x16x32_bf16 v[64:67], a[160:163], v[24:27], v[64:67]\n"
  "ds_read_b128 v[16:19], v1 offset:26624\n"
  "v_mfma_f32_16x16x32_bf16 v[68:71], a[164:167], v[24:27], v[68:71]\n"
  "ds_write_b128 v97, v[76:79] offset:8448\n"
  "v_mfma_f32_16x16x32_bf16 v[64:67], a[168:171], v[28:31], v[64:67]\n"
  "ds_read_b128 v[20:23], v1 offset:27648\n"
  "v_mfma_f32_16x16x32_bf16 v[68:71], a[172:175], v[28:31], v[68:71]\n"
  "s_waitcnt lgkmcnt(3)\n"
  "v_mfma_f32_16x16x32_bf16 v[64:67], a[176:179], v[32:35], v[64:67]\n"
  "ds_read_b128 v[24:27], v1 offset:28672\n"
  "s_add_u32 s76, s84, s66\n"
  "v_mfma_f32_16x16x32_bf16 v[68:71], a[180:183], v[32:35], v[68:71]\n"
  "buffer_load_dwordx4 v[168:171], v0, s[36:39], s76 offen offset:1024 sc1\n"
  "v_mfma_f32_16x16x32_bf16 v[64:67], a[184:187], v[36:39], v[64:67]\n"
  "ds_read_b128 v[28:31], v1 offset:29696\n"
  "v_mfma_f32_16x16x32_bf16 v[68:71], a[188:191], v[36:39], v[68:71]\n"
  "s_waitcnt lgkmcnt(2)\n"
  "v_mfma_f32_16x16x32_bf16 v[64:67], a[192:195], v[16:19], v[64:67]\n"
  "ds_read_b128 v[32:35], v1 offset:30720\n"
  "v_mfma_f32_16x16x32_bf16 v[68:71], a[196:199], v[16:19], v[68:71]\n"
  "s_add_u32 s76, s84, s66\n"
  "v_mfma_f32_16x16x32_bf16 v[64:67], a[200:203], v[20:23], v[64:67]\n"
  "ds_read_b128 v[36:39], v1 offset:31744\n"
  "buffer_load_dwordx4 v[172:175], v0, s[36:39], s76 offen offset:2048 sc1\n"
  "v_mfma_f32_16x16x32_bf16 v[68:71], a[204:207], v[20:23], v[68:71]\n"
  "s_waitcnt lgkmcnt(2)\n"
  "v_mfma_f32_16x16x32_bf16 v[64:67], a[208:211], v[24:27], v[64:67]\n"
  "ds_read_b128 v[16:19], v1 offset:32768\n"
  "v_mfma_f32_16x16x32_bf16 v[68:71], a[212:215], v[24:27], v[68:71]\n"
  "v_mfma_f32_16x16x32_bf16 v[64:67], a[216:219], v[28:31], v[64:67]\n"
  "ds_read_b128 v[20:23], v1 offset:33792\n"
  "s_add_u32 s76, s84, s66\n"
  "v_mfma_f32_16x16x32_bf16 v[68:71], a[220:223], v[28:31], v[68:71]\n"
  "buffer_load_dwordx4 v[176:179], v0, s[36:39], s76 offen offset:3072 sc1\n"
  "s_waitcnt lgkmcnt(2)\n"
  "v_mfma_f32_16x16x32_bf16 v[64:67], a[224:227], v[32:35], v[64:67]\n"
  "ds_read_b128 v[24:27], v1 offset:34816\n"
  "v_mfma_f32_16x16x32_bf16 v[68:71], a[228:231], v[32:35], v[68:71]\n"
  "v_mfma_f32_16x16x32_bf16 v[64:67], a[232:235], v[36:39], v[64:67]\n"
  "ds_read_b128 v[28:31], v1 offset:35840\n"
  "v_mfma_f32_16x16x32_bf16 v[68:71], a[236:239], v[36:39], v[68:71]\n"
  "buffer_load_dword v4, v2, s[36:39], 0 offen sc1\n"
  "s_add_u32 s75, s70, 0\n"
  "v_mov_b32 v6, s75\n"
  "s_waitcnt lgkmcnt(2)\n"
  "v_mfma_f32_16x16x32_bf16 v[64:67], a[240:243], v[16:19], v[64:67]\n"
  "ds_read_b128 v[32:35], v1 offset:18432\n"
  "ds_read_b128 v[100:103], v99 offset:0\n"
  "v_mfma_f32_16x16x32_bf16 v[68:71], a[244:247], v[16:19], v[68:71]\n"
  "ds_read_b128 v[116:119], v98 offset:8192\n"
  "v_mfma_f32_16x16x32_bf16 v[64:67], a[248:251], v[20:23], v[64:67]\n"
  "ds_read_b128 v[36:39], v1 offset:19456\n"
  "ds_read_b128 v[132:135], v98 offset:8448\n"
  "v_mfma_f32_16x16x32_bf16 v[68:71], a[252:255], v[20:23], v[68:71]\n"
  "ds_read_b128 v[104:107], v99 offset:2048\n"
  "s_waitcnt lgkmcnt(6)\n"
  "v_mfma_f32_16x16x32_bf16 v[64:67], v[224:227], v[24:27], v[64:67]\n"
  "ds_read_b128 v[120:123], v98 offset:10240\n"
  "v_mfma_f32_16x16x32_bf16 v[68:71], v[228:231], v[24:27], v[68:71]\n"
  "ds_read_b128 v[136:139], v98 offset:10496\n"
  "v_mfma_f32_16x16x32_bf16 v[64:67], v[232:235], v[28:31], v[64:67]\n"
  "ds_read_b128 v[108:111], v99 offset:4096\n"
  "v_mfma_f32_16x16x32_bf16 v[68:71], v[236:239], v[28:31], v[68:71]\n"
  "ds_read_b128 v[124:127], v98 offset:12288\n"
  "s_waitcnt lgkmcnt(6)\n"
  "v_mfma_f32_16x16x32_bf16 v[64:67], v[240:243], v[32:35], v[64:67]\n"
  "ds_read_b128 v[140:143], v98 offset:12544\n"
  "v_mfma_f32_16x16x32_bf16 v[68:71], v[244:247], v[32:35], v[68:71]\n"
  "ds_read_b128 v[112:115], v99 offset:6144\n"
  "v_mfma_f32_16x16x32_bf16 v[64:67], v[248:251], v[36:39], v[64:67]\n"
  "ds_read_b128 v[128:131], v98 offset:14336\n"
  "v_mfma_f32_16x16x32_bf16 v[68:71], v[252:255], v[36:39], v[68:71]\n"
  "ds_read_b128 v[144:147], v98 offset:14592\n"
  "s_waitcnt lgkmcnt(9)\n"
  "v_mfma_f32_16x16x32_bf16 v[184:187], v[116:119], v[100:103], 0\n"
  "s_xor_b32 s81, s81, 65536\n"
  "v_xor_b32 v1, 65536, v1\n"
  "s_mov_b32 s82, 0\n"
  "v_mfma_f32_16x16x32_bf16 v[188:191], v[132:135], v[100:103], 0\n"
  "s_waitcnt lgkmcnt(6)\n"
  "v_mfma_f32_16x16x32_bf16 v[184:187], v[120:123], v[104:107], v[184:187]\n"
  "v_cvt_pk_bf16_f32 v80, v64, v65\n"
  "v_cvt_pk_bf16_f32 v81, v66, v67\n"
  "v_mfma_f32_16x16x32_bf16 v[188:191], v[136:139], v[104:107], v[188:191]\n"
  "v_cvt_pk_bf16_f32 v82, v68, v69\n"
  "v_cvt_pk_bf16_f32 v83, v70, v71\n"
  "s_waitcnt lgkmcnt(3)\n"
  "v_mfma_f32_16x16x32_bf16 v[184:187], v[124:127], v[108:111], v[184:187]\n"
  "v_mfma_f32_16x16x32_bf16 v[188:191], v[140:143], v[108:111], v[188:191]\n"
  "s_waitcnt lgkmcnt(0)\n"
  "v_mfma_f32_16x16x32_bf16 v[184:187], v[128:131], v[112:115], v[184:187]\n"
  "ds_write_b128 v97, v[80:83] offset:256\n"
  "v_mfma_f32_16x16x32_bf16 v[188:191], v[144:147], v[112:115], v[188:191]\n"
  "s_waitcnt vmcnt(4)\n"
  "s_barrier\n"
  "buffer_store_dword v6, v5, s[36:39], s92 offen sc1\n"
  "s_add_u32 s70, s70, 1\n"
  "s_add_u32 s85, s85, 1\n"
  "s_sub_u32 s77, s70, 1\n"
  "s_add_u32 s78, s70, 0\n"
  "s_and_b32 s78, s78, s61\n"
  "s_mul_i32 s78, s78, s60\n"
  "s_lshr_b32 s76, s78, 8\n"
  "s_and_b32 s76, s76, 8192\n"
  "s_add_u32 s78, s78, s59\n"
  "s_xor_b32 s78, s78, s76\n"
  "s_add_u32 s79, s81, s57\n"
  "v_cndmask_b32 v184, v72, v184, s[4:5]\n"
  "v_cndmask_b32 v188, v72, v188, s[12:13]\n"
  "v_cndmask_b32 v185, v72, v185, s[6:7]\n"
  "v_cndmask_b32 v189, v72, v189, s[14:15]\n"
  "v_cndmask_b32 v186, v72, v186, s[8:9]\n"
  "v_cndmask_b32 v190, v72, v190, s[16:17]\n"
  "v_cndmask_b32 v187, v72, v187, s[10:11]\n"
  "v_cndmask_b32 v191, v72, v191, s[18:19]\n"
  "v_max3_f32 v212, v184, v185, v186\n"
  "v_max3_f32 v213, v188, v189, v190\n"
  "v_max3_f32 v212, v212, v187, v191\n"
  "v_max_f32 v212, v212, v213\n"
  "s_cmp_lt_i32 s85, 0\n"
  "s_cbranch_scc1 L_first_%=\n"
  "L_att_%=:\n"
  "s_waitcnt vmcnt(1)\n"
  "s_cmp_eq_u32 s83, 0\n"
  "s_cbranch_scc1 L_aslow_%=\n"
  "L_aok_%=:\n"
  "s_barrier\n"
  "ds_read_b64_tr_b16 v[148:149], v13 offset:0\n"
  "ds_read_b64_tr_b16 v[150:151], v13 offset:256\n"
  "ds_read_b64_tr_b16 v[152:153], v13 offset:1024\n"
  "ds_read_b64_tr_b16 v[154:155], v13 offset:1280\n"
  "ds_read_b64_tr_b16 v[156:157], v13 offset:2048\n"
  "ds_read_b64_tr_b16 v[158:159], v13 offset:2304\n"
  "ds_read_b64_tr_b16 v[160:161], v13 offset:3072\n"
  "ds_read_b64_tr_b16 v[162:163], v13 offset:3328\n"
  "v_subrev_u32 v6, v3, v4\n"
  "v_cmp_lt_i32 vcc, s77, v6\n"
  "s_cmp_lt_i32 s77, 63\n"
  "s_cselect_b32 s94, -1, 0\n"
  "s_cmp_eq_u64 vcc, s[94:95]\n"
  "s_cselect_b32 s71, 1, 0\n"
  "s_cselect_b32 s78, s78, 0x80000000\n"
  "s_waitcnt lgkmcnt(6)\n"
  "v_mfma_f32_16x16x32_bf16 v[192:195], v[148:151], v[100:103], 0\n"
  "s_add_u32 m0, s79, 0\n"
  "s_add_u32 s76, s78, 0\n"
  "buffer_load_dwordx4 v12, s[36:39], s76 offen sc1 lds\n"
  "s_waitcnt lgkmcnt(4)\n"
  "v_mfma_f32_16x16x32_bf16 v[192:195], v[152:155], v[104:107], v[192:195]\n"
  "s_add_u32 s80, s85, 0\n"
  "s_and_b32 s80, s80, 15\n"
  "s_waitcnt lgkmcnt(2)\n"
  "v_mfma_f32_16x16x32_bf16 v[192:195], v[156:159], v[108:111], v[192:195]\n"
  "s_lshl_b32 s80, s80, 22\n"
  "s_add_u32 s80, s80, s67\n"
  "s_waitcnt lgkmcnt(0)\n"
  "v_mfma_f32_16x16x32_bf16 v[192:195], v[160:163], v[112:115], v[192:195]\n"
  "s_nop 3\n"
  "s_add_u32 m0, s79, 4096\n"
  "s_add_u32 s76, s78, 16384\n"
  "buffer_load_dwordx4 v12, s[36:39], s76 offen sc1 lds\n"
  "s_nop 0\n"
  "v_max3_f32 v213, v212, v192, v193\n"
  "v_max3_f32 v212, v212, v192, v193\n"
  "s_nop 1\n"
  "v_permlane16_swap_b32 v212, v213\n"
  "v_max_f32 v212, v212, v213\n"
  "v_max_f32 v213, v212, v213\n"
  "s_add_u32 m0, s79, 8192\n"
  "s_add_u32 s76, s78, 32768\n"
  "buffer_load_dwordx4 v12, s[36:39], s76 offen sc1 lds\n"
  "v_permlane32_swap_b32 v212, v213\n"
  "v_max_f32 v212, v212, v213\n"
  "v_mul_f32 v212, 0xbe0293ee, v212\n"
  "s_mov_b32 s90, 0x3e0293ee\n"
  "v_pk_fma_f32 v[184:185], v[184:185], s[90:91], v[212:213] op_sel_hi:[1,0,0]\n"
  "v_pk_fma_f32 v[186:187], v[186:187], s[90:91], v[212:213] op_sel_hi:[1,0,0]\n"
  "v_pk_fma_f32 v[188:189], v[188:189], s[90:91], v[212:213] op_sel_hi:[1,0,0]\n"
  "v_pk_fma_f32 v[190:191], v[190:191], s[90:91], v[212:213] op_sel_hi:[1,0,0]\n"
  "v_pk_fma_f32 v[192:193], v[192:193], s[90:91], v[212:213] op_sel_hi:[1,0,0]\n"
  "v_exp_f32 v184, v184\n"
  "v_exp_f32 v185, v185\n"
  "s_add_u32 m0, s79, 12288\n"
  "s_add_u32 s76, s78, 49152\n"
  "buffer_load_dwordx4 v12, s[36:39], s76 offen sc1 lds\n"
  "v_exp_f32 v186, v186\n"
  "v_exp_f32 v187, v187\n"
  "v_exp_f32 v188, v188\n"
  "v_exp_f32 v189, v189\n"
  "v_exp_f32 v190, v190\n"
  "v_exp_f32 v191, v191\n"
  "v_exp_f32 v192, v192\n"
  "s_add_u32 m0, s79, 16384\n"
  "s_add_u32 s76, s78, s56\n"
  "buffer_load_dwordx4 v12, s[36:39], s76 offen sc1 lds\n"
  "v_exp_f32 v193, v193\n"
  "v_cvt_pk_bf16_f32 v216, v184, v185\n"
  "v_cvt_pk_bf16_f32 v217, v186, v187\n"
  "v_cvt_pk_bf16_f32 v218, v188, v189\n"
  "v_cvt_pk_bf16_f32 v219, v190, v191\n"
  "v_cvt_pk_bf16_f32 v220, v192, v193\n"
  "s_waitcnt vmcnt(5)\n"
  "v_mfma_f32_16x16x32_bf16 v[88:91], v[84:87], v[216:219], 0\n"
  "v_cvt_pk_bf16_f32 v223, v192, v193\n"
  "v_mfma_f32_16x16x32_bf16 v[196:199], v[164:167], v[216:219], 0\n"
  "v_mfma_f32_16x16x32_bf16 v[200:203], v[168:171], v[216:219], 0\n"
  "buffer_load_dword v4, v2, s[36:39], 0 offen sc1\n"
  "v_mfma_f32_16x16x32_bf16 v[204:207], v[172:175], v[216:219], 0\n"
  "v_mfma_f32_16x16x32_bf16 v[208:211], v[176:179], v[216:219], 0\n"
  "v_mfma_f32_16x16x16_bf16 v[88:91], v[84:85], v[220:221], v[88:91]\n"
  "v_mfma_f32_16x16x16_bf16 v[196:199], v[180:181], v[220:221], v[196:199]\n"
  "v_mfma_f32_16x16x16_bf16 v[200:203], v[180:181], v[222:223], v[200:203]\n"
  "v_mfma_f32_16x16x16_bf16 v[204:207], v[182:183], v[220:221], v[204:207]\n"
  "v_mfma_f32_16x16x16_bf16 v[208:211], v[182:183], v[222:223], v[208:211]\n"
  "s_nop 3\n"
  "v_rcp_f32 v212, v88\n"
  "s_nop 0\n"
  "v_mul_f32 v196, v196, v212\n"
  "v_mul_f32 v197, v197, v212\n"
  "v_mul_f32 v198, v198, v212\n"
  "v_mul_f32 v199, v199, v212\n"
  "v_mul_f32 v200, v200, v212\n"
  "v_mul_f32 v201, v201, v212\n"
  "v_mul_f32 v202, v202, v212\n"
  "v_mul_f32 v203, v203, v212\n"
  "v_cvt_pk_bf16_f32 v76, v196, v197\n"
  "v_cvt_pk_bf16_f32 v77, v198, v199\n"
  "v_cvt_pk_bf16_f32 v78, v200, v201\n"
  "v_cvt_pk_bf16_f32 v79, v202, v203\n"
  "buffer_store_dwordx4 v[76:79], v15, s[36:39], s80 offen offset:0 sc1\n"
  "v_pk_mul_f32 v[204:205], v[204:205], v[212:213] op_sel_hi:[1,0]\n"
  "v_pk_mul_f32 v[206:207], v[206:207], v[212:213] op_sel_hi:[1,0]\n"
  "v_pk_mul_f32 v[208:209], v[208:209], v[212:213] op_sel_hi:[1,0]\n"
  "v_pk_mul_f32 v[210:211], v[210:211], v[212:213] op_sel_hi:[1,0]\n"
  "v_cvt_pk_bf16_f32 v80, v204, v205\n"
  "v_cvt_pk_bf16_f32 v81, v206, v207\n"
  "v_cvt_pk_bf16_f32 v82, v208, v209\n"
  "v_cvt_pk_bf16_f32 v83, v210, v211\n"
  "buffer_store_dwordx4 v[80:83], v15, s[36:39], s80 offen offset:2048 sc1\n"
  "s_mov_b32 s83, 0\n"
  "s_mov_b32 s82, s71\n"
  "s_cmp_lg_u32 s71, 0\n"
  "s_cbranch_scc1 L_pf_%=\n"
  "s_waitcnt vmcnt(2)\n"
  "v_subrev_u32 v6, v3, v4\n"
  "s_add_u32 s74, s70, 0\n"
  "v_cmp_le_i32 vcc, s74, v6\n"
  "s_cmp_eq_u64 vcc, exec\n"
  "s_cbranch_scc0 L_idle_%=\n"
  "s_cmp_ge_i32 s70, 64\n"
  "s_cbranch_scc1 L_idle_%=\n"
  "s_add_u32 s78, s70, 0\n"
  "s_and_b32 s78, s78, s61\n"
  "s_mul_i32 s78, s78, s60\n"
  "s_lshr_b32 s76, s78, 8\n"
  "s_and_b32 s76, s76, 8192\n"
  "s_add_u32 s78, s78, s59\n"
  "s_xor_b32 s78, s78, s76\n"
  "s_add_u32 s79, s81, s57\n"
  "s_add_u32 m0, s79, 0\n"
  "s_add_u32 s76, s78, 0\n"
  "buffer_load_dwordx4 v12, s[36:39], s76 offen sc1 lds\n"
  "s_add_u32 m0, s79, 4096\n"
  "s_add_u32 s76, s78, 16384\n"
  "buffer_load_dwordx4 v12, s[36:39], s76 offen sc1 lds\n"
  "s_add_u32 m0, s79, 8192\n"
  "s_add_u32 s76, s78, 32768\n"
  "buffer_load_dwordx4 v12, s[36:39], s76 offen sc1 lds\n"
  "s_add_u32 m0, s79, 12288\n"
  "s_add_u32 s76, s78, 49152\n"
  "buffer_load_dwordx4 v12, s[36:39], s76 offen sc1 lds\n"
  "s_add_u32 m0, s79, 16384\n"
  "s_add_u32 s76, s78, s56\n"
  "buffer_load_dwordx4 v12, s[36:39], s76 offen sc1 lds\n"
  "s_waitcnt vmcnt(5)\n"
  "s_add_u32 s75, s85, 0\n"
  "s_lshl_b32 s74, s75, 2\n"
  "s_add_u32 s74, s74, 163584\n"
  "v_mov_b32 v16, s74\n"
  "v_mov_b32 v6, 1\n"
  "s_mov_b64 exec, 1\n"
  "ds_add_rtn_u32 v16, v16, v6\n"
  "s_mov_b64 exec, -1\n"
  "v_mov_b32 v6, s75\n"
  "s_waitcnt lgkmcnt(0)\n"
  "v_readfirstlane_b32 s74, v16\n"
  "s_cmp_eq_u32 s74, 3\n"
  "s_cselect_b32 s74, s64, 0x80000000\n"
  "buffer_store_dword v6, v5, s[36:39], s74 offen sc1\n"
  "s_waitcnt vmcnt(3)\n"
  "s_branch L_go_%=\n"
  "L_idle_%=:\n"
  "buffer_load_dword v4, v2, s[36:39], 0 offen sc1\n"
  "s_waitcnt vmcnt(1)\n"
  "s_add_u32 s75, s85, 0\n"
  "s_lshl_b32 s74, s75, 2\n"
  "s_add_u32 s74, s74, 163584\n"
  "v_mov_b32 v16, s74\n"
  "v_mov_b32 v6, 1\n"
  "s_mov_b64 exec, 1\n"
  "ds_add_rtn_u32 v16, v16, v6\n"
  "s_mov_b64 exec, -1\n"
  "v_mov_b32 v6, s75\n"
  "s_waitcnt lgkmcnt(0)\n"
  "v_readfirstlane_b32 s74, v16\n"
  "s_cmp_eq_u32 s74, 3\n"
  "s_cselect_b32 s74, s64, 0x80000000\n"
  "buffer_store_dword v6, v5, s[36:39], s74 offen sc1\n"
  "s_waitcnt vmcnt(1)\n"
  "s_cmp_ge_i32 s70, 64\n"
  "s_cbranch_scc1 L_fin_%=\n"
  "v_subrev_u32 v6, v3, v4\n"
  "s_add_u32 s74, s70, 0\n"
  "v_cmp_le_i32 vcc, s74, v6\n"
  "s_cmp_eq_u64 vcc, exec\n"
  "s_cbranch_scc1 L_poll_x_%=\n"
  "L_first_%=:\n"
  "s_cmp_lg_u32 s72, 0\n"
  "s_cselect_b32 s73, 0, 32768\n"
  "v_add_u32 v6, s70, v3\n"
  "L_poll_%=:\n"
  "buffer_load_dword v4, v2, s[36:39], 0 offen sc1\n"
  "s_sub_u32 s73, s73, 1\n"
  "s_cselect_b32 s72, 1, s72\n"
  "s_cselect_b64 s[2:3], 0, -1\n"
  "s_waitcnt vmcnt(0)\n"
  "v_cmp_gt_i32 vcc, v6, v4\n"
  "s_and_b64 vcc, vcc, s[2:3]\n"
  "s_cbranch_scc1 L_poll_%=\n"
  "L_poll_x_%=:\n"
  "s_add_u32 s78, s70, 0\n"
  "s_and_b32 s78, s78, s61\n"
  "s_mul_i32 s78, s78, s60\n"
  "s_lshr_b32 s76, s78, 8\n"
  "s_and_b32 s76, s76, 8192\n"
  "s_add_u32 s78, s78, s59\n"
  "s_xor_b32 s78, s78, s76\n"
  "s_add_u32 s79, s81, s57\n"
  "s_add_u32 m0, s79, 0\n"
  "s_add_u32 s76, s78, 0\n"
  "buffer_load_dwordx4 v12, s[36:39], s76 offen sc1 lds\n"
  "s_add_u32 m0, s79, 4096\n"
  "s_add_u32 s76, s78, 16384\n"
  "buffer_load_dwordx4 v12, s[36:39], s76 offen sc1 lds\n"
  "s_add_u32 m0, s79, 8192\n"
  "s_add_u32 s76, s78, 32768\n"
  "buffer_load_dwordx4 v12, s[36:39], s76 offen sc1 lds\n"
  "s_add_u32 m0, s79, 12288\n"
  "s_add_u32 s76, s78, 49152\n"
  "buffer_load_dwordx4 v12, s[36:39], s76 offen sc1 lds\n"
  "s_add_u32 m0, s79, 16384\n"
  "s_add_u32 s76, s78, s56\n"
  "buffer_load_dwordx4 v12, s[36:39], s76 offen sc1 lds\n"
  "s_waitcnt vmcnt(2)\n"
  "s_branch L_go_%=\n"
  "L_aslow_%=:\n"
  "v_subrev_u32 v6, v11, v4\n"
  "s_add_u32 s74, s85, 0\n"
  "v_cmp_le_i32 vcc, s74, v6\n"
  "s_cmp_eq_u64 vcc, exec\n"
  "s_cbranch_scc1 L_apoll_x_%=\n"
  "s_cmp_lg_u32 s72, 0\n"
  "s_cselect_b32 s73, 0, 32768\n"
  "v_add_u32 v6, s85, v11\n"
  "L_apoll_%=:\n"
  "buffer_load_dword v4, v2, s[36:39], 0 offen sc1\n"
  "s_sub_u32 s73, s73, 1\n"
  "s_cselect_b32 s72, 1, s72\n"
  "s_cselect_b64 s[2:3], 0, -1\n"
  "s_waitcnt vmcnt(0)\n"
  "v_cmp_gt_i32 vcc, v6, v4\n"
  "s_and_b64 vcc, vcc, s[2:3]\n"
  "s_cbranch_scc1 L_apoll_%=\n"
  "L_apoll_x_%=:\n"
  "s_add_u32 s84, s85, 0\n"
  "s_and_b32 s84, s84, 15\n"
  "s_lshl_b32 s84, s84, 22\n"
  "s_add_u32 s84, s84, s87\n"
  "s_mov_b32 m0, s86\n"
  "s_add_u32 s76, s84, s65\n"
  "buffer_load_dwordx4 v7, s[36:39], s76 offen sc1 lds\n"
  "s_add_u32 s76, s84, s69\n"
  "buffer_load_dwordx2 v[180:181], v96, s[36:39], s76 offen offset:0 sc1\n"
  "s_add_u32 s76, s84, s69\n"
  "buffer_load_dwordx2 v[182:183], v96, s[36:39], s76 offen offset:512 sc1\n"
  "s_add_u32 s76, s84, s66\n"
  "buffer_load_dwordx4 v[164:167], v0, s[36:39], s76 offen offset:0 sc1\n"
  "s_add_u32 s76, s84, s66\n"
  "buffer_load_dwordx4 v[168:171], v0, s[36:39], s76 offen offset:1024 sc1\n"
  "s_add_u32 s76, s84, s66\n"
  "buffer_load_dwordx4 v[172:175], v0, s[36:39], s76 offen offset:2048 sc1\n"
  "s_add_u32 s76, s84, s66\n"
  "buffer_load_dwordx4 v[176:179], v0, s[36:39], s76 offen offset:3072 sc1\n"
  "s_waitcnt vmcnt(6)\n"
  "s_branch L_aok_%=\n"
  "L_fin_%=:\n"
  "s_cmp_eq_u32 s85, 63\n"
  "s_cbranch_scc1 L_done_%=\n"
  "s_barrier\n"
  "s_add_u32 s74, s85, 1\n"
  "s_and_b32 s74, s74, 3\n"
  "s_lshl_b32 s75, s74, 14\n"
  "s_add_u32 s75, s75, 86016\n"
  "s_cmp_eq_u32 s74, 0\n"
  "s_cselect_b32 s75, 36864, s75\n"
  "v_add_u32 v98, s75, v12\n"
  "s_add_u32 s75, s75, s68\n"
  "v_add_u32 v99, s75, v12\n"
  "ds_read_b128 v[100:103], v99 offset:0\n"
  "ds_read_b128 v[116:119], v98 offset:8192\n"
  "ds_read_b128 v[132:135], v98 offset:8448\n"
  "ds_read_b128 v[104:107], v99 offset:2048\n"
  "ds_read_b128 v[120:123], v98 offset:10240\n"
  "ds_read_b128 v[136:139], v98 offset:10496\n"
  "ds_read_b128 v[108:111], v99 offset:4096\n"
  "ds_read_b128 v[124:127], v98 offset:12288\n"
  "ds_read_b128 v[140:143], v98 offset:12544\n"
  "ds_read_b128 v[112:115], v99 offset:6144\n"
  "ds_read_b128 v[128:131], v98 offset:14336\n"
  "ds_read_b128 v[144:147], v98 offset:14592\n"
  "s_waitcnt lgkmcnt(9)\n"
  "v_mfma_f32_16x16x32_bf16 v[184:187], v[116:119], v[100:103], 0\n"
  "v_mfma_f32_16x16x32_bf16 v[188:191], v[132:135], v[100:103], 0\n"
  "s_waitcnt lgkmcnt(6)\n"
  "v_mfma_f32_16x16x32_bf16 v[184:187], v[120:123], v[104:107], v[184:187]\n"
  "v_mfma_f32_16x16x32_bf16 v[188:191], v[136:139], v[104:107], v[188:191]\n"
  "s_waitcnt lgkmcnt(3)\n"
  "v_mfma_f32_16x16x32_bf16 v[184:187], v[124:127], v[108:111], v[184:187]\n"
  "v_mfma_f32_16x16x32_bf16 v[188:191], v[140:143], v[108:111], v[188:191]\n"
  "s_waitcnt lgkmcnt(0)\n"
  "v_mfma_f32_16x16x32_bf16 v[184:187], v[128:131], v[112:115], v[184:187]\n"
  "v_mfma_f32_16x16x32_bf16 v[188:191], v[144:147], v[112:115], v[188:191]\n"
  "s_add_u32 s70, s70, 1\n"
  "s_add_u32 s85, s85, 1\n"
  "s_sub_u32 s77, s70, 1\n"
  "s_add_u32 s78, s70, 0\n"
  "s_and_b32 s78, s78, s61\n"
  "s_mul_i32 s78, s78, s60\n"
  "s_lshr_b32 s76, s78, 8\n"
  "s_and_b32 s76, s76, 8192\n"
  "s_add_u32 s78, s78, s59\n"
  "s_xor_b32 s78, s78, s76\n"
  "s_add_u32 s79, s81, s57\n"
  "v_cndmask_b32 v184, v72, v184, s[4:5]\n"
  "v_cndmask_b32 v188, v72, v188, s[12:13]\n"
  "v_cndmask_b32 v185, v72, v185, s[6:7]\n"
  "v_cndmask_b32 v189, v72, v189, s[14:15]\n"
  "v_cndmask_b32 v186, v72, v186, s[8:9]\n"
  "v_cndmask_b32 v190, v72, v190, s[16:17]\n"
  "v_cndmask_b32 v187, v72, v187, s[10:11]\n"
  "v_cndmask_b32 v191, v72, v191, s[18:19]\n"
  "v_max3_f32 v212, v184, v185, v186\n"
  "v_max3_f32 v213, v188, v189, v190\n"
  "v_max3_f32 v212, v212, v187, v191\n"
  "v_max_f32 v212, v212, v213\n"
  "s_branch L_att_%=\n"
  "L_done_%=:\n"
  "s_waitcnt vmcnt(0)\n"
  "L_c1_%=:\n"
  "s_endpgm\n"
      :
      : "{v0}"(ln * 16u), "{v1}"(ln * 16u), "{v3}"(xl ? 0 : NEVER),
        "{v7}"((((ln >> 2) & 3u) * 16u + g * 4u + (ln & 3u)) * 16u), "{v8}"(a.voff), "{v9}"(a.ra),
        "{v2}"(ln < 4 ? c.flag(F_C + h) : (ln == 4 ? c.flag(F_V + h / 3) : (ln < 9 ? c.dflag(c.lane - 5) : (xl ? c.xflag(c.lane - 12) : OOB)))),
        "{v11}"(ln < 5 ? 0 : (ln < 9 ? -RING : NEVER)), "{v12}"(g * 512u + n * 16u),
        "{v13}"(53248u + (g & 1u) * 512u + (ln & 3u) * 64u + ((ln >> 2) & 3u) * 16u + (g >> 1) * 8u), "{v14}"((int)(16u * qh + n) - (int)(4u * g)),
        "{v15}"((8u * dh + g) * 512u + (16u * qh + n) * 16u), "{v96}"(ln * 8u),
        "{s[40:43]}"(mkrsrc(p.Wk, 576 * 4096 * 2)), "{s[44:47]}"(mkrsrc(p.Wq, 576 * 4096 * 2)), COMMON,
        "{s[64:67]}"(uni((v4u){c.flag(F_XO + h), TR(L_KVM) + hoff + w * 1024u, TR(L_V) + hoff + dh * 4096u, c.slab0(0) + TR(L_OI) + hoff})),
        "{s[68:69]}"((v2u){qh * 256u, TR(L_KVM) + hoff + 4096u + dh * 1024u}), "{s87}"(c.slab0(0))
      : OWNED);
}

typedef unsigned v16u __attribute__((ext_vector_type(16)));
DI void role_r(Ctx& c, const Params& p, int stg, int tg) {
  const unsigned ln = (unsigned)c.lane, w = (unsigned)c.wave, n = ln & 15u, g = ln >> 4;
  v16u sel;
#pragma unroll
  for (int j = 0; j < 4; ++j) {
    const unsigned lo = n == 2u * j ? 0x3f80u : (n == 2u * j + 1 ? 0x3f800000u : 0u), hi = n == 2u * j + 8 ? 0x3f80u : (n == 2u * j + 9 ? 0x3f800000u : 0u);
    sel[j] = lo; sel[4 + j] = hi;
    sel[8 + j] = g == 0 ? lo : (g == 1 ? hi : 0u);
    sel[12 + j] = g == 2 ? lo : (g == 3 ? hi : 0u);
  }
  const unsigned tok = (unsigned)(8 * tg) + (n & 7u), tokb = tok * 16u;
  const rsrc_t rb = mkrsrc(p.b2, 576 * 2);
  v16f cb; v4f cb4;
#pragma unroll
  for (int d = 0; d < 5; ++d) {
    const unsigned i = 2u * d + (g >> 1);
    const v2u x = __builtin_amdgcn_raw_buffer_load_b64(rb, (stg && i < 9) ? (18u * w + 2u * i + (n >> 3)) * 16u + (g & 1u) * 8u : OOB, 0, 0);
    const v4f y = {asf(x[0] << 16), asf(x[0] & 0xffff0000u), asf(x[1] << 16), asf(x[1] & 0xffff0000u)};
    if (d < 4) { cb[4 * d] = y[0]; cb[4 * d + 1] = y[1]; cb[4 * d + 2] = y[2]; cb[4 * d + 3] = y[3]; } else cb4 = y;
  }
  const bool last = stg && c.L;
  auto af = [&](unsigned o) { return TR(o * 512u) + tokb; };
  auto rm = [&](unsigned o) { return tok * 1152u + o * 16u; };
  auto pk = [&](unsigned j, bool row) { const unsigned i = 4u * j + g, o = 18u * w + 2u * i + (n >> 3); return i < 9 ? (row ? rm(o) : af(o)) : OOB; };
  auto oo = [&](unsigned h) { return 18u * w + 2u * (2u * (2u * h + (g & 1u)) + (g >> 1)) + (n >> 3); };
  const unsigned o4 = 18u * w + 16u + (n >> 3), XO = TR(40 * KB);
  const unsigned in0 = af(oo(0)) + XO, in1 = af(oo(1)) + XO, in4 = g < 2 ? af(o4) + (g ? 0u : XO) : OOB;
  const unsigned ox0 = last ? rm(oo(0)) : (stg ? in0 : af(oo(0))), ox1 = last ? rm(oo(1)) : (stg ? in1 : af(oo(1)));
  const unsigned ox4 = last ? (g == 0 ? rm(o4) : OOB) : (stg ? in4 : (g < 2 ? af(o4) + (g ? XO : 0u) : OOB));
  const unsigned np = stg ? 15u : 12u;
  const bool rw = ln == 20 || ln == 21;
  const unsigned fword = stg ? c.dflag(tg) : c.flag(F_R1 + tg);
  const unsigned srcb = stg ? c.slab1(0) + TR(L_X1) : c.xin() + XO, srcm = stg ? 4 * MB : 256 * KB, srck = stg ? RING - 1 : 63;
  const unsigned dstb = c.oflag(F_R0 + 4 * stg + tg);
#define R_OPERANDS                                                                                                                    \
  "{v0}"(g * 81920u + (n >> 3) * 512u + tokb), "{v1}"((n & 7u) * 32u),                                                                \
  "{v2}"(ln < np ? c.flag((stg ? F_F2 : F_O) + c.lane) : (rw ? (stg ? c.flag(F_R1 + tg) : c.xflag(tg + 4 * (c.lane & 1))) : OOB)),   \
  "{v3}"(ln < np ? 0 : NEVER), "{v7}"((n & 7u) * 32u + w * 8u), "{v9}"(rw ? 0 : NEVER),                                               \
  "{v10}"(pk(0, false)), "{v11}"(pk(1, false)), "{v12}"(pk(2, false)), "{v13}"(ox0), "{v14}"(ox1), "{v15}"(ox4),                      \
  "{v[16:31]}"(sel), "{v[32:47]}"(cb), "{v[48:51]}"(cb4), "{v[52:55]}"((v4u){pk(0, true), pk(1, true), pk(2, true), in0}),            \
  "{v[56:57]}"((v2u){in1, in4}), "{s[40:43]}"(mkrsrc(p.out, 2048 * 576 * 2)), "{s[44:47]}"(mkrsrc(p.x, 2048 * 576 * 2)), COMMON,      \
  "{s[64:67]}"(uni((v4u){c.slab1(0) + TR(stg ? L_PF : L_PO), stg ? c.xin() ^ 8192u : c.slab1(0) + TR(L_X1), c.slab1(0) + TR(L_XN2), c.L ? c.xin() ^ 8192u : 0u}))
  if (!stg) {
    asm volatile(
  "s_getpc_b64 s[0:1]\n"
  "L_c0_%=:\n"
  "v_mbcnt_lo_u32_b32 v5, -1, 0\n"
  "v_mbcnt_hi_u32_b32 v5, -1, v5\n"
  "v_lshlrev_b32 v5, 4, v5\n"
  "v_add_lshl_u32 v5, v5, s57, 2\n"
  "v_min_u32 v5, L_c1_%=-L_c0_%=, v5\n"
  "global_load_dword v5, v5, s[0:1]\n"
  "s_nop 4\n"
  "s_lshr_b32 s75, s57, 10\n"
  "s_mul_i32 s75, s75, 9\n"
  "s_add_u32 s74, s75, 0\n"
  "s_lshr_b32 s76, s74, 3\n"
  "s_lshl_b32 s76, s76, 14\n"
  "s_and_b32 s74, s74, 7\n"
  "s_lshl_b32 s74, s74, 10\n"
  "s_or_b32 s4, s74, s76\n"
  "s_add_u32 s74, s75, 1\n"
  "s_lshr_b32 s76, s74, 3\n"
  "s_lshl_b32 s76, s76, 14\n"
  "s_and_b32 s74, s74, 7\n"
  "s_lshl_b32 s74, s74, 10\n"
  "s_or_b32 s5, s74, s76\n"
  "s_add_u32 s74, s75, 2\n"
  "s_lshr_b32 s76, s74, 3\n"
  "s_lshl_b32 s76, s76, 14\n"
  "s_and_b32 s74, s74, 7\n"
  "s_lshl_b32 s74, s74, 10\n"
  "s_or_b32 s6, s74, s76\n"
  "s_add_u32 s74, s75, 3\n"
  "s_lshr_b32 s76, s74, 3\n"
  "s_lshl_b32 s76, s76, 14\n"
  "s_and_b32 s74, s74, 7\n"
  "s_lshl_b32 s74, s74, 10\n"
  "s_or_b32 s7, s74, s76\n"
  "s_add_u32 s74, s75, 4\n"
  "s_lshr_b32 s76, s74, 3\n"
  "s_lshl_b32 s76, s76, 14\n"
  "s_and_b32 s74, s74, 7\n"
  "s_lshl_b32 s74, s74, 10\n"
  "s_or_b32 s8, s74, s76\n"
  "s_add_u32 s74, s75, 5\n"
  "s_lshr_b32 s76, s74, 3\n"
  "s_lshl_b32 s76, s76, 14\n"
  "s_and_b32 s74, s74, 7\n"
  "s_lshl_b32 s74, s74, 10\n"
  "s_or_b32 s9, s74, s76\n"
  "s_add_u32 s74, s75, 6\n"
  "s_lshr_b32 s76, s74, 3\n"
  "s_lshl_b32 s76, s76, 14\n"
  "s_and_b32 s74, s74, 7\n"
  "s_lshl_b32 s74, s74, 10\n"
  "s_or_b32 s10, s74, s76\n"
  "s_add_u32 s74, s75, 7\n"
  "s_lshr_b32 s76, s74, 3\n"
  "s_lshl_b32 s76, s76, 14\n"
  "s_and_b32 s74, s74, 7\n"
  "s_lshl_b32 s74, s74, 10\n"
  "s_or_b32 s11, s74, s76\n"
  "s_add_u32 s74, s75, 8\n"
  "s_lshr_b32 s76, s74, 3\n"
  "s_lshl_b32 s76, s76, 14\n"
  "s_and_b32 s74, s74, 7\n"
  "s_lshl_b32 s74, s74, 10\n"
  "s_or_b32 s12, s74, s76\n"
  "s_mov_b32 s70, 0\n"
  "s_mov_b32 s72, 0\n"
  "s_mov_b32 s68, -1\n"
  "s_mov_b32 s69, 0\n"
  "s_cmp_eq_u32 s57, 0\n"
  "s_cselect_b32 s58, s58, 0x80000000\n"
  "s_cmp_eq_u32 s67, 0\n"
  "s_cbranch_scc1 L_main_%=\n"
  "s_mov_b32 s70, 0\n"
  "s_mov_b32 s78, 0\n"
  "s_cmp_eq_u32 s57, 0\n"
  "s_cselect_b32 s62, s62, 0x80000000\n"
  "buffer_load_dwordx4 v[192:195], v52, s[44:47], s78 offen\n"
  "buffer_load_dwordx4 v[196:199], v53, s[44:47], s78 offen\n"
  "buffer_load_dwordx4 v[200:203], v54, s[44:47], s78 offen\n"
  "s_waitcnt vmcnt(0)\n"
  "L_r0b_%=:\n"
  "s_lshl_b32 s80, s70, 18\n"
  "s_lshr_b32 s76, s80, 8\n"
  "s_and_b32 s76, s76, 8192\n"
  "s_add_u32 s80, s80, s67\n"
  "s_xor_b32 s80, s80, s76\n"
  "s_sub_u32 s79, s80, 81920\n"
  "s_add_u32 s78, s78, 73728\n"
  "v_mfma_f32_16x16x32_bf16 v[112:115], v[24:27], v[192:195], 0\n"
  "v_mfma_f32_16x16x32_bf16 v[116:119], v[28:31], v[192:195], 0\n"
  "v_mfma_f32_16x16x32_bf16 v[120:123], v[24:27], v[196:199], 0\n"
  "v_mfma_f32_16x16x32_bf16 v[124:127], v[28:31], v[196:199], 0\n"
  "v_mfma_f32_16x16x32_bf16 v[128:131], v[24:27], v[200:203], 0\n"
  "buffer_load_dwordx4 v[192:195], v52, s[44:47], s78 offen\n"
  "buffer_load_dwordx4 v[196:199], v53, s[44:47], s78 offen\n"
  "buffer_load_dwordx4 v[200:203], v54, s[44:47], s78 offen\n"
  "s_nop 1\n"
  "v_pk_add_f32 v[160:161], v[112:113], v[114:115]\n"
  "v_pk_add_f32 v[162:163], v[116:117], v[118:119]\n"
  "v_pk_add_f32 v[164:165], v[120:121], v[122:123]\n"
  "v_pk_add_f32 v[166:167], v[124:125], v[126:127]\n"
  "v_pk_add_f32 v[160:161], v[160:161], v[162:163]\n"
  "v_pk_add_f32 v[164:165], v[164:165], v[166:167]\n"
  "v_pk_add_f32 v[168:169], v[128:129], v[130:131]\n"
  "v_pk_add_f32 v[160:161], v[160:161], v[164:165]\n"
  "v_pk_add_f32 v[160:161], v[160:161], v[168:169]\n"
  "v_add_f32 v160, v160, v161\n"
  "s_nop 1\n"
  "v_add_f32_dpp v160, v160, v160 row_ror:8 row_mask:0xf bank_mask:0xf\n"
  "v_mov_b32 v161, v160\n"
  "s_nop 1\n"
  "v_permlane16_swap_b32 v160, v161\n"
  "v_add_f32 v160, v160, v161\n"
  "v_mov_b32 v161, v160\n"
  "s_nop 1\n"
  "v_permlane32_swap_b32 v160, v161\n"
  "v_add_f32 v160, v160, v161\n"
  "v_mul_f32 v160, 0x3be38e39, v160\n"
  "v_pk_add_f32 v[168:169], v[112:113], v[160:161] op_sel_hi:[1,0] neg_lo:[0,1] neg_hi:[0,1]\n"
  "v_pk_add_f32 v[170:171], v[114:115], v[160:161] op_sel_hi:[1,0] neg_lo:[0,1] neg_hi:[0,1]\n"
  "v_pk_add_f32 v[172:173], v[116:117], v[160:161] op_sel_hi:[1,0] neg_lo:[0,1] neg_hi:[0,1]\n"
  "v_pk_add_f32 v[174:175], v[118:119], v[160:161] op_sel_hi:[1,0] neg_lo:[0,1] neg_hi:[0,1]\n"
  "v_pk_add_f32 v[176:177], v[120:121], v[160:161] op_sel_hi:[1,0] neg_lo:[0,1] neg_hi:[0,1]\n"
  "v_pk_add_f32 v[178:179], v[122:123], v[160:161] op_sel_hi:[1,0] neg_lo:[0,1] neg_hi:[0,1]\n"
  "v_pk_add_f32 v[180:181], v[124:125], v[160:161] op_sel_hi:[1,0] neg_lo:[0,1] neg_hi:[0,1]\n"
  "v_pk_add_f32 v[182:183], v[126:127], v[160:161] op_sel_hi:[1,0] neg_lo:[0,1] neg_hi:[0,1]\n"
  "v_pk_add_f32 v[184:185], v[128:129], v[160:161] op_sel_hi:[1,0] neg_lo:[0,1] neg_hi:[0,1]\n"
  "v_pk_add_f32 v[186:187], v[130:131], v[160:161] op_sel_hi:[1,0] neg_lo:[0,1] neg_hi:[0,1]\n"
  "v_pk_mul_f32 v[162:163], v[168:169], v[168:169]\n"
  "v_pk_mul_f32 v[164:165], v[184:185], v[184:185]\n"
  "v_pk_fma_f32 v[162:163], v[170:171], v[170:171], v[162:163]\n"
  "v_pk_fma_f32 v[162:163], v[172:173], v[172:173], v[162:163]\n"
  "v_pk_fma_f32 v[162:163], v[174:175], v[174:175], v[162:163]\n"
  "v_pk_fma_f32 v[162:163], v[176:177], v[176:177], v[162:163]\n"
  "v_pk_fma_f32 v[162:163], v[178:179], v[178:179], v[162:163]\n"
  "v_pk_fma_f32 v[162:163], v[180:181], v[180:181], v[162:163]\n"
  "v_pk_fma_f32 v[162:163], v[182:183], v[182:183], v[162:163]\n"
  "v_pk_fma_f32 v[164:165], v[186:187], v[186:187], v[164:165]\n"
  "v_add_f32 v164, v164, v165\n"
  "v_add_f32 v162, v162, v163\n"
  "v_cndmask_b32 v164, 0, v164, s[68:69]\n"
  "v_add_f32 v161, v162, v164\n"
  "s_nop 1\n"
  "v_add_f32_dpp v161, v161, v161 row_ror:8 row_mask:0xf bank_mask:0xf\n"
  "v_mov_b32 v162, v161\n"
  "s_nop 1\n"
  "v_permlane16_swap_b32 v161, v162\n"
  "v_add_f32 v161, v161, v162\n"
  "v_mov_b32 v162, v161\n"
  "s_nop 1\n"
  "v_permlane32_swap_b32 v161, v162\n"
  "v_add_f32 v161, v161, v162\n"
  "ds_write_b64 v7, v[160:161]\n"
  "v_cvt_pk_bf16_f32 v136, v112, v113\n"
  "v_cvt_pk_bf16_f32 v137, v114, v115\n"
  "v_cvt_pk_bf16_f32 v138, v116, v117\n"
  "v_cvt_pk_bf16_f32 v139, v118, v119\n"
  "v_cvt_pk_bf16_f32 v140, v120, v121\n"
  "v_cvt_pk_bf16_f32 v141, v122, v123\n"
  "v_cvt_pk_bf16_f32 v142, v124, v125\n"
  "v_cvt_pk_bf16_f32 v143, v126, v127\n"
  "v_permlane16_swap_b32 v136, v138\n"
  "v_permlane16_swap_b32 v137, v139\n"
  "v_permlane16_swap_b32 v140, v142\n"
  "v_permlane16_swap_b32 v141, v143\n"
  "s_waitcnt lgkmcnt(0)\n"
  "s_barrier\n"
  "ds_read_b128 v[160:163], v1\n"
  "ds_read_b128 v[164:167], v1 offset:16\n"
  "buffer_store_dwordx4 v[136:139], v55, s[36:39], s80 offen sc1\n"
  "v_cvt_pk_bf16_f32 v144, v128, v129\n"
  "v_cvt_pk_bf16_f32 v145, v130, v131\n"
  "v_xor_b32 v7, 256, v7\n"
  "v_xor_b32 v1, 256, v1\n"
  "buffer_store_dwordx4 v[140:143], v56, s[36:39], s80 offen sc1\n"
  "s_waitcnt lgkmcnt(0)\n"
  "v_pk_add_f32 v[168:169], v[160:161], v[162:163]\n"
  "v_pk_add_f32 v[170:171], v[164:165], v[166:167]\n"
  "v_pk_add_f32 v[168:169], v[168:169], v[170:171]\n"
  "v_mul_f32 v168, 0x3e800000, v168\n"
  "v_sub_f32 v160, v160, v168\n"
  "v_sub_f32 v162, v162, v168\n"
  "v_sub_f32 v164, v164, v168\n"
  "v_sub_f32 v166, v166, v168\n"
  "v_mul_f32 v170, v160, v160\n"
  "v_fmac_f32 v170, v162, v162\n"
  "v_fmac_f32 v170, v164, v164\n"
  "v_fmac_f32 v170, v166, v166\n"
  "v_fmamk_f32 v169, v170, 0x43100000, v169\n"
  "v_mov_b32 v171, 0x3727c5ac\n"
  "v_fmamk_f32 v170, v169, 0x3ae38e39, v171\n"
  "v_rsq_f32 v170, v170\n"
  "s_nop 0\n"
  "v_mul_f32 v172, -v168, v170\n"
  "v_pk_fma_f32 v[112:113], v[112:113], v[170:171], v[172:173] op_sel_hi:[1,0,0]\n"
  "v_pk_fma_f32 v[114:115], v[114:115], v[170:171], v[172:173] op_sel_hi:[1,0,0]\n"
  "v_pk_fma_f32 v[116:117], v[116:117], v[170:171], v[172:173] op_sel_hi:[1,0,0]\n"
  "v_pk_fma_f32 v[118:119], v[118:119], v[170:171], v[172:173] op_sel_hi:[1,0,0]\n"
  "v_pk_fma_f32 v[120:121], v[120:121], v[170:171], v[172:173] op_sel_hi:[1,0,0]\n"
  "v_pk_fma_f32 v[122:123], v[122:123], v[170:171], v[172:173] op_sel_hi:[1,0,0]\n"
  "v_pk_fma_f32 v[124:125], v[124:125], v[170:171], v[172:173] op_sel_hi:[1,0,0]\n"
  "v_pk_fma_f32 v[126:127], v[126:127], v[170:171], v[172:173] op_sel_hi:[1,0,0]\n"
  "v_pk_fma_f32 v[128:129], v[128:129], v[170:171], v[172:173] op_sel_hi:[1,0,0]\n"
  "v_pk_fma_f32 v[130:131], v[130:131], v[170:171], v[172:173] op_sel_hi:[1,0,0]\n"
  "v_cvt_pk_bf16_f32 v148, v112, v113\n"
  "v_cvt_pk_bf16_f32 v149, v114, v115\n"
  "v_cvt_pk_bf16_f32 v150, v116, v117\n"
  "v_cvt_pk_bf16_f32 v151, v118, v119\n"
  "v_cvt_pk_bf16_f32 v152, v120, v121\n"
  "v_cvt_pk_bf16_f32 v153, v122, v123\n"
  "v_cvt_pk_bf16_f32 v154, v124, v125\n"
  "v_cvt_pk_bf16_f32 v155, v126, v127\n"
  "v_permlane16_swap_b32 v148, v150\n"
  "v_permlane16_swap_b32 v149, v151\n"
  "v_permlane16_swap_b32 v152, v154\n"
  "v_permlane16_swap_b32 v153, v155\n"
  "buffer_store_dwordx4 v[148:151], v55, s[36:39], s79 offen sc1\n"
  "v_cvt_pk_bf16_f32 v146, v128, v129\n"
  "v_cvt_pk_bf16_f32 v147, v130, v131\n"
  "buffer_store_dwordx4 v[152:155], v56, s[36:39], s79 offen sc1\n"
  "s_nop 0\n"
  "v_permlane16_swap_b32 v144, v146\n"
  "v_permlane16_swap_b32 v145, v147\n"
  "buffer_store_dwordx4 v[144:147], v57, s[36:39], s80 offen sc1\n"
  "s_add_u32 s70, s70, 1\n"
  "v_mov_b32 v160, s70\n"
  "s_add_u32 s70, s70, 1\n"
  "s_waitcnt vmcnt(0)\n"
  "s_barrier\n"
  "s_mov_b64 exec, 1\n"
  "buffer_store_dword v160, off, s[36:39], s62 sc1\n"
  "s_mov_b64 exec, -1\n"
  "s_cmp_lt_u32 s70, 64\n"
  "s_cbranch_scc1 L_r0b_%=\n"
  "s_mov_b32 s70, 0\n"
  "L_main_%=:\n"
  "s_add_u32 s78, s70, 0\n"
  "s_and_b32 s78, s78, s61\n"
  "s_mul_i32 s78, s78, s60\n"
  "s_lshr_b32 s76, s78, 8\n"
  "s_and_b32 s76, s76, 8192\n"
  "s_add_u32 s78, s78, s59\n"
  "s_xor_b32 s78, s78, s76\n"
  "s_add_u32 s84, s70, 0\n"
  "s_and_b32 s84, s84, 15\n"
  "s_lshl_b32 s84, s84, 22\n"
  "s_add_u32 s84, s84, s64\n"
  "s_add_u32 s80, s70, 0\n"
  "s_and_b32 s80, s80, 15\n"
  "s_lshl_b32 s80, s80, 22\n"
  "s_add_u32 s80, s80, s65\n"
  "s_add_u32 s79, s70, 0\n"
  "s_and_b32 s79, s79, 15\n"
  "s_lshl_b32 s79, s79, 22\n"
  "s_add_u32 s79, s79, s66\n"
  "s_add_u32 s77, s84, 327680\n"
  "L_wait_%=:\n"
  "s_cmp_lg_u32 s72, 0\n"
  "s_cselect_b32 s73, 0, 32768\n"
  "v_add_u32 v6, s70, v9\n"
  "L_rpoll_%=:\n"
  "buffer_load_dword v4, v2, s[36:39], 0 offen sc1\n"
  "s_sub_u32 s73, s73, 1\n"
  "s_cselect_b32 s72, 1, s72\n"
  "s_cselect_b64 s[2:3], 0, -1\n"
  "s_waitcnt vmcnt(0)\n"
  "v_cmp_gt_i32 vcc, v6, v4\n"
  "s_and_b64 vcc, vcc, s[2:3]\n"
  "s_cbranch_scc1 L_rpoll_%=\n"
  "L_rpoll_x_%=:\n"
  "buffer_load_dwordx4 v[192:195], v10, s[36:39], s78 offen sc1\n"
  "buffer_load_dwordx4 v[196:199], v11, s[36:39], s78 offen sc1\n"
  "buffer_load_dwordx4 v[200:203], v12, s[36:39], s78 offen sc1\n"
  "s_cmp_lg_u32 s72, 0\n"
  "s_cselect_b32 s73, 0, 32768\n"
  "v_add_u32 v6, s70, v3\n"
  "L_poll_%=:\n"
  "buffer_load_dword v4, v2, s[36:39], 0 offen sc1\n"
  "s_sub_u32 s73, s73, 1\n"
  "s_cselect_b32 s72, 1, s72\n"
  "s_cselect_b64 s[2:3], 0, -1\n"
  "s_waitcnt vmcnt(0)\n"
  "v_cmp_gt_i32 vcc, v6, v4\n"
  "s_and_b64 vcc, vcc, s[2:3]\n"
  "s_cbranch_scc1 L_poll_%=\n"
  "L_poll_x_%=:\n"
  "s_branch L_job_%=\n"
  "L_both_%=:\n"
  "buffer_load_dwordx4 v[192:195], v10, s[36:39], s78 offen sc1\n"
  "buffer_load_dwordx4 v[196:199], v11, s[36:39], s78 offen sc1\n"
  "buffer_load_dwordx4 v[200:203], v12, s[36:39], s78 offen sc1\n"
  "L_job_%=:\n"
  "s_add_u32 s76, s84, s4\n"
  "buffer_load_dwordx4 v[64:67], v0, s[36:39], s76 offen sc1\n"
  "s_add_u32 s76, s84, s5\n"
  "buffer_load_dwordx4 v[68:71], v0, s[36:39], s76 offen sc1\n"
  "s_add_u32 s76, s84, s6\n"
  "buffer_load_dwordx4 v[72:75], v0, s[36:39], s76 offen sc1\n"
  "s_add_u32 s76, s84, s7\n"
  "buffer_load_dwordx4 v[76:79], v0, s[36:39], s76 offen sc1\n"
  "s_add_u32 s76, s84, s8\n"
  "buffer_load_dwordx4 v[80:83], v0, s[36:39], s76 offen sc1\n"
  "s_add_u32 s76, s84, s9\n"
  "buffer_load_dwordx4 v[84:87], v0, s[36:39], s76 offen sc1\n"
  "s_add_u32 s76, s84, s10\n"
  "buffer_load_dwordx4 v[88:91], v0, s[36:39], s76 offen sc1\n"
  "s_add_u32 s76, s84, s11\n"
  "buffer_load_dwordx4 v[92:95], v0, s[36:39], s76 offen sc1\n"
  "s_add_u32 s76, s84, s12\n"
  "buffer_load_dwordx4 v[96:99], v0, s[36:39], s76 offen sc1\n"
  "s_waitcnt vmcnt(9)\n"
  "v_mfma_f32_16x16x32_bf16 v[112:115], v[24:27], v[192:195], v[32:35]\n"
  "v_mfma_f32_16x16x32_bf16 v[116:119], v[28:31], v[192:195], v[36:39]\n"
  "v_mfma_f32_16x16x32_bf16 v[120:123], v[24:27], v[196:199], v[40:43]\n"
  "v_mfma_f32_16x16x32_bf16 v[124:127], v[28:31], v[196:199], v[44:47]\n"
  "v_mfma_f32_16x16x32_bf16 v[128:131], v[24:27], v[200:203], v[48:51]\n"
  "s_waitcnt vmcnt(8)\n"
  "v_mfma_f32_16x16x32_bf16 v[112:115], v[16:19], v[64:67], v[112:115]\n"
  "s_waitcnt vmcnt(7)\n"
  "v_mfma_f32_16x16x32_bf16 v[112:115], v[20:23], v[68:71], v[112:115]\n"
  "s_waitcnt vmcnt(6)\n"
  "v_mfma_f32_16x16x32_bf16 v[116:119], v[16:19], v[72:75], v[116:119]\n"
  "s_waitcnt vmcnt(5)\n"
  "v_mfma_f32_16x16x32_bf16 v[116:119], v[20:23], v[76:79], v[116:119]\n"
  "s_waitcnt vmcnt(4)\n"
  "v_mfma_f32_16x16x32_bf16 v[120:123], v[16:19], v[80:83], v[120:123]\n"
  "s_waitcnt vmcnt(3)\n"
  "v_mfma_f32_16x16x32_bf16 v[120:123], v[20:23], v[84:87], v[120:123]\n"
  "s_waitcnt vmcnt(2)\n"
  "v_mfma_f32_16x16x32_bf16 v[124:127], v[16:19], v[88:91], v[124:127]\n"
  "s_waitcnt vmcnt(1)\n"
  "v_mfma_f32_16x16x32_bf16 v[124:127], v[20:23], v[92:95], v[124:127]\n"
  "s_waitcnt vmcnt(0)\n"
  "v_mfma_f32_16x16x32_bf16 v[128:131], v[16:19], v[96:99], v[128:131]\n"
  "s_nop 1\n"
  "v_pk_add_f32 v[160:161], v[112:113], v[114:115]\n"
  "v_pk_add_f32 v[162:163], v[116:117], v[118:119]\n"
  "v_pk_add_f32 v[164:165], v[120:121], v[122:123]\n"
  "v_pk_add_f32 v[166:167], v[124:125], v[126:127]\n"
  "v_pk_add_f32 v[160:161], v[160:161], v[162:163]\n"
  "v_pk_add_f32 v[164:165], v[164:165], v[166:167]\n"
  "v_pk_add_f32 v[168:169], v[128:129], v[130:131]\n"
  "v_pk_add_f32 v[160:161], v[160:161], v[164:165]\n"
  "v_pk_add_f32 v[160:161], v[160:161], v[168:169]\n"
  "v_add_f32 v160, v160, v161\n"
  "s_nop 1\n"
  "v_add_f32_dpp v160, v160, v160 row_ror:8 row_mask:0xf bank_mask:0xf\n"
  "v_mov_b32 v161, v160\n"
  "s_nop 1\n"
  "v_permlane16_swap_b32 v160, v161\n"
  "v_add_f32 v160, v160, v161\n"
  "v_mov_b32 v161, v160\n"
  "s_nop 1\n"
  "v_permlane32_swap_b32 v160, v161\n"
  "v_add_f32 v160, v160, v161\n"
  "v_mul_f32 v160, 0x3be38e39, v160\n"
  "v_pk_add_f32 v[168:169], v[112:113], v[160:161] op_sel_hi:[1,0] neg_lo:[0,1] neg_hi:[0,1]\n"
  "v_pk_add_f32 v[170:171], v[114:115], v[160:161] op_sel_hi:[1,0] neg_lo:[0,1] neg_hi:[0,1]\n"
  "v_pk_add_f32 v[172:173], v[116:117], v[160:161] op_sel_hi:[1,0] neg_lo:[0,1] neg_hi:[0,1]\n"
  "v_pk_add_f32 v[174:175], v[118:119], v[160:161] op_sel_hi:[1,0] neg_lo:[0,1] neg_hi:[0,1]\n"
  "v_pk_add_f32 v[176:177], v[120:121], v[160:161] op_sel_hi:[1,0] neg_lo:[0,1] neg_hi:[0,1]\n"
  "v_pk_add_f32 v[178:179], v[122:123], v[160:161] op_sel_hi:[1,0] neg_lo:[0,1] neg_hi:[0,1]\n"
  "v_pk_add_f32 v[180:181], v[124:125], v[160:161] op_sel_hi:[1,0] neg_lo:[0,1] neg_hi:[0,1]\n"
  "v_pk_add_f32 v[182:183], v[126:127], v[160:161] op_sel_hi:[1,0] neg_lo:[0,1] neg_hi:[0,1]\n"
  "v_pk_add_f32 v[184:185], v[128:129], v[160:161] op_sel_hi:[1,0] neg_lo:[0,1] neg_hi:[0,1]\n"
  "v_pk_add_f32 v[186:187], v[130:131], v[160:161] op_sel_hi:[1,0] neg_lo:[0,1] neg_hi:[0,1]\n"
  "v_pk_mul_f32 v[162:163], v[168:169], v[168:169]\n"
  "v_pk_mul_f32 v[164:165], v[184:185], v[184:185]\n"
  "v_pk_fma_f32 v[162:163], v[170:171], v[170:171], v[162:163]\n"
  "v_pk_fma_f32 v[162:163], v[172:173], v[172:173], v[162:163]\n"
  "v_pk_fma_f32 v[162:163], v[174:175], v[174:175], v[162:163]\n"
  "v_pk_fma_f32 v[162:163], v[176:177], v[176:177], v[162:163]\n"
  "v_pk_fma_f32 v[162:163], v[178:179], v[178:179], v[162:163]\n"
  "v_pk_fma_f32 v[162:163], v[180:181], v[180:181], v[162:163]\n"
  "v_pk_fma_f32 v[162:163], v[182:183], v[182:183], v[162:163]\n"
  "v_pk_fma_f32 v[164:165], v[186:187], v[186:187], v[164:165]\n"
  "v_add_f32 v164, v164, v165\n"
  "v_add_f32 v162, v162, v163\n"
  "v_cndmask_b32 v164, 0, v164, s[68:69]\n"
  "v_add_f32 v161, v162, v164\n"
  "s_nop 1\n"
  "v_add_f32_dpp v161, v161, v161 row_ror:8 row_mask:0xf bank_mask:0xf\n"
  "v_mov_b32 v162, v161\n"
  "s_nop 1\n"
  "v_permlane16_swap_b32 v161, v162\n"
  "v_add_f32 v161, v161, v162\n"
  "v_mov_b32 v162, v161\n"
  "s_nop 1\n"
  "v_permlane32_swap_b32 v161, v162\n"
  "v_add_f32 v161, v161, v162\n"
  "ds_write_b64 v7, v[160:161]\n"
  "v_cvt_pk_bf16_f32 v136, v112, v113\n"
  "v_cvt_pk_bf16_f32 v137, v114, v115\n"
  "v_cvt_pk_bf16_f32 v138, v116, v117\n"
  "v_cvt_pk_bf16_f32 v139, v118, v119\n"
  "v_cvt_pk_bf16_f32 v140, v120, v121\n"
  "v_cvt_pk_bf16_f32 v141, v122, v123\n"
  "v_cvt_pk_bf16_f32 v142, v124, v125\n"
  "v_cvt_pk_bf16_f32 v143, v126, v127\n"
  "v_permlane16_swap_b32 v136, v138\n"
  "v_permlane16_swap_b32 v137, v139\n"
  "v_permlane16_swap_b32 v140, v142\n"
  "v_permlane16_swap_b32 v141, v143\n"
  "s_waitcnt lgkmcnt(0)\n"
  "s_barrier\n"
  "ds_read_b128 v[160:163], v1\n"
  "ds_read_b128 v[164:167], v1 offset:16\n"
  "buffer_store_dwordx4 v[136:139], v13, s[36:39], s80 offen sc1\n"
  "v_cvt_pk_bf16_f32 v144, v128, v129\n"
  "v_cvt_pk_bf16_f32 v145, v130, v131\n"
  "v_xor_b32 v7, 256, v7\n"
  "v_xor_b32 v1, 256, v1\n"
  "buffer_store_dwordx4 v[140:143], v14, s[36:39], s80 offen sc1\n"
  "s_waitcnt lgkmcnt(0)\n"
  "v_pk_add_f32 v[168:169], v[160:161], v[162:163]\n"
  "v_pk_add_f32 v[170:171], v[164:165], v[166:167]\n"
  "v_pk_add_f32 v[168:169], v[168:169], v[170:171]\n"
  "v_mul_f32 v168, 0x3e800000, v168\n"
  "v_sub_f32 v160, v160, v168\n"
  "v_sub_f32 v162, v162, v168\n"
  "v_sub_f32 v164, v164, v168\n"
  "v_sub_f32 v166, v166, v168\n"
  "v_mul_f32 v170, v160, v160\n"
  "v_fmac_f32 v170, v162, v162\n"
  "v_fmac_f32 v170, v164, v164\n"
  "v_fmac_f32 v170, v166, v166\n"
  "v_fmamk_f32 v169, v170, 0x43100000, v169\n"
  "v_mov_b32 v171, 0x3727c5ac\n"
  "v_fmamk_f32 v170, v169, 0x3ae38e39, v171\n"
  "v_rsq_f32 v170, v170\n"
  "s_nop 0\n"
  "v_mul_f32 v172, -v168, v170\n"
  "v_pk_fma_f32 v[112:113], v[112:113], v[170:171], v[172:173] op_sel_hi:[1,0,0]\n"
  "v_pk_fma_f32 v[114:115], v[114:115], v[170:171], v[172:173] op_sel_hi:[1,0,0]\n"
  "v_pk_fma_f32 v[116:117], v[116:117], v[170:171], v[172:173] op_sel_hi:[1,0,0]\n"
  "v_pk_fma_f32 v[118:119], v[118:119], v[170:171], v[172:173] op_sel_hi:[1,0,0]\n"
  "v_pk_fma_f32 v[120:121], v[120:121], v[170:171], v[172:173] op_sel_hi:[1,0,0]\n"
  "v_pk_fma_f32 v[122:123], v[122:123], v[170:171], v[172:173] op_sel_hi:[1,0,0]\n"
  "v_pk_fma_f32 v[124:125], v[124:125], v[170:171], v[172:173] op_sel_hi:[1,0,0]\n"
  "v_pk_fma_f32 v[126:127], v[126:127], v[170:171], v[172:173] op_sel_hi:[1,0,0]\n"
  "v_pk_fma_f32 v[128:129], v[128:129], v[170:171], v[172:173] op_sel_hi:[1,0,0]\n"
  "v_pk_fma_f32 v[130:131], v[130:131], v[170:171], v[172:173] op_sel_hi:[1,0,0]\n"
  "v_cvt_pk_bf16_f32 v148, v112, v113\n"
  "v_cvt_pk_bf16_f32 v149, v114, v115\n"
  "v_cvt_pk_bf16_f32 v150, v116, v117\n"
  "v_cvt_pk_bf16_f32 v151, v118, v119\n"
  "v_cvt_pk_bf16_f32 v152, v120, v121\n"
  "v_cvt_pk_bf16_f32 v153, v122, v123\n"
  "v_cvt_pk_bf16_f32 v154, v124, v125\n"
  "v_cvt_pk_bf16_f32 v155, v126, v127\n"
  "v_permlane16_swap_b32 v148, v150\n"
  "v_permlane16_swap_b32 v149, v151\n"
  "v_permlane16_swap_b32 v152, v154\n"
  "v_permlane16_swap_b32 v153, v155\n"
  "buffer_store_dwordx4 v[148:151], v13, s[36:39], s79 offen sc1\n"
  "v_cvt_pk_bf16_f32 v146, v128, v129\n"
  "v_cvt_pk_bf16_f32 v147, v130, v131\n"
  "buffer_store_dwordx4 v[152:155], v14, s[36:39], s79 offen sc1\n"
  "s_nop 0\n"
  "v_permlane16_swap_b32 v144, v146\n"
  "v_permlane16_swap_b32 v145, v147\n"
  "buffer_store_dwordx4 v[144:147], v15, s[36:39], s80 offen sc1\n"
  "buffer_load_dword v4, v2, s[36:39], 0 offen sc1\n"
  "v_mov_b32 v160, s70\n"
  "s_add_u32 s70, s70, 1\n"
  "s_add_u32 s78, s70, 0\n"
  "s_and_b32 s78, s78, s61\n"
  "s_mul_i32 s78, s78, s60\n"
  "s_lshr_b32 s76, s78, 8\n"
  "s_and_b32 s76, s76, 8192\n"
  "s_add_u32 s78, s78, s59\n"
  "s_xor_b32 s78, s78, s76\n"
  "s_add_u32 s84, s70, 0\n"
  "s_and_b32 s84, s84, 15\n"
  "s_lshl_b32 s84, s84, 22\n"
  "s_add_u32 s84, s84, s64\n"
  "s_add_u32 s80, s70, 0\n"
  "s_and_b32 s80, s80, 15\n"
  "s_lshl_b32 s80, s80, 22\n"
  "s_add_u32 s80, s80, s65\n"
  "s_add_u32 s79, s70, 0\n"
  "s_and_b32 s79, s79, 15\n"
  "s_lshl_b32 s79, s79, 22\n"
  "s_add_u32 s79, s79, s66\n"
  "s_add_u32 s77, s84, 327680\n"
  "s_waitcnt vmcnt(1)\n"
  "s_barrier\n"
  "s_mov_b64 exec, 1\n"
  "buffer_store_dword v160, off, s[36:39], s58 sc1\n"
  "s_mov_b64 exec, -1\n"
  "s_waitcnt vmcnt(1)\n"
  "v_subrev_u32 v6, v3, v4\n"
  "s_add_u32 s74, s70, 0\n"
  "v_cmp_le_i32 vcc, s74, v6\n"
  "s_cmp_eq_u64 vcc, exec\n"
  "s_cbranch_scc1 L_both_%=\n"
  "s_cmp_lt_u32 s70, 64\n"
  "s_cbranch_scc1 L_wait_%=\n"
  "s_waitcnt vmcnt(0)\n"
  "L_c1_%=:\n"
  "s_endpgm\n"
      : : R_OPERANDS : OWNED);
  } else if (!last) {
    asm volatile(
  "s_getpc_b64 s[0:1]\n"
  "L_c0_%=:\n"
  "v_mbcnt_lo_u32_b32 v5, -1, 0\n"
  "v_mbcnt_hi_u32_b32 v5, -1, v5\n"
  "v_lshlrev_b32 v5, 4, v5\n"
  "v_add_lshl_u32 v5, v5, s57, 2\n"
  "v_min_u32 v5, L_c1_%=-L_c0_%=, v5\n"
  "global_load_dword v5, v5, s[0:1]\n"
  "s_nop 4\n"
  "s_lshr_b32 s75, s57, 10\n"
  "s_mul_i32 s75, s75, 9\n"
  "s_add_u32 s74, s75, 0\n"
  "s_lshr_b32 s76, s74, 3\n"
  "s_lshl_b32 s76, s76, 14\n"
  "s_and_b32 s74, s74, 7\n"
  "s_lshl_b32 s74, s74, 10\n"
  "s_or_b32 s4, s74, s76\n"
  "s_add_u32 s74, s75, 1\n"
  "s_lshr_b32 s76, s74, 3\n"
  "s_lshl_b32 s76, s76, 14\n"
  "s_and_b32 s74, s74, 7\n"
  "s_lshl_b32 s74, s74, 10\n"
  "s_or_b32 s5, s74, s76\n"
  "s_add_u32 s74, s75, 2\n"
  "s_lshr_b32 s76, s74, 3\n"
  "s_lshl_b32 s76, s76, 14\n"
  "s_and_b32 s74, s74, 7\n"
  "s_lshl_b32 s74, s74, 10\n"
  "s_or_b32 s6, s74, s76\n"
  "s_add_u32 s74, s75, 3\n"
  "s_lshr_b32 s76, s74, 3\n"
  "s_lshl_b32 s76, s76, 14\n"
  "s_and_b32 s74, s74, 7\n"
  "s_lshl_b32 s74, s74, 10\n"
  "s_or_b32 s7, s74, s76\n"
  "s_add_u32 s74, s75, 4\n"
  "s_lshr_b32 s76, s74, 3\n"
  "s_lshl_b32 s76, s76, 14\n"
  "s_and_b32 s74, s74, 7\n"
  "s_lshl_b32 s74, s74, 10\n"
  "s_or_b32 s8, s74, s76\n"
  "s_add_u32 s74, s75, 5\n"
  "s_lshr_b32 s76, s74, 3\n"
  "s_lshl_b32 s76, s76, 14\n"
  "s_and_b32 s74, s74, 7\n"
  "s_lshl_b32 s74, s74, 10\n"
  "s_or_b32 s9, s74, s76\n"
  "s_add_u32 s74, s75, 6\n"
  "s_lshr_b32 s76, s74, 3\n"
  "s_lshl_b32 s76, s76, 14\n"
  "s_and_b32 s74, s74, 7\n"
  "s_lshl_b32 s74, s74, 10\n"
  "s_or_b32 s10, s74, s76\n"
  "s_add_u32 s74, s75, 7\n"
  "s_lshr_b32 s76, s74, 3\n"
  "s_lshl_b32 s76, s76, 14\n"
  "s_and_b32 s74, s74, 7\n"
  "s_lshl_b32 s74, s74, 10\n"
  "s_or_b32 s11, s74, s76\n"
  "s_add_u32 s74, s75, 8\n"
  "s_lshr_b32 s76, s74, 3\n"
  "s_lshl_b32 s76, s76, 14\n"
  "s_and_b32 s74, s74, 7\n"
  "s_lshl_b32 s74, s74, 10\n"
  "s_or_b32 s12, s74, s76\n"
  "s_mov_b32 s70, 0\n"
  "s_mov_b32 s72, 0\n"
  "s_mov_b32 s68, -1\n"
  "s_mov_b32 s69, 0\n"
  "s_cmp_eq_u32 s57, 0\n"
  "s_cselect_b32 s58, s58, 0x80000000\n"
  "s_add_u32 s78, s70, 0\n"
  "s_and_b32 s78, s78, s61\n"
  "s_mul_i32 s78, s78, s60\n"
  "s_lshr_b32 s76, s78, 8\n"
  "s_and_b32 s76, s76, 8192\n"
  "s_add_u32 s78, s78, s59\n"
  "s_xor_b32 s78, s78, s76\n"
  "s_add_u32 s84, s70, 0\n"
  "s_and_b32 s84, s84, 15\n"
  "s_lshl_b32 s84, s84, 22\n"
  "s_add_u32 s84, s84, s64\n"
  "s_lshl_b32 s80, s70, 18\n"
  "s_lshr_b32 s76, s80, 8\n"
  "s_and_b32 s76, s76, 8192\n"
  "s_add_u32 s80, s80, s65\n"
  "s_xor_b32 s80, s80, s76\n"
  "s_sub_u32 s79, s80, 81920\n"
  "s_add_u32 s77, s84, 327680\n"
  "L_wait_%=:\n"
  "s_cmp_lg_u32 s72, 0\n"
  "s_cselect_b32 s73, 0, 32768\n"
  "v_add_u32 v6, s70, v9\n"
  "L_rpoll_%=:\n"
  "buffer_load_dword v4, v2, s[36:39], 0 offen sc1\n"
  "s_sub_u32 s73, s73, 1\n"
  "s_cselect_b32 s72, 1, s72\n"
  "s_cselect_b64 s[2:3], 0, -1\n"
  "s_waitcnt vmcnt(0)\n"
  "v_cmp_gt_i32 vcc, v6, v4\n"
  "s_and_b64 vcc, vcc, s[2:3]\n"
  "s_cbranch_scc1 L_rpoll_%=\n"
  "L_rpoll_x_%=:\n"
  "buffer_load_dwordx4 v[192:195], v10, s[36:39], s78 offen sc1\n"
  "buffer_load_dwordx4 v[196:199], v11, s[36:39], s78 offen sc1\n"
  "buffer_load_dwordx4 v[200:203], v12, s[36:39], s78 offen sc1\n"
  "s_cmp_lg_u32 s72, 0\n"
  "s_cselect_b32 s73, 0, 32768\n"
  "v_add_u32 v6, s70, v3\n"
  "L_poll_%=:\n"
  "buffer_load_dword v4, v2, s[36:39], 0 offen sc1\n"
  "s_sub_u32 s73, s73, 1\n"
  "s_cselect_b32 s72, 1, s72\n"
  "s_cselect_b64 s[2:3], 0, -1\n"
  "s_waitcnt vmcnt(0)\n"
  "v_cmp_gt_i32 vcc, v6, v4\n"
  "s_and_b64 vcc, vcc, s[2:3]\n"
  "s_cbranch_scc1 L_poll_%=\n"
  "L_poll_x_%=:\n"
  "s_branch L_job_%=\n"
  "L_both_%=:\n"
  "buffer_load_dwordx4 v[192:195], v10, s[36:39], s78 offen sc1\n"
  "buffer_load_dwordx4 v[196:199], v11, s[36:39], s78 offen sc1\n"
  "buffer_load_dwordx4 v[200:203], v12, s[36:39], s78 offen sc1\n"
  "L_job_%=:\n"
  "s_add_u32 s76, s84, s4\n"
  "buffer_load_dwordx4 v[64:67], v0, s[36:39], s76 offen sc1\n"
  "s_add_u32 s76, s84, s5\n"
  "buffer_load_dwordx4 v[68:71], v0, s[36:39], s76 offen sc1\n"
  "s_add_u32 s76, s84, s6\n"
  "buffer_load_dwordx4 v[72:75], v0, s[36:39], s76 offen sc1\n"
  "s_add_u32 s76, s84, s7\n"
  "buffer_load_dwordx4 v[76:79], v0, s[36:39], s76 offen sc1\n"
  "s_add_u32 s76, s84, s8\n"
  "buffer_load_dwordx4 v[80:83], v0, s[36:39], s76 offen sc1\n"
  "s_add_u32 s76, s84, s9\n"
  "buffer_load_dwordx4 v[84:87], v0, s[36:39], s76 offen sc1\n"
  "s_add_u32 s76, s84, s10\n"
  "buffer_load_dwordx4 v[88:91], v0, s[36:39], s76 offen sc1\n"
  "s_add_u32 s76, s84, s11\n"
  "buffer_load_dwordx4 v[92:95], v0, s[36:39], s76 offen sc1\n"
  "s_add_u32 s76, s84, s12\n"
  "buffer_load_dwordx4 v[96:99], v0, s[36:39], s76 offen sc1\n"
  "buffer_load_dwordx4 v[100:103], v10, s[36:39], s77 offen sc1\n"
  "buffer_load_dwordx4 v[104:107], v11, s[36:39], s77 offen sc1\n"
  "buffer_load_dwordx4 v[108:111], v12, s[36:39], s77 offen sc1\n"
  "s_waitcnt vmcnt(12)\n"
  "v_mfma_f32_16x16x32_bf16 v[112:115], v[24:27], v[192:195], v[32:35]\n"
  "v_mfma_f32_16x16x32_bf16 v[116:119], v[28:31], v[192:195], v[36:39]\n"
  "v_mfma_f32_16x16x32_bf16 v[120:123], v[24:27], v[196:199], v[40:43]\n"
  "v_mfma_f32_16x16x32_bf16 v[124:127], v[28:31], v[196:199], v[44:47]\n"
  "v_mfma_f32_16x16x32_bf16 v[128:131], v[24:27], v[200:203], v[48:51]\n"
  "s_waitcnt vmcnt(11)\n"
  "v_mfma_f32_16x16x32_bf16 v[112:115], v[16:19], v[64:67], v[112:115]\n"
  "s_waitcnt vmcnt(10)\n"
  "v_mfma_f32_16x16x32_bf16 v[112:115], v[20:23], v[68:71], v[112:115]\n"
  "s_waitcnt vmcnt(9)\n"
  "v_mfma_f32_16x16x32_bf16 v[116:119], v[16:19], v[72:75], v[116:119]\n"
  "s_waitcnt vmcnt(8)\n"
  "v_mfma_f32_16x16x32_bf16 v[116:119], v[20:23], v[76:79], v[116:119]\n"
  "s_waitcnt vmcnt(7)\n"
  "v_mfma_f32_16x16x32_bf16 v[120:123], v[16:19], v[80:83], v[120:123]\n"
  "s_waitcnt vmcnt(6)\n"
  "v_mfma_f32_16x16x32_bf16 v[120:123], v[20:23], v[84:87], v[120:123]\n"
  "s_waitcnt vmcnt(5)\n"
  "v_mfma_f32_16x16x32_bf16 v[124:127], v[16:19], v[88:91], v[124:127]\n"
  "s_waitcnt vmcnt(4)\n"
  "v_mfma_f32_16x16x32_bf16 v[124:127], v[20:23], v[92:95], v[124:127]\n"
  "s_waitcnt vmcnt(3)\n"
  "v_mfma_f32_16x16x32_bf16 v[128:131], v[16:19], v[96:99], v[128:131]\n"
  "s_waitcnt vmcnt(2)\n"
  "v_mfma_f32_16x16x32_bf16 v[112:115], v[24:27], v[100:103], v[112:115]\n"
  "v_mfma_f32_16x16x32_bf16 v[116:119], v[28:31], v[100:103], v[116:119]\n"
  "s_waitcnt vmcnt(1)\n"
  "v_mfma_f32_16x16x32_bf16 v[120:123], v[24:27], v[104:107], v[120:123]\n"
  "v_mfma_f32_16x16x32_bf16 v[124:127], v[28:31], v[104:107], v[124:127]\n"
  "s_waitcnt vmcnt(0)\n"
  "v_mfma_f32_16x16x32_bf16 v[128:131], v[24:27], v[108:111], v[128:131]\n"
  "s_nop 1\n"
  "v_pk_add_f32 v[160:161], v[112:113], v[114:115]\n"
  "v_pk_add_f32 v[162:163], v[116:117], v[118:119]\n"
  "v_pk_add_f32 v[164:165], v[120:121], v[122:123]\n"
  "v_pk_add_f32 v[166:167], v[124:125], v[126:127]\n"
  "v_pk_add_f32 v[160:161], v[160:161], v[162:163]\n"
  "v_pk_add_f32 v[164:165], v[164:165], v[166:167]\n"
  "v_pk_add_f32 v[168:169], v[128:129], v[130:131]\n"
  "v_pk_add_f32 v[160:161], v[160:161], v[164:165]\n"
  "v_pk_add_f32 v[160:161], v[160:161], v[168:169]\n"
  "v_add_f32 v160, v160, v161\n"
  "s_nop 1\n"
  "v_add_f32_dpp v160, v160, v160 row_ror:8 row_mask:0xf bank_mask:0xf\n"
  "v_mov_b32 v161, v160\n"
  "s_nop 1\n"
  "v_permlane16_swap_b32 v160, v161\n"
  "v_add_f32 v160, v160, v161\n"
  "v_mov_b32 v161, v160\n"
  "s_nop 1\n"
  "v_permlane32_swap_b32 v160, v161\n"
  "v_add_f32 v160, v160, v161\n"
  "v_mul_f32 v160, 0x3be38e39, v160\n"
  "v_pk_add_f32 v[168:169], v[112:113], v[160:161] op_sel_hi:[1,0] neg_lo:[0,1] neg_hi:[0,1]\n"
  "v_pk_add_f32 v[170:171], v[114:115], v[160:161] op_sel_hi:[1,0] neg_lo:[0,1] neg_hi:[0,1]\n"
  "v_pk_add_f32 v[172:173], v[116:117], v[160:161] op_sel_hi:[1,0] neg_lo:[0,1] neg_hi:[0,1]\n"
  "v_pk_add_f32 v[174:175], v[118:119], v[160:161] op_sel_hi:[1,0] neg_lo:[0,1] neg_hi:[0,1]\n"
  "v_pk_add_f32 v[176:177], v[120:121], v[160:161] op_sel_hi:[1,0] neg_lo:[0,1] neg_hi:[0,1]\n"
  "v_pk_add_f32 v[178:179], v[122:123], v[160:161] op_sel_hi:[1,0] neg_lo:[0,1] neg_hi:[0,1]\n"
  "v_pk_add_f32 v[180:181], v[124:125], v[160:161] op_sel_hi:[1,0] neg_lo:[0,1] neg_hi:[0,1]\n"
  "v_pk_add_f32 v[182:183], v[126:127], v[160:161] op_sel_hi:[1,0] neg_lo:[0,1] neg_hi:[0,1]\n"
  "v_pk_add_f32 v[184:185], v[128:129], v[160:161] op_sel_hi:[1,0] neg_lo:[0,1] neg_hi:[0,1]\n"
  "v_pk_add_f32 v[186:187], v[130:131], v[160:161] op_sel_hi:[1,0] neg_lo:[0,1] neg_hi:[0,1]\n"
  "v_pk_mul_f32 v[162:163], v[168:169], v[168:169]\n"
  "v_pk_mul_f32 v[164:165], v[184:185], v[184:185]\n"
  "v_pk_fma_f32 v[162:163], v[170:171], v[170:171], v[162:163]\n"
  "v_pk_fma_f32 v[162:163], v[172:173], v[172:173], v[162:163]\n"
  "v_pk_fma_f32 v[162:163], v[174:175], v[174:175], v[162:163]\n"
  "v_pk_fma_f32 v[162:163], v[176:177], v[176:177], v[162:163]\n"
  "v_pk_fma_f32 v[162:163], v[178:179], v[178:179], v[162:163]\n"
  "v_pk_fma_f32 v[162:163], v[180:181], v[180:181], v[162:163]\n"
  "v_pk_fma_f32 v[162:163], v[182:183], v[182:183], v[162:163]\n"
  "v_pk_fma_f32 v[164:165], v[186:187], v[186:187], v[164:165]\n"
  "v_add_f32 v164, v164, v165\n"
  "v_add_f32 v162, v162, v163\n"
  "v_cndmask_b32 v164, 0, v164, s[68:69]\n"
  "v_add_f32 v161, v162, v164\n"
  "s_nop 1\n"
  "v_add_f32_dpp v161, v161, v161 row_ror:8 row_mask:0xf bank_mask:0xf\n"
  "v_mov_b32 v162, v161\n"
  "s_nop 1\n"
  "v_permlane16_swap_b32 v161, v162\n"
  "v_add_f32 v161, v161, v162\n"
  "v_mov_b32 v162, v161\n"
  "s_nop 1\n"
  "v_permlane32_swap_b32 v161, v162\n"
  "v_add_f32 v161, v161, v162\n"
  "ds_write_b64 v7, v[160:161]\n"
  "v_cvt_pk_bf16_f32 v136, v112, v113\n"
  "v_cvt_pk_bf16_f32 v137, v114, v115\n"
  "v_cvt_pk_bf16_f32 v138, v116, v117\n"
  "v_cvt_pk_bf16_f32 v139, v118, v119\n"
  "v_cvt_pk_bf16_f32 v140, v120, v121\n"
  "v_cvt_pk_bf16_f32 v141, v122, v123\n"
  "v_cvt_pk_bf16_f32 v142, v124, v125\n"
  "v_cvt_pk_bf16_f32 v143, v126, v127\n"
  "v_permlane16_swap_b32 v136, v138\n"
  "v_permlane16_swap_b32 v137, v139\n"
  "v_permlane16_swap_b32 v140, v142\n"
  "v_permlane16_swap_b32 v141, v143\n"
  "s_waitcnt lgkmcnt(0)\n"
  "s_barrier\n"
  "ds_read_b128 v[160:163], v1\n"
  "ds_read_b128 v[164:167], v1 offset:16\n"
  "buffer_store_dwordx4 v[136:139], v13, s[36:39], s80 offen sc1\n"
  "v_cvt_pk_bf16_f32 v144, v128, v129\n"
  "v_cvt_pk_bf16_f32 v145, v130, v131\n"
  "v_xor_b32 v7, 256, v7\n"
  "v_xor_b32 v1, 256, v1\n"
  "buffer_store_dwordx4 v[140:143], v14, s[36:39], s80 offen sc1\n"
  "s_waitcnt lgkmcnt(0)\n"
  "v_pk_add_f32 v[168:169], v[160:161], v[162:163]\n"
  "v_pk_add_f32 v[170:171], v[164:165], v[166:167]\n"
  "v_pk_add_f32 v[168:169], v[168:169], v[170:171]\n"
  "v_mul_f32 v168, 0x3e800000, v168\n"
  "v_sub_f32 v160, v160, v168\n"
  "v_sub_f32 v162, v162, v168\n"
  "v_sub_f32 v164, v164, v168\n"
  "v_sub_f32 v166, v166, v168\n"
  "v_mul_f32 v170, v160, v160\n"
  "v_fmac_f32 v170, v162, v162\n"
  "v_fmac_f32 v170, v164, v164\n"
  "v_fmac_f32 v170, v166, v166\n"
  "v_fmamk_f32 v169, v170, 0x43100000, v169\n"
  "v_mov_b32 v171, 0x3727c5ac\n"
  "v_fmamk_f32 v170, v169, 0x3ae38e39, v171\n"
  "v_rsq_f32 v170, v170\n"
  "s_nop 0\n"
  "v_mul_f32 v172, -v168, v170\n"
  "v_pk_fma_f32 v[112:113], v[112:113], v[170:171], v[172:173] op_sel_hi:[1,0,0]\n"
  "v_pk_fma_f32 v[114:115], v[114:115], v[170:171], v[172:173] op_sel_hi:[1,0,0]\n"
  "v_pk_fma_f32 v[116:117], v[116:117], v[170:171], v[172:173] op_sel_hi:[1,0,0]\n"
  "v_pk_fma_f32 v[118:119], v[118:119], v[170:171], v[172:173] op_sel_hi:[1,0,0]\n"
  "v_pk_fma_f32 v[120:121], v[120:121], v[170:171], v[172:173] op_sel_hi:[1,0,0]\n"
  "v_pk_fma_f32 v[122:123], v[122:123], v[170:171], v[172:173] op_sel_hi:[1,0,0]\n"
  "v_pk_fma_f32 v[124:125], v[124:125], v[170:171], v[172:173] op_sel_hi:[1,0,0]\n"
  "v_pk_fma_f32 v[126:127], v[126:127], v[170:171], v[172:173] op_sel_hi:[1,0,0]\n"
  "v_pk_fma_f32 v[128:129], v[128:129], v[170:171], v[172:173] op_sel_hi:[1,0,0]\n"
  "v_pk_fma_f32 v[130:131], v[130:131], v[170:171], v[172:173] op_sel_hi:[1,0,0]\n"
  "v_cvt_pk_bf16_f32 v148, v112, v113\n"
  "v_cvt_pk_bf16_f32 v149, v114, v115\n"
  "v_cvt_pk_bf16_f32 v150, v116, v117\n"
  "v_cvt_pk_bf16_f32 v151, v118, v119\n"
  "v_cvt_pk_bf16_f32 v152, v120, v121\n"
  "v_cvt_pk_bf16_f32 v153, v122, v123\n"
  "v_cvt_pk_bf16_f32 v154, v124, v125\n"
  "v_cvt_pk_bf16_f32 v155, v126, v127\n"
  "v_permlane16_swap_b32 v148, v150\n"
  "v_permlane16_swap_b32 v149, v151\n"
  "v_permlane16_swap_b32 v152, v154\n"
  "v_permlane16_swap_b32 v153, v155\n"
  "buffer_store_dwordx4 v[148:151], v13, s[36:39], s79 offen sc1\n"
  "v_cvt_pk_bf16_f32 v146, v128, v129\n"
  "v_cvt_pk_bf16_f32 v147, v130, v131\n"
  "buffer_store_dwordx4 v[152:155], v14, s[36:39], s79 offen sc1\n"
  "s_nop 0\n"
  "v_permlane16_swap_b32 v144, v146\n"
  "v_permlane16_swap_b32 v145, v147\n"
  "buffer_store_dwordx4 v[144:147], v15, s[36:39], s80 offen sc1\n"
  "buffer_load_dword v4, v2, s[36:39], 0 offen sc1\n"
  "v_mov_b32 v160, s70\n"
  "s_add_u32 s70, s70, 1\n"
  "s_add_u32 s78, s70, 0\n"
  "s_and_b32 s78, s78, s61\n"
  "s_mul_i32 s78, s78, s60\n"
  "s_lshr_b32 s76, s78, 8\n"
  "s_and_b32 s76, s76, 8192\n"
  "s_add_u32 s78, s78, s59\n"
  "s_xor_b32 s78, s78, s76\n"
  "s_add_u32 s84, s70, 0\n"
  "s_and_b32 s84, s84, 15\n"
  "s_lshl_b32 s84, s84, 22\n"
  "s_add_u32 s84, s84, s64\n"
  "s_lshl_b32 s80, s70, 18\n"
  "s_lshr_b32 s76, s80, 8\n"
  "s_and_b32 s76, s76, 8192\n"
  "s_add_u32 s80, s80, s65\n"
  "s_xor_b32 s80, s80, s76\n"
  "s_sub_u32 s79, s80, 81920\n"
  "s_add_u32 s77, s84, 327680\n"
  "s_waitcnt vmcnt(1)\n"
  "s_barrier\n"
  "s_mov_b64 exec, 1\n"
  "buffer_store_dword v160, off, s[36:39], s58 sc1\n"
  "s_mov_b64 exec, -1\n"
  "s_waitcnt vmcnt(1)\n"
  "v_subrev_u32 v6, v3, v4\n"
  "s_add_u32 s74, s70, 0\n"
  "v_cmp_le_i32 vcc, s74, v6\n"
  "s_cmp_eq_u64 vcc, exec\n"
  "s_cbranch_scc1 L_both_%=\n"
  "s_cmp_lt_u32 s70, 64\n"
  "s_cbranch_scc1 L_wait_%=\n"
  "s_waitcnt vmcnt(0)\n"
  "L_c1_%=:\n"
  "s_endpgm\n"
      : : R_OPERANDS : OWNED);
  } else {
    asm volatile(
  "s_getpc_b64 s[0:1]\n"
  "L_c0_%=:\n"
  "v_mbcnt_lo_u32_b32 v5, -1, 0\n"
  "v_mbcnt_hi_u32_b32 v5, -1, v5\n"
  "v_lshlrev_b32 v5, 4, v5\n"
  "v_add_lshl_u32 v5, v5, s57, 2\n"
  "v_min_u32 v5, L_c1_%=-L_c0_%=, v5\n"
  "global_load_dword v5, v5, s[0:1]\n"
  "s_nop 4\n"
  "s_lshr_b32 s75, s57, 10\n"
  "s_mul_i32 s75, s75, 9\n"
  "s_add_u32 s74, s75, 0\n"
  "s_lshr_b32 s76, s74, 3\n"
  "s_lshl_b32 s76, s76, 14\n"
  "s_and_b32 s74, s74, 7\n"
  "s_lshl_b32 s74, s74, 10\n"
  "s_or_b32 s4, s74, s76\n"
  "s_add_u32 s74, s75, 1\n"
  "s_lshr_b32 s76, s74, 3\n"
  "s_lshl_b32 s76, s76, 14\n"
  "s_and_b32 s74, s74, 7\n"
  "s_lshl_b32 s74, s74, 10\n"
  "s_or_b32 s5, s74, s76\n"
  "s_add_u32 s74, s75, 2\n"
  "s_lshr_b32 s76, s74, 3\n"
  "s_lshl_b32 s76, s76, 14\n"
  "s_and_b32 s74, s74, 7\n"
  "s_lshl_b32 s74, s74, 10\n"
  "s_or_b32 s6, s74, s76\n"
  "s_add_u32 s74, s75, 3\n"
  "s_lshr_b32 s76, s74, 3\n"
  "s_lshl_b32 s76, s76, 14\n"
  "s_and_b32 s74, s74, 7\n"
  "s_lshl_b32 s74, s74, 10\n"
  "s_or_b32 s7, s74, s76\n"
  "s_add_u32 s74, s75, 4\n"
  "s_lshr_b32 s76, s74, 3\n"
  "s_lshl_b32 s76, s76, 14\n"
  "s_and_b32 s74, s74, 7\n"
  "s_lshl_b32 s74, s74, 10\n"
  "s_or_b32 s8, s74, s76\n"
  "s_add_u32 s74, s75, 5\n"
  "s_lshr_b32 s76, s74, 3\n"
  "s_lshl_b32 s76, s76, 14\n"
  "s_and_b32 s74, s74, 7\n"
  "s_lshl_b32 s74, s74, 10\n"
  "s_or_b32 s9, s74, s76\n"
  "s_add_u32 s74, s75, 6\n"
  "s_lshr_b32 s76, s74, 3\n"
  "s_lshl_b32 s76, s76, 14\n"
  "s_and_b32 s74, s74, 7\n"
  "s_lshl_b32 s74, s74, 10\n"
  "s_or_b32 s10, s74, s76\n"
  "s_add_u32 s74, s75, 7\n"
  "s_lshr_b32 s76, s74, 3\n"
  "s_lshl_b32 s76, s76, 14\n"
  "s_and_b32 s74, s74, 7\n"
  "s_lshl_b32 s74, s74, 10\n"
  "s_or_b32 s11, s74, s76\n"
  "s_add_u32 s74, s75, 8\n"
  "s_lshr_b32 s76, s74, 3\n"
  "s_lshl_b32 s76, s76, 14\n"
  "s_and_b32 s74, s74, 7\n"
  "s_lshl_b32 s74, s74, 10\n"
  "s_or_b32 s12, s74, s76\n"
  "s_mov_b32 s70, 0\n"
  "s_mov_b32 s72, 0\n"
  "s_mov_b32 s68, -1\n"
  "s_mov_b32 s69, 0\n"
  "s_cmp_eq_u32 s57, 0\n"
  "s_cselect_b32 s58, s58, 0x80000000\n"
  "s_cmp_eq_u32 s67, 0\n"
  "s_cbranch_scc1 L_main_%=\n"
  "s_mov_b32 s70, 1\n"
  "s_mov_b32 s78, 36864\n"
  "s_cmp_eq_u32 s57, 0\n"
  "s_cselect_b32 s62, s62, 0x80000000\n"
  "buffer_load_dwordx4 v[192:195], v52, s[44:47], s78 offen\n"
  "buffer_load_dwordx4 v[196:199], v53, s[44:47], s78 offen\n"
  "buffer_load_dwordx4 v[200:203], v54, s[44:47], s78 offen\n"
  "s_waitcnt vmcnt(0)\n"
  "L_r0b_%=:\n"
  "s_lshl_b32 s80, s70, 18\n"
  "s_lshr_b32 s76, s80, 8\n"
  "s_and_b32 s76, s76, 8192\n"
  "s_add_u32 s80, s80, s67\n"
  "s_xor_b32 s80, s80, s76\n"
  "s_sub_u32 s79, s80, 81920\n"
  "s_add_u32 s78, s78, 73728\n"
  "v_mfma_f32_16x16x32_bf16 v[112:115], v[24:27], v[192:195], 0\n"
  "v_mfma_f32_16x16x32_bf16 v[116:119], v[28:31], v[192:195], 0\n"
  "v_mfma_f32_16x16x32_bf16 v[120:123], v[24:27], v[196:199], 0\n"
  "v_mfma_f32_16x16x32_bf16 v[124:127], v[28:31], v[196:199], 0\n"
  "v_mfma_f32_16x16x32_bf16 v[128:131], v[24:27], v[200:203], 0\n"
  "buffer_load_dwordx4 v[192:195], v52, s[44:47], s78 offen\n"
  "buffer_load_dwordx4 v[196:199], v53, s[44:47], s78 offen\n"
  "buffer_load_dwordx4 v[200:203], v54, s[44:47], s78 offen\n"
  "s_nop 1\n"
  "v_pk_add_f32 v[160:161], v[112:113], v[114:115]\n"
  "v_pk_add_f32 v[162:163], v[116:117], v[118:119]\n"
  "v_pk_add_f32 v[164:165], v[120:121], v[122:123]\n"
  "v_pk_add_f32 v[166:167], v[124:125], v[126:127]\n"
  "v_pk_add_f32 v[160:161], v[160:161], v[162:163]\n"
  "v_pk_add_f32 v[164:165], v[164:165], v[166:167]\n"
  "v_pk_add_f32 v[168:169], v[128:129], v[130:131]\n"
  "v_pk_add_f32 v[160:161], v[160:161], v[164:165]\n"
  "v_pk_add_f32 v[160:161], v[160:161], v[168:169]\n"
  "v_add_f32 v160, v160, v161\n"
  "s_nop 1\n"
  "v_add_f32_dpp v160, v160, v160 row_ror:8 row_mask:0xf bank_mask:0xf\n"
  "v_mov_b32 v161, v160\n"
  "s_nop 1\n"
  "v_permlane16_swap_b32 v160, v161\n"
  "v_add_f32 v160, v160, v161\n"
  "v_mov_b32 v161, v160\n"
  "s_nop 1\n"
  "v_permlane32_swap_b32 v160, v161\n"
  "v_add_f32 v160, v160, v161\n"
  "v_mul_f32 v160, 0x3be38e39, v160\n"
  "v_pk_add_f32 v[168:169], v[112:113], v[160:161] op_sel_hi:[1,0] neg_lo:[0,1] neg_hi:[0,1]\n"
  "v_pk_add_f32 v[170:171], v[114:115], v[160:161] op_sel_hi:[1,0] neg_lo:[0,1] neg_hi:[0,1]\n"
  "v_pk_add_f32 v[172:173], v[116:117], v[160:161] op_sel_hi:[1,0] neg_lo:[0,1] neg_hi:[0,1]\n"
  "v_pk_add_f32 v[174:175], v[118:119], v[160:161] op_sel_hi:[1,0] neg_lo:[0,1] neg_hi:[0,1]\n"
  "v_pk_add_f32 v[176:177], v[120:121], v[160:161] op_sel_hi:[1,0] neg_lo:[0,1] neg_hi:[0,1]\n"
  "v_pk_add_f32 v[178:179], v[122:123], v[160:161] op_sel_hi:[1,0] neg_lo:[0,1] neg_hi:[0,1]\n"
  "v_pk_add_f32 v[180:181], v[124:125], v[160:161] op_sel_hi:[1,0] neg_lo:[0,1] neg_hi:[0,1]\n"
  "v_pk_add_f32 v[182:183], v[126:127], v[160:161] op_sel_hi:[1,0] neg_lo:[0,1] neg_hi:[0,1]\n"
  "v_pk_add_f32 v[184:185], v[128:129], v[160:161] op_sel_hi:[1,0] neg_lo:[0,1] neg_hi:[0,1]\n"
  "v_pk_add_f32 v[186:187], v[130:131], v[160:161] op_sel_hi:[1,0] neg_lo:[0,1] neg_hi:[0,1]\n"
  "v_pk_mul_f32 v[162:163], v[168:169], v[168:169]\n"
  "v_pk_mul_f32 v[164:165], v[184:185], v[184:185]\n"
  "v_pk_fma_f32 v[162:163], v[170:171], v[170:171], v[162:163]\n"
  "v_pk_fma_f32 v[162:163], v[172:173], v[172:173], v[162:163]\n"
  "v_pk_fma_f32 v[162:163], v[174:175], v[174:175], v[162:163]\n"
  "v_pk_fma_f32 v[162:163], v[176:177], v[176:177], v[162:163]\n"
  "v_pk_fma_f32 v[162:163], v[178:179], v[178:179], v[162:163]\n"
  "v_pk_fma_f32 v[162:163], v[180:181], v[180:181], v[162:163]\n"
  "v_pk_fma_f32 v[162:163], v[182:183], v[182:183], v[162:163]\n"
  "v_pk_fma_f32 v[164:165], v[186:187], v[186:187], v[164:165]\n"
  "v_add_f32 v164, v164, v165\n"
  "v_add_f32 v162, v162, v163\n"
  "v_cndmask_b32 v164, 0, v164, s[68:69]\n"
  "v_add_f32 v161, v162, v164\n"
  "s_nop 1\n"
  "v_add_f32_dpp v161, v161, v161 row_ror:8 row_mask:0xf bank_mask:0xf\n"
  "v_mov_b32 v162, v161\n"
  "s_nop 1\n"
  "v_permlane16_swap_b32 v161, v162\n"
  "v_add_f32 v161, v161, v162\n"
  "v_mov_b32 v162, v161\n"
  "s_nop 1\n"
  "v_permlane32_swap_b32 v161, v162\n"
  "v_add_f32 v161, v161, v162\n"
  "ds_write_b64 v7, v[160:161]\n"
  "v_cvt_pk_bf16_f32 v136, v112, v113\n"
  "v_cvt_pk_bf16_f32 v137, v114, v115\n"
  "v_cvt_pk_bf16_f32 v138, v116, v117\n"
  "v_cvt_pk_bf16_f32 v139, v118, v119\n"
  "v_cvt_pk_bf16_f32 v140, v120, v121\n"
  "v_cvt_pk_bf16_f32 v141, v122, v123\n"
  "v_cvt_pk_bf16_f32 v142, v124, v125\n"
  "v_cvt_pk_bf16_f32 v143, v126, v127\n"
  "v_permlane16_swap_b32 v136, v138\n"
  "v_permlane16_swap_b32 v137, v139\n"
  "v_permlane16_swap_b32 v140, v142\n"
  "v_permlane16_swap_b32 v141, v143\n"
  "s_waitcnt lgkmcnt(0)\n"
  "s_barrier\n"
  "ds_read_b128 v[160:163], v1\n"
  "ds_read_b128 v[164:167], v1 offset:16\n"
  "buffer_store_dwordx4 v[136:139], v55, s[36:39], s80 offen sc1\n"
  "v_cvt_pk_bf16_f32 v144, v128, v129\n"
  "v_cvt_pk_bf16_f32 v145, v130, v131\n"
  "v_xor_b32 v7, 256, v7\n"
  "v_xor_b32 v1, 256, v1\n"
  "buffer_store_dwordx4 v[140:143], v56, s[36:39], s80 offen sc1\n"
  "s_waitcnt lgkmcnt(0)\n"
  "v_pk_add_f32 v[168:169], v[160:161], v[162:163]\n"
  "v_pk_add_f32 v[170:171], v[164:165], v[166:167]\n"
  "v_pk_add_f32 v[168:169], v[168:169], v[170:171]\n"
  "v_mul_f32 v168, 0x3e800000, v168\n"
  "v_sub_f32 v160, v160, v168\n"
  "v_sub_f32 v162, v162, v168\n"
  "v_sub_f32 v164, v164, v168\n"
  "v_sub_f32 v166, v166, v168\n"
  "v_mul_f32 v170, v160, v160\n"
  "v_fmac_f32 v170, v162, v162\n"
  "v_fmac_f32 v170, v164, v164\n"
  "v_fmac_f32 v170, v166, v166\n"
  "v_fmamk_f32 v169, v170, 0x43100000, v169\n"
  "v_mov_b32 v171, 0x3727c5ac\n"
  "v_fmamk_f32 v170, v169, 0x3ae38e39, v171\n"
  "v_rsq_f32 v170, v170\n"
  "s_nop 0\n"
  "v_mul_f32 v172, -v168, v170\n"
  "v_pk_fma_f32 v[112:113], v[112:113], v[170:171], v[172:173] op_sel_hi:[1,0,0]\n"
  "v_pk_fma_f32 v[114:115], v[114:115], v[170:171], v[172:173] op_sel_hi:[1,0,0]\n"
  "v_pk_fma_f32 v[116:117], v[116:117], v[170:171], v[172:173] op_sel_hi:[1,0,0]\n"
  "v_pk_fma_f32 v[118:119], v[118:119], v[170:171], v[172:173] op_sel_hi:[1,0,0]\n"
  "v_pk_fma_f32 v[120:121], v[120:121], v[170:171], v[172:173] op_sel_hi:[1,0,0]\n"
  "v_pk_fma_f32 v[122:123], v[122:123], v[170:171], v[172:173] op_sel_hi:[1,0,0]\n"
  "v_pk_fma_f32 v[124:125], v[124:125], v[170:171], v[172:173] op_sel_hi:[1,0,0]\n"
  "v_pk_fma_f32 v[126:127], v[126:127], v[170:171], v[172:173] op_sel_hi:[1,0,0]\n"
  "v_pk_fma_f32 v[128:129], v[128:129], v[170:171], v[172:173] op_sel_hi:[1,0,0]\n"
  "v_pk_fma_f32 v[130:131], v[130:131], v[170:171], v[172:173] op_sel_hi:[1,0,0]\n"
  "v_cvt_pk_bf16_f32 v148, v112, v113\n"
  "v_cvt_pk_bf16_f32 v149, v114, v115\n"
  "v_cvt_pk_bf16_f32 v150, v116, v117\n"
  "v_cvt_pk_bf16_f32 v151, v118, v119\n"
  "v_cvt_pk_bf16_f32 v152, v120, v121\n"
  "v_cvt_pk_bf16_f32 v153, v122, v123\n"
  "v_cvt_pk_bf16_f32 v154, v124, v125\n"
  "v_cvt_pk_bf16_f32 v155, v126, v127\n"
  "v_permlane16_swap_b32 v148, v150\n"
  "v_permlane16_swap_b32 v149, v151\n"
  "v_permlane16_swap_b32 v152, v154\n"
  "v_permlane16_swap_b32 v153, v155\n"
  "buffer_store_dwordx4 v[148:151], v55, s[36:39], s79 offen sc1\n"
  "v_cvt_pk_bf16_f32 v146, v128, v129\n"
  "v_cvt_pk_bf16_f32 v147, v130, v131\n"
  "buffer_store_dwordx4 v[152:155], v56, s[36:39], s79 offen sc1\n"
  "s_nop 0\n"
  "v_permlane16_swap_b32 v144, v146\n"
  "v_permlane16_swap_b32 v145, v147\n"
  "buffer_store_dwordx4 v[144:147], v57, s[36:39], s80 offen sc1\n"
  "s_add_u32 s70, s70, 1\n"
  "v_mov_b32 v160, s70\n"
  "s_add_u32 s70, s70, 1\n"
  "s_waitcnt vmcnt(0)\n"
  "s_barrier\n"
  "s_mov_b64 exec, 1\n"
  "buffer_store_dword v160, off, s[36:39], s62 sc1\n"
  "s_mov_b64 exec, -1\n"
  "s_cmp_lt_u32 s70, 64\n"
  "s_cbranch_scc1 L_r0b_%=\n"
  "s_mov_b32 s70, 0\n"
  "L_main_%=:\n"
  "s_add_u32 s78, s70, 0\n"
  "s_and_b32 s78, s78, s61\n"
  "s_mul_i32 s78, s78, s60\n"
  "s_lshr_b32 s76, s78, 8\n"
  "s_and_b32 s76, s76, 8192\n"
  "s_add_u32 s78, s78, s59\n"
  "s_xor_b32 s78, s78, s76\n"
  "s_add_u32 s84, s70, 0\n"
  "s_and_b32 s84, s84, 15\n"
  "s_lshl_b32 s84, s84, 22\n"
  "s_add_u32 s84, s84, s64\n"
  "s_mul_i32 s80, s70, 36864\n"
  "s_add_u32 s77, s84, 327680\n"
  "L_wait_%=:\n"
  "s_cmp_lg_u32 s72, 0\n"
  "s_cselect_b32 s73, 0, 32768\n"
  "v_add_u32 v6, s70, v9\n"
  "L_rpoll_%=:\n"
  "buffer_load_dword v4, v2, s[36:39], 0 offen sc1\n"
  "s_sub_u32 s73, s73, 1\n"
  "s_cselect_b32 s72, 1, s72\n"
  "s_cselect_b64 s[2:3], 0, -1\n"
  "s_waitcnt vmcnt(0)\n"
  "v_cmp_gt_i32 vcc, v6, v4\n"
  "s_and_b64 vcc, vcc, s[2:3]\n"
  "s_cbranch_scc1 L_rpoll_%=\n"
  "L_rpoll_x_%=:\n"
  "buffer_load_dwordx4 v[192:195], v10, s[36:39], s78 offen sc1\n"
  "buffer_load_dwordx4 v[196:199], v11, s[36:39], s78 offen sc1\n"
  "buffer_load_dwordx4 v[200:203], v12, s[36:39], s78 offen sc1\n"
  "s_cmp_lg_u32 s72, 0\n"
  "s_cselect_b32 s73, 0, 32768\n"
  "v_add_u32 v6, s70, v3\n"
  "L_poll_%=:\n"
  "buffer_load_dword v4, v2, s[36:39], 0 offen sc1\n"
  "s_sub_u32 s73, s73, 1\n"
  "s_cselect_b32 s72, 1, s72\n"
  "s_cselect_b64 s[2:3], 0, -1\n"
  "s_waitcnt vmcnt(0)\n"
  "v_cmp_gt_i32 vcc, v6, v4\n"
  "s_and_b64 vcc, vcc, s[2:3]\n"
  "s_cbranch_scc1 L_poll_%=\n"
  "L_poll_x_%=:\n"
  "s_branch L_job_%=\n"
  "L_both_%=:\n"
  "buffer_load_dwordx4 v[192:195], v10, s[36:39], s78 offen sc1\n"
  "buffer_load_dwordx4 v[196:199], v11, s[36:39], s78 offen sc1\n"
  "buffer_load_dwordx4 v[200:203], v12, s[36:39], s78 offen sc1\n"
  "L_job_%=:\n"
  "s_add_u32 s76, s84, s4\n"
  "buffer_load_dwordx4 v[64:67], v0, s[36:39], s76 offen sc1\n"
  "s_add_u32 s76, s84, s5\n"
  "buffer_load_dwordx4 v[68:71], v0, s[36:39], s76 offen sc1\n"
  "s_add_u32 s76, s84, s6\n"
  "buffer_load_dwordx4 v[72:75], v0, s[36:39], s76 offen sc1\n"
  "s_add_u32 s76, s84, s7\n"
  "buffer_load_dwordx4 v[76:79], v0, s[36:39], s76 offen sc1\n"
  "s_add_u32 s76, s84, s8\n"
  "buffer_load_dwordx4 v[80:83], v0, s[36:39], s76 offen sc1\n"
  "s_add_u32 s76, s84, s9\n"
  "buffer_load_dwordx4 v[84:87], v0, s[36:39], s76 offen sc1\n"
  "s_add_u32 s76, s84, s10\n"
  "buffer_load_dwordx4 v[88:91], v0, s[36:39], s76 offen sc1\n"
  "s_add_u32 s76, s84, s11\n"
  "buffer_load_dwordx4 v[92:95], v0, s[36:39], s76 offen sc1\n"
  "s_add_u32 s76, s84, s12\n"
  "buffer_load_dwordx4 v[96:99], v0, s[36:39], s76 offen sc1\n"
  "buffer_load_dwordx4 v[100:103], v10, s[36:39], s77 offen sc1\n"
  "buffer_load_dwordx4 v[104:107], v11, s[36:39], s77 offen sc1\n"
  "buffer_load_dwordx4 v[108:111], v12, s[36:39], s77 offen sc1\n"
  "s_waitcnt vmcnt(12)\n"
  "v_mfma_f32_16x16x32_bf16 v[112:115], v[24:27], v[192:195], v[32:35]\n"
  "v_mfma_f32_16x16x32_bf16 v[116:119], v[28:31], v[192:195], v[36:39]\n"
  "v_mfma_f32_16x16x32_bf16 v[120:123], v[24:27], v[196:199], v[40:43]\n"
  "v_mfma_f32_16x16x32_bf16 v[124:127], v[28:31], v[196:199], v[44:47]\n"
  "v_mfma_f32_16x16x32_bf16 v[128:131], v[24:27], v[200:203], v[48:51]\n"
  "s_waitcnt vmcnt(11)\n"
  "v_mfma_f32_16x16x32_bf16 v[112:115], v[16:19], v[64:67], v[112:115]\n"
  "s_waitcnt vmcnt(10)\n"
  "v_mfma_f32_16x16x32_bf16 v[112:115], v[20:23], v[68:71], v[112:115]\n"
  "s_waitcnt vmcnt(9)\n"
  "v_mfma_f32_16x16x32_bf16 v[116:119], v[16:19], v[72:75], v[116:119]\n"
  "s_waitcnt vmcnt(8)\n"
  "v_mfma_f32_16x16x32_bf16 v[116:119], v[20:23], v[76:79], v[116:119]\n"
  "s_waitcnt vmcnt(7)\n"
  "v_mfma_f32_16x16x32_bf16 v[120:123], v[16:19], v[80:83], v[120:123]\n"
  "s_waitcnt vmcnt(6)\n"
  "v_mfma_f32_16x16x32_bf16 v[120:123], v[20:23], v[84:87], v[120:123]\n"
  "s_waitcnt vmcnt(5)\n"
  "v_mfma_f32_16x16x32_bf16 v[124:127], v[16:19], v[88:91], v[124:127]\n"
  "s_waitcnt vmcnt(4)\n"
  "v_mfma_f32_16x16x32_bf16 v[124:127], v[20:23], v[92:95], v[124:127]\n"
  "s_waitcnt vmcnt(3)\n"
  "v_mfma_f32_16x16x32_bf16 v[128:131], v[16:19], v[96:99], v[128:131]\n"
  "s_waitcnt vmcnt(2)\n"
  "v_mfma_f32_16x16x32_bf16 v[112:115], v[24:27], v[100:103], v[112:115]\n"
  "v_mfma_f32_16x16x32_bf16 v[116:119], v[28:31], v[100:103], v[116:119]\n"
  "s_waitcnt vmcnt(1)\n"
  "v_mfma_f32_16x16x32_bf16 v[120:123], v[24:27], v[104:107], v[120:123]\n"
  "v_mfma_f32_16x16x32_bf16 v[124:127], v[28:31], v[104:107], v[124:127]\n"
  "s_waitcnt vmcnt(0)\n"
  "v_mfma_f32_16x16x32_bf16 v[128:131], v[24:27], v[108:111], v[128:131]\n"
  "s_nop 1\n"
  "v_cvt_pk_bf16_f32 v136, v112, v113\n"
  "v_cvt_pk_bf16_f32 v137, v114, v115\n"
  "v_cvt_pk_bf16_f32 v138, v116, v117\n"
  "v_cvt_pk_bf16_f32 v139, v118, v119\n"
  "v_cvt_pk_bf16_f32 v140, v120, v121\n"
  "v_cvt_pk_bf16_f32 v141, v122, v123\n"
  "v_cvt_pk_bf16_f32 v142, v124, v125\n"
  "v_cvt_pk_bf16_f32 v143, v126, v127\n"
  "v_permlane16_swap_b32 v136, v138\n"
  "v_permlane16_swap_b32 v137, v139\n"
  "v_permlane16_swap_b32 v140, v142\n"
  "v_permlane16_swap_b32 v141, v143\n"
  "buffer_store_dwordx4 v[136:139], v13, s[40:43], s80 offen\n"
  "v_cvt_pk_bf16_f32 v144, v128, v129\n"
  "v_cvt_pk_bf16_f32 v145, v130, v131\n"
  "v_cvt_pk_bf16_f32 v146, v128, v129\n"
  "v_cvt_pk_bf16_f32 v147, v130, v131\n"
  "buffer_store_dwordx4 v[140:143], v14, s[40:43], s80 offen\n"
  "s_nop 0\n"
  "v_permlane16_swap_b32 v144, v146\n"
  "v_permlane16_swap_b32 v145, v147\n"
  "buffer_store_dwordx4 v[144:147], v15, s[40:43], s80 offen\n"
  "buffer_load_dword v4, v2, s[36:39], 0 offen sc1\n"
  "v_mov_b32 v160, s70\n"
  "s_add_u32 s70, s70, 1\n"
  "s_add_u32 s78, s70, 0\n"
  "s_and_b32 s78, s78, s61\n"
  "s_mul_i32 s78, s78, s60\n"
  "s_lshr_b32 s76, s78, 8\n"
  "s_and_b32 s76, s76, 8192\n"
  "s_add_u32 s78, s78, s59\n"
  "s_xor_b32 s78, s78, s76\n"
  "s_add_u32 s84, s70, 0\n"
  "s_and_b32 s84, s84, 15\n"
  "s_lshl_b32 s84, s84, 22\n"
  "s_add_u32 s84, s84, s64\n"
  "s_mul_i32 s80, s70, 36864\n"
  "s_add_u32 s77, s84, 327680\n"
  "s_waitcnt vmcnt(1)\n"
  "s_barrier\n"
  "s_mov_b64 exec, 1\n"
  "buffer_store_dword v160, off, s[36:39], s58 sc1\n"
  "s_mov_b64 exec, -1\n"
  "s_waitcnt vmcnt(1)\n"
  "v_subrev_u32 v6, v3, v4\n"
  "s_add_u32 s74, s70, 0\n"
  "v_cmp_le_i32 vcc, s74, v6\n"
  "s_cmp_eq_u64 vcc, exec\n"
  "s_cbranch_scc1 L_both_%=\n"
  "s_cmp_lt_u32 s70, 64\n"
  "s_cbranch_scc1 L_wait_%=\n"
  "s_waitcnt vmcnt(0)\n"
  "L_c1_%=:\n"
  "s_endpgm\n"
      : : R_OPERANDS : OWNED);
  }
}

constexpr unsigned C_KX = 147456, C_QK = 155648, C_OX = 159744, C_DUM = 162816;
DI void role_chain(Ctx& c, const Params& p, int h) {
  const unsigned ln = (unsigned)c.lane, w = (unsigned)c.wave, g = ln >> 4, l16 = ln * 16u, wbase = w * 36864u;
  auto rowof = [](unsigned i) { const unsigned gb = (i >> 2) & 3, q = i & 3; return 16 * (gb >> 1) + 4 * (gb & 1) + 8 * (q >> 1) + (q & 1); };
  const unsigned ocol = (unsigned)(128 * h) * 1152u, q = (ln >> 2) & 3u, pp = ln & 3u;
  const unsigned v64 = rowof(4u * w + q) * 8192u + g * 64u + pp * 16u + (unsigned)(256 * h);
  const unsigned vo0 = rowof(ln >> 3) * 1152u + (ln & 7u) * 16u + ocol + w * 256u;
  const unsigned vo8 = (rowof((ln >> 1) & 15u) + 2u * (ln >> 5)) * 1152u + (ln & 1u) * 16u + ocol + 1024u + w * 32u;
  const unsigned r64 = g * 1024u + w * 256u + q * 64u;
  const int page0 = 16 * (h >> 3) + (h & 7), page2 = (h & 3) == 3 ? 64 + (h >> 2) : -1;
  const int page = c.wave < 2 ? page0 + 8 * c.wave : (c.wave == 2 ? page2 : -1);
  const unsigned n = ln & 15u, ag0 = P_AG + (c.bit ^ 8192u);
  v4u sel;
#pragma unroll
  for (int j = 0; j < 4; ++j) { const unsigned n0 = 4 * (j >> 1) + (j & 1); sel[j] = n == n0 ? 0x3f80u : (n == n0 + 2 ? 0x3f800000u : 0u); }
  asm volatile(
  "s_getpc_b64 s[0:1]\n"
  "L_c0_%=:\n"
  "v_mbcnt_lo_u32_b32 v10, -1, 0\n"
  "v_mbcnt_hi_u32_b32 v10, -1, v10\n"
  "v_lshlrev_b32 v10, 4, v10\n"
  "v_add_lshl_u32 v10, v10, s68, 2\n"
  "v_min_u32 v10, L_c1_%=-L_c0_%=, v10\n"
  "global_load_dword v10, v10, s[0:1]\n"
  "s_nop 4\n"
  "ds_write_b128 v8, v[20:23] offset:2048\n"
  "s_add_u32 m0, s68, 0\n"
  "s_mov_b32 s74, 0\n"
  "buffer_load_dwordx4 v12, s[44:47], s74 offen lds\n"
  "s_add_u32 m0, s68, 4096\n"
  "s_mov_b32 s74, 16384\n"
  "buffer_load_dwordx4 v12, s[44:47], s74 offen lds\n"
  "s_add_u32 m0, s68, 8192\n"
  "s_mov_b32 s74, 262144\n"
  "buffer_load_dwordx4 v12, s[44:47], s74 offen lds\n"
  "s_add_u32 m0, s68, 12288\n"
  "s_mov_b32 s74, 278528\n"
  "buffer_load_dwordx4 v12, s[44:47], s74 offen lds\n"
  "s_add_u32 m0, s68, 16384\n"
  "s_mov_b32 s74, 524288\n"
  "buffer_load_dwordx4 v12, s[44:47], s74 offen lds\n"
  "s_add_u32 m0, s68, 20480\n"
  "s_mov_b32 s74, 540672\n"
  "buffer_load_dwordx4 v12, s[44:47], s74 offen lds\n"
  "s_add_u32 m0, s68, 24576\n"
  "s_mov_b32 s74, 786432\n"
  "buffer_load_dwordx4 v12, s[44:47], s74 offen lds\n"
  "s_add_u32 m0, s68, 28672\n"
  "s_mov_b32 s74, 802816\n"
  "buffer_load_dwordx4 v12, s[44:47], s74 offen lds\n"
  "s_add_u32 m0, s68, 32768\n"
  "s_mov_b32 s74, 1048576\n"
  "buffer_load_dwordx4 v12, s[44:47], s74 offen lds\n"
  "s_add_u32 m0, s68, 36864\n"
  "s_mov_b32 s74, 1064960\n"
  "buffer_load_dwordx4 v12, s[44:47], s74 offen lds\n"
  "s_add_u32 m0, s68, 40960\n"
  "s_mov_b32 s74, 1310720\n"
  "buffer_load_dwordx4 v12, s[44:47], s74 offen lds\n"
  "s_add_u32 m0, s68, 45056\n"
  "s_mov_b32 s74, 1327104\n"
  "buffer_load_dwordx4 v12, s[44:47], s74 offen lds\n"
  "s_add_u32 m0, s68, 49152\n"
  "s_mov_b32 s74, 1572864\n"
  "buffer_load_dwordx4 v12, s[44:47], s74 offen lds\n"
  "s_add_u32 m0, s68, 53248\n"
  "s_mov_b32 s74, 1589248\n"
  "buffer_load_dwordx4 v12, s[44:47], s74 offen lds\n"
  "s_add_u32 m0, s68, 57344\n"
  "s_mov_b32 s74, 1835008\n"
  "buffer_load_dwordx4 v12, s[44:47], s74 offen lds\n"
  "s_add_u32 m0, s68, 61440\n"
  "s_mov_b32 s74, 1851392\n"
  "buffer_load_dwordx4 v12, s[44:47], s74 offen lds\n"
  "s_add_u32 m0, s68, 65536\n"
  "s_mov_b32 s74, 2097152\n"
  "buffer_load_dwordx4 v12, s[44:47], s74 offen lds\n"
  "s_add_u32 m0, s68, 69632\n"
  "s_mov_b32 s74, 2113536\n"
  "buffer_load_dwordx4 v12, s[44:47], s74 offen lds\n"
  "s_add_u32 m0, s68, 73728\n"
  "s_mov_b32 s74, 2359296\n"
  "buffer_load_dwordx4 v12, s[44:47], s74 offen lds\n"
  "s_add_u32 m0, s68, 77824\n"
  "s_mov_b32 s74, 2375680\n"
  "buffer_load_dwordx4 v12, s[44:47], s74 offen lds\n"
  "s_add_u32 m0, s68, 81920\n"
  "s_mov_b32 s74, 2621440\n"
  "buffer_load_dwordx4 v12, s[44:47], s74 offen lds\n"
  "s_add_u32 m0, s68, 86016\n"
  "s_mov_b32 s74, 2637824\n"
  "buffer_load_dwordx4 v12, s[44:47], s74 offen lds\n"
  "s_add_u32 m0, s68, 90112\n"
  "s_mov_b32 s74, 2883584\n"
  "buffer_load_dwordx4 v12, s[44:47], s74 offen lds\n"
  "s_add_u32 m0, s68, 94208\n"
  "s_mov_b32 s74, 2899968\n"
  "buffer_load_dwordx4 v12, s[44:47], s74 offen lds\n"
  "s_waitcnt vmcnt(20) lgkmcnt(0)\n"
  "s_barrier\n"
  "s_add_u32 m0, s68, 98304\n"
  "s_mov_b32 s74, 3145728\n"
  "buffer_load_dwordx4 v12, s[44:47], s74 offen lds\n"
  "s_add_u32 m0, s68, 102400\n"
  "s_mov_b32 s74, 3162112\n"
  "buffer_load_dwordx4 v12, s[44:47], s74 offen lds\n"
  "s_add_u32 m0, s68, 106496\n"
  "s_mov_b32 s74, 3407872\n"
  "buffer_load_dwordx4 v12, s[44:47], s74 offen lds\n"
  "s_add_u32 m0, s68, 110592\n"
  "s_mov_b32 s74, 3424256\n"
  "buffer_load_dwordx4 v12, s[44:47], s74 offen lds\n"
  "v_add_u32 v11, 0, v15\n"
  "ds_read_b64_tr_b16 a[16:17], v11 offset:0\n"
  "ds_read_b64_tr_b16 a[18:19], v11 offset:4096\n"
  "ds_read_b64_tr_b16 a[20:21], v11 offset:8192\n"
  "ds_read_b64_tr_b16 a[22:23], v11 offset:12288\n"
  "ds_read_b64_tr_b16 a[24:25], v11 offset:32\n"
  "ds_read_b64_tr_b16 a[26:27], v11 offset:4128\n"
  "ds_read_b64_tr_b16 a[28:29], v11 offset:8224\n"
  "ds_read_b64_tr_b16 a[30:31], v11 offset:12320\n"
  "s_waitcnt vmcnt(20) lgkmcnt(0)\n"
  "s_barrier\n"
  "s_add_u32 m0, s68, 114688\n"
  "s_mov_b32 s74, 3670016\n"
  "buffer_load_dwordx4 v12, s[44:47], s74 offen lds\n"
  "s_add_u32 m0, s68, 118784\n"
  "s_mov_b32 s74, 3686400\n"
  "buffer_load_dwordx4 v12, s[44:47], s74 offen lds\n"
  "s_add_u32 m0, s68, 122880\n"
  "s_mov_b32 s74, 3932160\n"
  "buffer_load_dwordx4 v12, s[44:47], s74 offen lds\n"
  "s_add_u32 m0, s68, 126976\n"
  "s_mov_b32 s74, 3948544\n"
  "buffer_load_dwordx4 v12, s[44:47], s74 offen lds\n"
  "v_add_u32 v11, 16384, v15\n"
  "ds_read_b64_tr_b16 a[64:65], v11 offset:0\n"
  "ds_read_b64_tr_b16 a[66:67], v11 offset:4096\n"
  "ds_read_b64_tr_b16 a[68:69], v11 offset:8192\n"
  "ds_read_b64_tr_b16 a[70:71], v11 offset:12288\n"
  "ds_read_b64_tr_b16 a[72:73], v11 offset:32\n"
  "ds_read_b64_tr_b16 a[74:75], v11 offset:4128\n"
  "ds_read_b64_tr_b16 a[76:77], v11 offset:8224\n"
  "ds_read_b64_tr_b16 a[78:79], v11 offset:12320\n"
  "s_waitcnt vmcnt(20) lgkmcnt(0)\n"
  "s_barrier\n"
  "s_add_u32 m0, s68, 131072\n"
  "s_mov_b32 s74, 4194304\n"
  "buffer_load_dwordx4 v12, s[44:47], s74 offen lds\n"
  "s_add_u32 m0, s68, 135168\n"
  "s_mov_b32 s74, 4210688\n"
  "buffer_load_dwordx4 v12, s[44:47], s74 offen lds\n"
  "s_add_u32 m0, s68, 139264\n"
  "s_mov_b32 s74, 4456448\n"
  "buffer_load_dwordx4 v12, s[44:47], s74 offen lds\n"
  "s_add_u32 m0, s68, 143360\n"
  "s_mov_b32 s74, 4472832\n"
  "buffer_load_dwordx4 v12, s[44:47], s74 offen lds\n"
  "v_add_u32 v11, 32768, v15\n"
  "ds_read_b64_tr_b16 a[112:113], v11 offset:0\n"
  "ds_read_b64_tr_b16 a[114:115], v11 offset:4096\n"
  "ds_read_b64_tr_b16 a[116:117], v11 offset:8192\n"
  "ds_read_b64_tr_b16 a[118:119], v11 offset:12288\n"
  "ds_read_b64_tr_b16 a[120:121], v11 offset:32\n"
  "ds_read_b64_tr_b16 a[122:123], v11 offset:4128\n"
  "ds_read_b64_tr_b16 a[124:125], v11 offset:8224\n"
  "ds_read_b64_tr_b16 a[126:127], v11 offset:12320\n"
  "s_waitcnt vmcnt(20) lgkmcnt(0)\n"
  "s_barrier\n"
  "s_add_u32 m0, s68, 0\n"
  "s_mov_b32 s74, 0\n"
  "buffer_load_dwordx4 v12, s[40:43], s74 offen lds\n"
  "s_add_u32 m0, s68, 4096\n"
  "s_mov_b32 s74, 16384\n"
  "buffer_load_dwordx4 v12, s[40:43], s74 offen lds\n"
  "s_add_u32 m0, s68, 8192\n"
  "s_mov_b32 s74, 262144\n"
  "buffer_load_dwordx4 v12, s[40:43], s74 offen lds\n"
  "s_add_u32 m0, s68, 12288\n"
  "s_mov_b32 s74, 278528\n"
  "buffer_load_dwordx4 v12, s[40:43], s74 offen lds\n"
  "v_add_u32 v11, 49152, v15\n"
  "ds_read_b64_tr_b16 a[160:161], v11 offset:0\n"
  "ds_read_b64_tr_b16 a[162:163], v11 offset:4096\n"
  "ds_read_b64_tr_b16 a[164:165], v11 offset:8192\n"
  "ds_read_b64_tr_b16 a[166:167], v11 offset:12288\n"
  "ds_read_b64_tr_b16 a[168:169], v11 offset:32\n"
  "ds_read_b64_tr_b16 a[170:171], v11 offset:4128\n"
  "ds_read_b64_tr_b16 a[172:173], v11 offset:8224\n"
  "ds_read_b64_tr_b16 a[174:175], v11 offset:12320\n"
  "s_waitcnt vmcnt(20) lgkmcnt(0)\n"
  "s_barrier\n"
  "s_add_u32 m0, s68, 16384\n"
  "s_mov_b32 s74, 524288\n"
  "buffer_load_dwordx4 v12, s[40:43], s74 offen lds\n"
  "s_add_u32 m0, s68, 20480\n"
  "s_mov_b32 s74, 540672\n"
  "buffer_load_dwordx4 v12, s[40:43], s74 offen lds\n"
  "s_add_u32 m0, s68, 24576\n"
  "s_mov_b32 s74, 786432\n"
  "buffer_load_dwordx4 v12, s[40:43], s74 offen lds\n"
  "s_add_u32 m0, s68, 28672\n"
  "s_mov_b32 s74, 802816\n"
  "buffer_load_dwordx4 v12, s[40:43], s74 offen lds\n"
  "v_add_u32 v11, 65536, v15\n"
  "ds_read_b64_tr_b16 a[208:209], v11 offset:0\n"
  "ds_read_b64_tr_b16 a[210:211], v11 offset:4096\n"
  "ds_read_b64_tr_b16 a[212:213], v11 offset:8192\n"
  "ds_read_b64_tr_b16 a[214:215], v11 offset:12288\n"
  "ds_read_b64_tr_b16 a[216:217], v11 offset:32\n"
  "ds_read_b64_tr_b16 a[218:219], v11 offset:4128\n"
  "ds_read_b64_tr_b16 a[220:221], v11 offset:8224\n"
  "ds_read_b64_tr_b16 a[222:223], v11 offset:12320\n"
  "s_waitcnt vmcnt(20) lgkmcnt(0)\n"
  "s_barrier\n"
  "s_add_u32 m0, s68, 32768\n"
  "s_mov_b32 s74, 1048576\n"
  "buffer_load_dwordx4 v12, s[40:43], s74 offen lds\n"
  "s_add_u32 m0, s68, 36864\n"
  "s_mov_b32 s74, 1064960\n"
  "buffer_load_dwordx4 v12, s[40:43], s74 offen lds\n"
  "s_add_u32 m0, s68, 40960\n"
  "s_mov_b32 s74, 1310720\n"
  "buffer_load_dwordx4 v12, s[40:43], s74 offen lds\n"
  "s_add_u32 m0, s68, 45056\n"
  "s_mov_b32 s74, 1327104\n"
  "buffer_load_dwordx4 v12, s[40:43], s74 offen lds\n"
  "v_add_u32 v11, 81920, v15\n"
  "ds_read_b64_tr_b16 v[80:81], v11 offset:0\n"
  "ds_read_b64_tr_b16 v[82:83], v11 offset:4096\n"
  "ds_read_b64_tr_b16 v[84:85], v11 offset:8192\n"
  "ds_read_b64_tr_b16 v[86:87], v11 offset:12288\n"
  "ds_read_b64_tr_b16 v[88:89], v11 offset:32\n"
  "ds_read_b64_tr_b16 v[90:91], v11 offset:4128\n"
  "ds_read_b64_tr_b16 v[92:93], v11 offset:8224\n"
  "ds_read_b64_tr_b16 v[94:95], v11 offset:12320\n"
  "s_waitcnt vmcnt(20) lgkmcnt(0)\n"
  "s_barrier\n"
  "s_add_u32 m0, s68, 49152\n"
  "s_mov_b32 s74, 1572864\n"
  "buffer_load_dwordx4 v12, s[40:43], s74 offen lds\n"
  "s_add_u32 m0, s68, 53248\n"
  "s_mov_b32 s74, 1589248\n"
  "buffer_load_dwordx4 v12, s[40:43], s74 offen lds\n"
  "s_add_u32 m0, s68, 57344\n"
  "s_mov_b32 s74, 1835008\n"
  "buffer_load_dwordx4 v12, s[40:43], s74 offen lds\n"
  "s_add_u32 m0, s68, 61440\n"
  "s_mov_b32 s74, 1851392\n"
  "buffer_load_dwordx4 v12, s[40:43], s74 offen lds\n"
  "v_add_u32 v11, 98304, v15\n"
  "ds_read_b64_tr_b16 v[128:129], v11 offset:0\n"
  "ds_read_b64_tr_b16 v[130:131], v11 offset:4096\n"
  "ds_read_b64_tr_b16 v[132:133], v11 offset:8192\n"
  "ds_read_b64_tr_b16 v[134:135], v11 offset:12288\n"
  "ds_read_b64_tr_b16 v[136:137], v11 offset:32\n"
  "ds_read_b64_tr_b16 v[138:139], v11 offset:4128\n"
  "ds_read_b64_tr_b16 v[140:141], v11 offset:8224\n"
  "ds_read_b64_tr_b16 v[142:143], v11 offset:12320\n"
  "s_waitcnt vmcnt(20) lgkmcnt(0)\n"
  "s_barrier\n"
  "s_add_u32 m0, s68, 65536\n"
  "s_mov_b32 s74, 2097152\n"
  "buffer_load_dwordx4 v12, s[40:43], s74 offen lds\n"
  "s_add_u32 m0, s68, 69632\n"
  "s_mov_b32 s74, 2113536\n"
  "buffer_load_dwordx4 v12, s[40:43], s74 offen lds\n"
  "s_add_u32 m0, s68, 73728\n"
  "s_mov_b32 s74, 2359296\n"
  "buffer_load_dwordx4 v12, s[40:43], s74 offen lds\n"
  "s_add_u32 m0, s68, 77824\n"
  "s_mov_b32 s74, 2375680\n"
  "buffer_load_dwordx4 v12, s[40:43], s74 offen lds\n"
  "v_add_u32 v11, 114688, v15\n"
  "ds_read_b64_tr_b16 v[176:177], v11 offset:0\n"
  "ds_read_b64_tr_b16 v[178:179], v11 offset:4096\n"
  "ds_read_b64_tr_b16 v[180:181], v11 offset:8192\n"
  "ds_read_b64_tr_b16 v[182:183], v11 offset:12288\n"
  "ds_read_b64_tr_b16 v[184:185], v11 offset:32\n"
  "ds_read_b64_tr_b16 v[186:187], v11 offset:4128\n"
  "ds_read_b64_tr_b16 v[188:189], v11 offset:8224\n"
  "ds_read_b64_tr_b16 v[190:191], v11 offset:12320\n"
  "s_waitcnt vmcnt(20) lgkmcnt(0)\n"
  "s_barrier\n"
  "s_add_u32 m0, s68, 81920\n"
  "s_mov_b32 s74, 2621440\n"
  "buffer_load_dwordx4 v12, s[40:43], s74 offen lds\n"
  "s_add_u32 m0, s68, 86016\n"
  "s_mov_b32 s74, 2637824\n"
  "buffer_load_dwordx4 v12, s[40:43], s74 offen lds\n"
  "s_add_u32 m0, s68, 90112\n"
  "s_mov_b32 s74, 2883584\n"
  "buffer_load_dwordx4 v12, s[40:43], s74 offen lds\n"
  "s_add_u32 m0, s68, 94208\n"
  "s_mov_b32 s74, 2899968\n"
  "buffer_load_dwordx4 v12, s[40:43], s74 offen lds\n"
  "v_add_u32 v11, 131072, v15\n"
  "ds_read_b64_tr_b16 v[224:225], v11 offset:0\n"
  "ds_read_b64_tr_b16 v[226:227], v11 offset:4096\n"
  "ds_read_b64_tr_b16 v[228:229], v11 offset:8192\n"
  "ds_read_b64_tr_b16 v[230:231], v11 offset:12288\n"
  "ds_read_b64_tr_b16 v[232:233], v11 offset:32\n"
  "ds_read_b64_tr_b16 v[234:235], v11 offset:4128\n"
  "ds_read_b64_tr_b16 v[236:237], v11 offset:8224\n"
  "ds_read_b64_tr_b16 v[238:239], v11 offset:12320\n"
  "s_waitcnt vmcnt(20) lgkmcnt(0)\n"
  "s_barrier\n"
  "s_add_u32 m0, s68, 98304\n"
  "s_mov_b32 s74, 3145728\n"
  "buffer_load_dwordx4 v12, s[40:43], s74 offen lds\n"
  "s_add_u32 m0, s68, 102400\n"
  "s_mov_b32 s74, 3162112\n"
  "buffer_load_dwordx4 v12, s[40:43], s74 offen lds\n"
  "s_add_u32 m0, s68, 106496\n"
  "s_mov_b32 s74, 3407872\n"
  "buffer_load_dwordx4 v12, s[40:43], s74 offen lds\n"
  "s_add_u32 m0, s68, 110592\n"
  "s_mov_b32 s74, 3424256\n"
  "buffer_load_dwordx4 v12, s[40:43], s74 offen lds\n"
  "v_add_u32 v11, 0, v15\n"
  "ds_read_b64_tr_b16 a[0:1], v11 offset:0\n"
  "ds_read_b64_tr_b16 a[2:3], v11 offset:4096\n"
  "ds_read_b64_tr_b16 a[4:5], v11 offset:8192\n"
  "ds_read_b64_tr_b16 a[6:7], v11 offset:12288\n"
  "ds_read_b64_tr_b16 a[8:9], v11 offset:32\n"
  "ds_read_b64_tr_b16 a[10:11], v11 offset:4128\n"
  "ds_read_b64_tr_b16 a[12:13], v11 offset:8224\n"
  "ds_read_b64_tr_b16 a[14:15], v11 offset:12320\n"
  "s_waitcnt vmcnt(20) lgkmcnt(0)\n"
  "s_barrier\n"
  "s_add_u32 m0, s68, 114688\n"
  "s_mov_b32 s74, 3670016\n"
  "buffer_load_dwordx4 v12, s[40:43], s74 offen lds\n"
  "s_add_u32 m0, s68, 118784\n"
  "s_mov_b32 s74, 3686400\n"
  "buffer_load_dwordx4 v12, s[40:43], s74 offen lds\n"
  "s_add_u32 m0, s68, 122880\n"
  "s_mov_b32 s74, 3932160\n"
  "buffer_load_dwordx4 v12, s[40:43], s74 offen lds\n"
  "s_add_u32 m0, s68, 126976\n"
  "s_mov_b32 s74, 3948544\n"
  "buffer_load_dwordx4 v12, s[40:43], s74 offen lds\n"
  "v_add_u32 v11, 16384, v15\n"
  "ds_read_b64_tr_b16 a[48:49], v11 offset:0\n"
  "ds_read_b64_tr_b16 a[50:51], v11 offset:4096\n"
  "ds_read_b64_tr_b16 a[52:53], v11 offset:8192\n"
  "ds_read_b64_tr_b16 a[54:55], v11 offset:12288\n"
  "ds_read_b64_tr_b16 a[56:57], v11 offset:32\n"
  "ds_read_b64_tr_b16 a[58:59], v11 offset:4128\n"
  "ds_read_b64_tr_b16 a[60:61], v11 offset:8224\n"
  "ds_read_b64_tr_b16 a[62:63], v11 offset:12320\n"
  "s_waitcnt vmcnt(20) lgkmcnt(0)\n"
  "s_barrier\n"
  "s_add_u32 m0, s68, 131072\n"
  "s_mov_b32 s74, 4194304\n"
  "buffer_load_dwordx4 v12, s[40:43], s74 offen lds\n"
  "s_add_u32 m0, s68, 135168\n"
  "s_mov_b32 s74, 4210688\n"
  "buffer_load_dwordx4 v12, s[40:43], s74 offen lds\n"
  "s_add_u32 m0, s68, 139264\n"
  "s_mov_b32 s74, 4456448\n"
  "buffer_load_dwordx4 v12, s[40:43], s74 offen lds\n"
  "s_add_u32 m0, s68, 143360\n"
  "s_mov_b32 s74, 4472832\n"
  "buffer_load_dwordx4 v12, s[40:43], s74 offen lds\n"
  "v_add_u32 v11, 32768, v15\n"
  "ds_read_b64_tr_b16 a[96:97], v11 offset:0\n"
  "ds_read_b64_tr_b16 a[98:99], v11 offset:4096\n"
  "ds_read_b64_tr_b16 a[100:101], v11 offset:8192\n"
  "ds_read_b64_tr_b16 a[102:103], v11 offset:12288\n"
  "ds_read_b64_tr_b16 a[104:105], v11 offset:32\n"
  "ds_read_b64_tr_b16 a[106:107], v11 offset:4128\n"
  "ds_read_b64_tr_b16 a[108:109], v11 offset:8224\n"
  "ds_read_b64_tr_b16 a[110:111], v11 offset:12320\n"
  "s_waitcnt vmcnt(20) lgkmcnt(0)\n"
  "s_barrier\n"
  "s_add_u32 m0, s68, 0\n"
  "s_mov_b32 s74, 0\n"
  "buffer_load_dwordx4 v12, s[48:51], s74 offen lds\n"
  "s_add_u32 m0, s68, 4096\n"
  "s_mov_b32 s74, 16384\n"
  "buffer_load_dwordx4 v12, s[48:51], s74 offen lds\n"
  "s_add_u32 m0, s68, 8192\n"
  "s_mov_b32 s74, 262144\n"
  "buffer_load_dwordx4 v12, s[48:51], s74 offen lds\n"
  "s_add_u32 m0, s68, 12288\n"
  "s_mov_b32 s74, 278528\n"
  "buffer_load_dwordx4 v12, s[48:51], s74 offen lds\n"
  "v_add_u32 v11, 49152, v15\n"
  "ds_read_b64_tr_b16 a[144:145], v11 offset:0\n"
  "ds_read_b64_tr_b16 a[146:147], v11 offset:4096\n"
  "ds_read_b64_tr_b16 a[148:149], v11 offset:8192\n"
  "ds_read_b64_tr_b16 a[150:151], v11 offset:12288\n"
  "ds_read_b64_tr_b16 a[152:153], v11 offset:32\n"
  "ds_read_b64_tr_b16 a[154:155], v11 offset:4128\n"
  "ds_read_b64_tr_b16 a[156:157], v11 offset:8224\n"
  "ds_read_b64_tr_b16 a[158:159], v11 offset:12320\n"
  "s_waitcnt vmcnt(20) lgkmcnt(0)\n"
  "s_barrier\n"
  "s_add_u32 m0, s68, 16384\n"
  "s_mov_b32 s74, 524288\n"
  "buffer_load_dwordx4 v12, s[48:51], s74 offen lds\n"
  "s_add_u32 m0, s68, 20480\n"
  "s_mov_b32 s74, 540672\n"
  "buffer_load_dwordx4 v12, s[48:51], s74 offen lds\n"
  "s_add_u32 m0, s68, 24576\n"
  "s_mov_b32 s74, 786432\n"
  "buffer_load_dwordx4 v12, s[48:51], s74 offen lds\n"
  "s_add_u32 m0, s68, 28672\n"
  "s_mov_b32 s74, 802816\n"
  "buffer_load_dwordx4 v12, s[48:51], s74 offen lds\n"
  "v_add_u32 v11, 65536, v15\n"
  "ds_read_b64_tr_b16 a[192:193], v11 offset:0\n"
  "ds_read_b64_tr_b16 a[194:195], v11 offset:4096\n"
  "ds_read_b64_tr_b16 a[196:197], v11 offset:8192\n"
  "ds_read_b64_tr_b16 a[198:199], v11 offset:12288\n"
  "ds_read_b64_tr_b16 a[200:201], v11 offset:32\n"
  "ds_read_b64_tr_b16 a[202:203], v11 offset:4128\n"
  "ds_read_b64_tr_b16 a[204:205], v11 offset:8224\n"
  "ds_read_b64_tr_b16 a[206:207], v11 offset:12320\n"
  "s_waitcnt vmcnt(20) lgkmcnt(0)\n"
  "s_barrier\n"
  "s_add_u32 m0, s68, 32768\n"
  "s_mov_b32 s74, 1048576\n"
  "buffer_load_dwordx4 v12, s[48:51], s74 offen lds\n"
  "s_add_u32 m0, s68, 36864\n"
  "s_mov_b32 s74, 1064960\n"
  "buffer_load_dwordx4 v12, s[48:51], s74 offen lds\n"
  "s_add_u32 m0, s68, 40960\n"
  "s_mov_b32 s74, 1310720\n"
  "buffer_load_dwordx4 v12, s[48:51], s74 offen lds\n"
  "s_add_u32 m0, s68, 45056\n"
  "s_mov_b32 s74, 1327104\n"
  "buffer_load_dwordx4 v12, s[48:51], s74 offen lds\n"
  "v_add_u32 v11, 81920, v15\n"
  "ds_read_b64_tr_b16 a[240:241], v11 offset:0\n"
  "ds_read_b64_tr_b16 a[242:243], v11 offset:4096\n"
  "ds_read_b64_tr_b16 a[244:245], v11 offset:8192\n"
  "ds_read_b64_tr_b16 a[246:247], v11 offset:12288\n"
  "ds_read_b64_tr_b16 a[248:249], v11 offset:32\n"
  "ds_read_b64_tr_b16 a[250:251], v11 offset:4128\n"
  "ds_read_b64_tr_b16 a[252:253], v11 offset:8224\n"
  "ds_read_b64_tr_b16 a[254:255], v11 offset:12320\n"
  "s_waitcnt vmcnt(20) lgkmcnt(0)\n"
  "s_barrier\n"
  "s_add_u32 m0, s68, 49152\n"
  "s_mov_b32 s74, 1572864\n"
  "buffer_load_dwordx4 v12, s[48:51], s74 offen lds\n"
  "s_add_u32 m0, s68, 53248\n"
  "s_mov_b32 s74, 1589248\n"
  "buffer_load_dwordx4 v12, s[48:51], s74 offen lds\n"
  "s_add_u32 m0, s68, 57344\n"
  "s_mov_b32 s74, 1835008\n"
  "buffer_load_dwordx4 v12, s[48:51], s74 offen lds\n"
  "s_add_u32 m0, s68, 61440\n"
  "s_mov_b32 s74, 1851392\n"
  "buffer_load_dwordx4 v12, s[48:51], s74 offen lds\n"
  "v_add_u32 v11, 98304, v15\n"
  "ds_read_b64_tr_b16 v[112:113], v11 offset:0\n"
  "ds_read_b64_tr_b16 v[114:115], v11 offset:4096\n"
  "ds_read_b64_tr_b16 v[116:117], v11 offset:8192\n"
  "ds_read_b64_tr_b16 v[118:119], v11 offset:12288\n"
  "ds_read_b64_tr_b16 v[120:121], v11 offset:32\n"
  "ds_read_b64_tr_b16 v[122:123], v11 offset:4128\n"
  "ds_read_b64_tr_b16 v[124:125], v11 offset:8224\n"
  "ds_read_b64_tr_b16 v[126:127], v11 offset:12320\n"
  "s_waitcnt vmcnt(20) lgkmcnt(0)\n"
  "s_barrier\n"
  "s_add_u32 m0, s68, 65536\n"
  "s_mov_b32 s74, 2097152\n"
  "buffer_load_dwordx4 v12, s[48:51], s74 offen lds\n"
  "s_add_u32 m0, s68, 69632\n"
  "s_mov_b32 s74, 2113536\n"
  "buffer_load_dwordx4 v12, s[48:51], s74 offen lds\n"
  "s_add_u32 m0, s68, 73728\n"
  "s_mov_b32 s74, 2359296\n"
  "buffer_load_dwordx4 v12, s[48:51], s74 offen lds\n"
  "s_add_u32 m0, s68, 77824\n"
  "s_mov_b32 s74, 2375680\n"
  "buffer_load_dwordx4 v12, s[48:51], s74 offen lds\n"
  "v_add_u32 v11, 114688, v15\n"
  "ds_read_b64_tr_b16 v[160:161], v11 offset:0\n"
  "ds_read_b64_tr_b16 v[162:163], v11 offset:4096\n"
  "ds_read_b64_tr_b16 v[164:165], v11 offset:8192\n"
  "ds_read_b64_tr_b16 v[166:167], v11 offset:12288\n"
  "ds_read_b64_tr_b16 v[168:169], v11 offset:32\n"
  "ds_read_b64_tr_b16 v[170:171], v11 offset:4128\n"
  "ds_read_b64_tr_b16 v[172:173], v11 offset:8224\n"
  "ds_read_b64_tr_b16 v[174:175], v11 offset:12320\n"
  "s_waitcnt vmcnt(20) lgkmcnt(0)\n"
  "s_barrier\n"
  "s_add_u32 m0, s68, 81920\n"
  "s_mov_b32 s74, 2621440\n"
  "buffer_load_dwordx4 v12, s[48:51], s74 offen lds\n"
  "s_add_u32 m0, s68, 86016\n"
  "s_mov_b32 s74, 2637824\n"
  "buffer_load_dwordx4 v12, s[48:51], s74 offen lds\n"
  "s_add_u32 m0, s68, 90112\n"
  "s_mov_b32 s74, 2883584\n"
  "buffer_load_dwordx4 v12, s[48:51], s74 offen lds\n"
  "s_add_u32 m0, s68, 94208\n"
  "s_mov_b32 s74, 2899968\n"
  "buffer_load_dwordx4 v12, s[48:51], s74 offen lds\n"
  "v_add_u32 v11, 131072, v15\n"
  "ds_read_b64_tr_b16 v[208:209], v11 offset:0\n"
  "ds_read_b64_tr_b16 v[210:211], v11 offset:4096\n"
  "ds_read_b64_tr_b16 v[212:213], v11 offset:8192\n"
  "ds_read_b64_tr_b16 v[214:215], v11 offset:12288\n"
  "ds_read_b64_tr_b16 v[216:217], v11 offset:32\n"
  "ds_read_b64_tr_b16 v[218:219], v11 offset:4128\n"
  "ds_read_b64_tr_b16 v[220:221], v11 offset:8224\n"
  "ds_read_b64_tr_b16 v[222:223], v11 offset:12320\n"
  "s_waitcnt vmcnt(20) lgkmcnt(0)\n"
  "s_barrier\n"
  "s_add_u32 m0, s68, 98304\n"
  "s_mov_b32 s74, 3145728\n"
  "buffer_load_dwordx4 v12, s[48:51], s74 offen lds\n"
  "s_add_u32 m0, s68, 102400\n"
  "s_mov_b32 s74, 3162112\n"
  "buffer_load_dwordx4 v12, s[48:51], s74 offen lds\n"
  "s_add_u32 m0, s68, 106496\n"
  "s_mov_b32 s74, 3407872\n"
  "buffer_load_dwordx4 v12, s[48:51], s74 offen lds\n"
  "s_add_u32 m0, s68, 110592\n"
  "s_mov_b32 s74, 3424256\n"
  "buffer_load_dwordx4 v12, s[48:51], s74 offen lds\n"
  "v_add_u32 v11, 0, v16\n"
  "ds_read_b64_tr_b16 a[32:33], v11 offset:0\n"
  "ds_read_b64_tr_b16 a[34:35], v11 offset:4096\n"
  "ds_read_b64_tr_b16 a[36:37], v11 offset:8192\n"
  "ds_read_b64_tr_b16 a[38:39], v11 offset:12288\n"
  "ds_read_b64_tr_b16 a[40:41], v11 offset:8\n"
  "ds_read_b64_tr_b16 a[42:43], v11 offset:4104\n"
  "ds_read_b64_tr_b16 a[44:45], v11 offset:8200\n"
  "ds_read_b64_tr_b16 a[46:47], v11 offset:12296\n"
  "s_waitcnt vmcnt(20) lgkmcnt(0)\n"
  "s_barrier\n"
  "s_add_u32 m0, s68, 114688\n"
  "s_mov_b32 s74, 3670016\n"
  "buffer_load_dwordx4 v12, s[48:51], s74 offen lds\n"
  "s_add_u32 m0, s68, 118784\n"
  "s_mov_b32 s74, 3686400\n"
  "buffer_load_dwordx4 v12, s[48:51], s74 offen lds\n"
  "s_add_u32 m0, s68, 122880\n"
  "s_mov_b32 s74, 3932160\n"
  "buffer_load_dwordx4 v12, s[48:51], s74 offen lds\n"
  "s_add_u32 m0, s68, 126976\n"
  "s_mov_b32 s74, 3948544\n"
  "buffer_load_dwordx4 v12, s[48:51], s74 offen lds\n"
  "v_add_u32 v11, 16384, v16\n"
  "ds_read_b64_tr_b16 a[80:81], v11 offset:0\n"
  "ds_read_b64_tr_b16 a[82:83], v11 offset:4096\n"
  "ds_read_b64_tr_b16 a[84:85], v11 offset:8192\n"
  "ds_read_b64_tr_b16 a[86:87], v11 offset:12288\n"
  "ds_read_b64_tr_b16 a[88:89], v11 offset:8\n"
  "ds_read_b64_tr_b16 a[90:91], v11 offset:4104\n"
  "ds_read_b64_tr_b16 a[92:93], v11 offset:8200\n"
  "ds_read_b64_tr_b16 a[94:95], v11 offset:12296\n"
  "s_waitcnt vmcnt(20) lgkmcnt(0)\n"
  "s_barrier\n"
  "s_add_u32 m0, s68, 131072\n"
  "s_mov_b32 s74, 4194304\n"
  "buffer_load_dwordx4 v12, s[48:51], s74 offen lds\n"
  "s_add_u32 m0, s68, 135168\n"
  "s_mov_b32 s74, 4210688\n"
  "buffer_load_dwordx4 v12, s[48:51], s74 offen lds\n"
  "s_add_u32 m0, s68, 139264\n"
  "s_mov_b32 s74, 4456448\n"
  "buffer_load_dwordx4 v12, s[48:51], s74 offen lds\n"
  "s_add_u32 m0, s68, 143360\n"
  "s_mov_b32 s74, 4472832\n"
  "buffer_load_dwordx4 v12, s[48:51], s74 offen lds\n"
  "v_add_u32 v11, 32768, v16\n"
  "ds_read_b64_tr_b16 a[128:129], v11 offset:0\n"
  "ds_read_b64_tr_b16 a[130:131], v11 offset:4096\n"
  "ds_read_b64_tr_b16 a[132:133], v11 offset:8192\n"
  "ds_read_b64_tr_b16 a[134:135], v11 offset:12288\n"
  "ds_read_b64_tr_b16 a[136:137], v11 offset:8\n"
  "ds_read_b64_tr_b16 a[138:139], v11 offset:4104\n"
  "ds_read_b64_tr_b16 a[140:141], v11 offset:8200\n"
  "ds_read_b64_tr_b16 a[142:143], v11 offset:12296\n"
  "s_waitcnt vmcnt(20) lgkmcnt(0)\n"
  "s_barrier\n"
  "v_add_u32 v11, 49152, v16\n"
  "ds_read_b64_tr_b16 a[176:177], v11 offset:0\n"
  "ds_read_b64_tr_b16 a[178:179], v11 offset:4096\n"
  "ds_read_b64_tr_b16 a[180:181], v11 offset:8192\n"
  "ds_read_b64_tr_b16 a[182:183], v11 offset:12288\n"
  "ds_read_b64_tr_b16 a[184:185], v11 offset:8\n"
  "ds_read_b64_tr_b16 a[186:187], v11 offset:4104\n"
  "ds_read_b64_tr_b16 a[188:189], v11 offset:8200\n"
  "ds_read_b64_tr_b16 a[190:191], v11 offset:12296\n"
  "s_waitcnt vmcnt(16) lgkmcnt(0)\n"
  "s_barrier\n"
  "v_add_u32 v11, 65536, v16\n"
  "ds_read_b64_tr_b16 a[224:225], v11 offset:0\n"
  "ds_read_b64_tr_b16 a[226:227], v11 offset:4096\n"
  "ds_read_b64_tr_b16 a[228:229], v11 offset:8192\n"
  "ds_read_b64_tr_b16 a[230:231], v11 offset:12288\n"
  "ds_read_b64_tr_b16 a[232:233], v11 offset:8\n"
  "ds_read_b64_tr_b16 a[234:235], v11 offset:4104\n"
  "ds_read_b64_tr_b16 a[236:237], v11 offset:8200\n"
  "ds_read_b64_tr_b16 a[238:239], v11 offset:12296\n"
  "s_waitcnt vmcnt(12) lgkmcnt(0)\n"
  "s_barrier\n"
  "v_add_u32 v11, 81920, v16\n"
  "ds_read_b64_tr_b16 v[96:97], v11 offset:0\n"
  "ds_read_b64_tr_b16 v[98:99], v11 offset:4096\n"
  "ds_read_b64_tr_b16 v[100:101], v11 offset:8192\n"
  "ds_read_b64_tr_b16 v[102:103], v11 offset:12288\n"
  "ds_read_b64_tr_b16 v[104:105], v11 offset:8\n"
  "ds_read_b64_tr_b16 v[106:107], v11 offset:4104\n"
  "ds_read_b64_tr_b16 v[108:109], v11 offset:8200\n"
  "ds_read_b64_tr_b16 v[110:111], v11 offset:12296\n"
  "s_waitcnt vmcnt(8) lgkmcnt(0)\n"
  "s_barrier\n"
  "v_add_u32 v11, 98304, v16\n"
  "ds_read_b64_tr_b16 v[144:145], v11 offset:0\n"
  "ds_read_b64_tr_b16 v[146:147], v11 offset:4096\n"
  "ds_read_b64_tr_b16 v[148:149], v11 offset:8192\n"
  "ds_read_b64_tr_b16 v[150:151], v11 offset:12288\n"
  "ds_read_b64_tr_b16 v[152:153], v11 offset:8\n"
  "ds_read_b64_tr_b16 v[154:155], v11 offset:4104\n"
  "ds_read_b64_tr_b16 v[156:157], v11 offset:8200\n"
  "ds_read_b64_tr_b16 v[158:159], v11 offset:12296\n"
  "s_waitcnt vmcnt(4) lgkmcnt(0)\n"
  "s_barrier\n"
  "v_add_u32 v11, 114688, v16\n"
  "ds_read_b64_tr_b16 v[192:193], v11 offset:0\n"
  "ds_read_b64_tr_b16 v[194:195], v11 offset:4096\n"
  "ds_read_b64_tr_b16 v[196:197], v11 offset:8192\n"
  "ds_read_b64_tr_b16 v[198:199], v11 offset:12288\n"
  "ds_read_b64_tr_b16 v[200:201], v11 offset:8\n"
  "ds_read_b64_tr_b16 v[202:203], v11 offset:4104\n"
  "ds_read_b64_tr_b16 v[204:205], v11 offset:8200\n"
  "ds_read_b64_tr_b16 v[206:207], v11 offset:12296\n"
  "s_waitcnt vmcnt(0) lgkmcnt(0)\n"
  "s_barrier\n"
  "v_add_u32 v11, 131072, v16\n"
  "ds_read_b64_tr_b16 v[240:241], v11 offset:0\n"
  "ds_read_b64_tr_b16 v[242:243], v11 offset:4096\n"
  "ds_read_b64_tr_b16 v[244:245], v11 offset:8192\n"
  "ds_read_b64_tr_b16 v[246:247], v11 offset:12288\n"
  "ds_read_b64_tr_b16 v[248:249], v11 offset:8\n"
  "ds_read_b64_tr_b16 v[250:251], v11 offset:4104\n"
  "ds_read_b64_tr_b16 v[252:253], v11 offset:8200\n"
  "ds_read_b64_tr_b16 v[254:255], v11 offset:12296\n"
  "s_waitcnt lgkmcnt(0)\n"
  "s_barrier\n"
  "s_add_u32 m0, s56, 0\n"
  "s_mov_b32 s74, 0\n"
  "buffer_load_dwordx4 v13, s[52:55], s74 offen lds\n"
  "s_add_u32 m0, s56, 1024\n"
  "s_mov_b32 s74, 18432\n"
  "buffer_load_dwordx4 v13, s[52:55], s74 offen lds\n"
  "s_add_u32 m0, s56, 2048\n"
  "s_mov_b32 s74, 2304\n"
  "buffer_load_dwordx4 v13, s[52:55], s74 offen lds\n"
  "s_add_u32 m0, s56, 3072\n"
  "s_mov_b32 s74, 20736\n"
  "buffer_load_dwordx4 v13, s[52:55], s74 offen lds\n"
  "s_add_u32 m0, s56, 4096\n"
  "s_mov_b32 s74, 36864\n"
  "buffer_load_dwordx4 v13, s[52:55], s74 offen lds\n"
  "s_add_u32 m0, s56, 5120\n"
  "s_mov_b32 s74, 55296\n"
  "buffer_load_dwordx4 v13, s[52:55], s74 offen lds\n"
  "s_add_u32 m0, s56, 6144\n"
  "s_mov_b32 s74, 39168\n"
  "buffer_load_dwordx4 v13, s[52:55], s74 offen lds\n"
  "s_add_u32 m0, s56, 7168\n"
  "s_mov_b32 s74, 57600\n"
  "buffer_load_dwordx4 v13, s[52:55], s74 offen lds\n"
  "s_add_u32 m0, s56, 8192\n"
  "s_mov_b32 s74, 73728\n"
  "buffer_load_dwordx4 v13, s[52:55], s74 offen lds\n"
  "s_add_u32 m0, s56, 9216\n"
  "s_mov_b32 s74, 92160\n"
  "buffer_load_dwordx4 v13, s[52:55], s74 offen lds\n"
  "s_add_u32 m0, s56, 10240\n"
  "s_mov_b32 s74, 76032\n"
  "buffer_load_dwordx4 v13, s[52:55], s74 offen lds\n"
  "s_add_u32 m0, s56, 11264\n"
  "s_mov_b32 s74, 94464\n"
  "buffer_load_dwordx4 v13, s[52:55], s74 offen lds\n"
  "s_add_u32 m0, s56, 12288\n"
  "s_mov_b32 s74, 110592\n"
  "buffer_load_dwordx4 v13, s[52:55], s74 offen lds\n"
  "s_add_u32 m0, s56, 13312\n"
  "s_mov_b32 s74, 129024\n"
  "buffer_load_dwordx4 v13, s[52:55], s74 offen lds\n"
  "s_add_u32 m0, s56, 14336\n"
  "s_mov_b32 s74, 112896\n"
  "buffer_load_dwordx4 v13, s[52:55], s74 offen lds\n"
  "s_add_u32 m0, s56, 15360\n"
  "s_mov_b32 s74, 131328\n"
  "buffer_load_dwordx4 v13, s[52:55], s74 offen lds\n"
  "s_waitcnt vmcnt(8)\n"
  "ds_read_b64_tr_b16 v[20:21], v17 offset:0\n"
  "ds_read_b64_tr_b16 v[22:23], v17 offset:2048\n"
  "ds_read_b64_tr_b16 v[24:25], v17 offset:4096\n"
  "ds_read_b64_tr_b16 v[26:27], v17 offset:6144\n"
  "ds_read_b64_tr_b16 v[28:29], v17 offset:8\n"
  "ds_read_b64_tr_b16 v[30:31], v17 offset:2056\n"
  "ds_read_b64_tr_b16 v[32:33], v17 offset:4104\n"
  "ds_read_b64_tr_b16 v[34:35], v17 offset:6152\n"
  "s_waitcnt lgkmcnt(0)\n"
  "ds_read_b64_tr_b16 v[36:37], v17 offset:16\n"
  "ds_read_b64_tr_b16 v[38:39], v17 offset:2064\n"
  "ds_read_b64_tr_b16 v[40:41], v17 offset:4112\n"
  "ds_read_b64_tr_b16 v[42:43], v17 offset:6160\n"
  "ds_read_b64_tr_b16 v[44:45], v17 offset:24\n"
  "ds_read_b64_tr_b16 v[46:47], v17 offset:2072\n"
  "ds_read_b64_tr_b16 v[48:49], v17 offset:4120\n"
  "ds_read_b64_tr_b16 v[50:51], v17 offset:6168\n"
  "s_add_u32 m0, s56, 16384\n"
  "s_mov_b32 s74, 128\n"
  "buffer_load_dwordx4 v13, s[52:55], s74 offen lds\n"
  "s_add_u32 m0, s56, 17408\n"
  "s_mov_b32 s74, 18560\n"
  "buffer_load_dwordx4 v13, s[52:55], s74 offen lds\n"
  "s_add_u32 m0, s56, 18432\n"
  "s_mov_b32 s74, 2432\n"
  "buffer_load_dwordx4 v13, s[52:55], s74 offen lds\n"
  "s_add_u32 m0, s56, 19456\n"
  "s_mov_b32 s74, 20864\n"
  "buffer_load_dwordx4 v13, s[52:55], s74 offen lds\n"
  "s_waitcnt lgkmcnt(0)\n"
  "ds_write_b128 v7, v[20:23] offset:0\n"
  "ds_write_b128 v7, v[24:27] offset:1024\n"
  "ds_write_b128 v7, v[28:31] offset:2048\n"
  "ds_write_b128 v7, v[32:35] offset:3072\n"
  "ds_write_b128 v7, v[36:39] offset:4096\n"
  "ds_write_b128 v7, v[40:43] offset:5120\n"
  "ds_write_b128 v7, v[44:47] offset:6144\n"
  "ds_write_b128 v7, v[48:51] offset:7168\n"
  "s_add_u32 m0, s56, 20480\n"
  "s_mov_b32 s74, 36992\n"
  "buffer_load_dwordx4 v13, s[52:55], s74 offen lds\n"
  "s_add_u32 m0, s56, 21504\n"
  "s_mov_b32 s74, 55424\n"
  "buffer_load_dwordx4 v13, s[52:55], s74 offen lds\n"
  "s_add_u32 m0, s56, 22528\n"
  "s_mov_b32 s74, 39296\n"
  "buffer_load_dwordx4 v13, s[52:55], s74 offen lds\n"
  "s_add_u32 m0, s56, 23552\n"
  "s_mov_b32 s74, 57728\n"
  "buffer_load_dwordx4 v13, s[52:55], s74 offen lds\n"
  "s_waitcnt lgkmcnt(0)\n"
  "s_waitcnt vmcnt(8)\n"
  "ds_read_b64_tr_b16 v[20:21], v17 offset:8192\n"
  "ds_read_b64_tr_b16 v[22:23], v17 offset:10240\n"
  "ds_read_b64_tr_b16 v[24:25], v17 offset:12288\n"
  "ds_read_b64_tr_b16 v[26:27], v17 offset:14336\n"
  "ds_read_b64_tr_b16 v[28:29], v17 offset:8200\n"
  "ds_read_b64_tr_b16 v[30:31], v17 offset:10248\n"
  "ds_read_b64_tr_b16 v[32:33], v17 offset:12296\n"
  "ds_read_b64_tr_b16 v[34:35], v17 offset:14344\n"
  "s_waitcnt lgkmcnt(0)\n"
  "ds_read_b64_tr_b16 v[36:37], v17 offset:8208\n"
  "ds_read_b64_tr_b16 v[38:39], v17 offset:10256\n"
  "ds_read_b64_tr_b16 v[40:41], v17 offset:12304\n"
  "ds_read_b64_tr_b16 v[42:43], v17 offset:14352\n"
  "ds_read_b64_tr_b16 v[44:45], v17 offset:8216\n"
  "ds_read_b64_tr_b16 v[46:47], v17 offset:10264\n"
  "ds_read_b64_tr_b16 v[48:49], v17 offset:12312\n"
  "ds_read_b64_tr_b16 v[50:51], v17 offset:14360\n"
  "s_add_u32 m0, s56, 24576\n"
  "s_mov_b32 s74, 73856\n"
  "buffer_load_dwordx4 v13, s[52:55], s74 offen lds\n"
  "s_add_u32 m0, s56, 25600\n"
  "s_mov_b32 s74, 92288\n"
  "buffer_load_dwordx4 v13, s[52:55], s74 offen lds\n"
  "s_add_u32 m0, s56, 26624\n"
  "s_mov_b32 s74, 76160\n"
  "buffer_load_dwordx4 v13, s[52:55], s74 offen lds\n"
  "s_add_u32 m0, s56, 27648\n"
  "s_mov_b32 s74, 94592\n"
  "buffer_load_dwordx4 v13, s[52:55], s74 offen lds\n"
  "s_waitcnt lgkmcnt(0)\n"
  "ds_write_b128 v7, v[20:23] offset:8192\n"
  "ds_write_b128 v7, v[24:27] offset:9216\n"
  "ds_write_b128 v7, v[28:31] offset:10240\n"
  "ds_write_b128 v7, v[32:35] offset:11264\n"
  "ds_write_b128 v7, v[36:39] offset:12288\n"
  "ds_write_b128 v7, v[40:43] offset:13312\n"
  "ds_write_b128 v7, v[44:47] offset:14336\n"
  "ds_write_b128 v7, v[48:51] offset:15360\n"
  "s_add_u32 m0, s56, 28672\n"
  "s_mov_b32 s74, 110720\n"
  "buffer_load_dwordx4 v13, s[52:55], s74 offen lds\n"
  "s_add_u32 m0, s56, 29696\n"
  "s_mov_b32 s74, 129152\n"
  "buffer_load_dwordx4 v13, s[52:55], s74 offen lds\n"
  "s_add_u32 m0, s56, 30720\n"
  "s_mov_b32 s74, 113024\n"
  "buffer_load_dwordx4 v13, s[52:55], s74 offen lds\n"
  "s_add_u32 m0, s56, 31744\n"
  "s_mov_b32 s74, 131456\n"
  "buffer_load_dwordx4 v13, s[52:55], s74 offen lds\n"
  "s_memtime s[20:21]\n"
  "s_memrealtime s[22:23]\n"
  "s_nop 3\n"
  "s_memrealtime s[24:25]\n"
  "s_nop 3\n"
  "s_memrealtime s[26:27]\n"
  "s_nop 3\n"
  "s_memrealtime s[28:29]\n"
  "s_nop 3\n"
  "s_memrealtime s[30:31]\n"
  "s_nop 3\n"
  "s_memrealtime s[32:33]\n"
  "s_mov_b32 s34, 0\n"
  "s_waitcnt lgkmcnt(0)\n"
  "s_cmp_eq_u32 s24, s22\n"
  "s_addc_u32 s34, s34, 0\n"
  "s_cmp_eq_u32 s26, s22\n"
  "s_addc_u32 s34, s34, 0\n"
  "s_cmp_eq_u32 s28, s22\n"
  "s_addc_u32 s34, s34, 0\n"
  "s_cmp_eq_u32 s30, s22\n"
  "s_addc_u32 s34, s34, 0\n"
  "s_cmp_eq_u32 s32, s22\n"
  "s_addc_u32 s34, s34, 0\n"
  "s_mul_i32 s34, s34, 20\n"
  "s_add_u32 s20, s20, s34\n"
  "s_add_u32 s22, s22, 4\n"
  "s_lshl_b32 s22, s22, 5\n"
  "s_mov_b32 s48, s20\n"
  "s_mov_b32 s49, s22\n"
  "s_waitcnt lgkmcnt(0)\n"
  "s_waitcnt vmcnt(8)\n"
  "ds_read_b64_tr_b16 v[20:21], v17 offset:16384\n"
  "ds_read_b64_tr_b16 v[22:23], v17 offset:18432\n"
  "ds_read_b64_tr_b16 v[24:25], v17 offset:20480\n"
  "ds_read_b64_tr_b16 v[26:27], v17 offset:22528\n"
  "ds_read_b64_tr_b16 v[28:29], v17 offset:16392\n"
  "ds_read_b64_tr_b16 v[30:31], v17 offset:18440\n"
  "ds_read_b64_tr_b16 v[32:33], v17 offset:20488\n"
  "ds_read_b64_tr_b16 v[34:35], v17 offset:22536\n"
  "s_waitcnt lgkmcnt(0)\n"
  "ds_read_b64_tr_b16 v[36:37], v17 offset:16400\n"
  "ds_read_b64_tr_b16 v[38:39], v17 offset:18448\n"
  "ds_read_b64_tr_b16 v[40:41], v17 offset:20496\n"
  "ds_read_b64_tr_b16 v[42:43], v17 offset:22544\n"
  "ds_read_b64_tr_b16 v[44:45], v17 offset:16408\n"
  "ds_read_b64_tr_b16 v[46:47], v17 offset:18456\n"
  "ds_read_b64_tr_b16 v[48:49], v17 offset:20504\n"
  "ds_read_b64_tr_b16 v[50:51], v17 offset:22552\n"
  "s_add_u32 m0, s56, 32768\n"
  "s_mov_b32 s74, 0\n"
  "buffer_load_dwordx4 v14, s[52:55], s74 offen lds\n"
  "s_add_u32 m0, s56, 33792\n"
  "s_mov_b32 s74, 36864\n"
  "buffer_load_dwordx4 v14, s[52:55], s74 offen lds\n"
  "s_add_u32 m0, s56, 34816\n"
  "s_mov_b32 s74, 73728\n"
  "buffer_load_dwordx4 v14, s[52:55], s74 offen lds\n"
  "s_add_u32 m0, s56, 35840\n"
  "s_mov_b32 s74, 110592\n"
  "buffer_load_dwordx4 v14, s[52:55], s74 offen lds\n"
  "s_waitcnt lgkmcnt(0)\n"
  "ds_write_b128 v7, v[20:23] offset:16384\n"
  "ds_write_b128 v7, v[24:27] offset:17408\n"
  "ds_write_b128 v7, v[28:31] offset:18432\n"
  "ds_write_b128 v7, v[32:35] offset:19456\n"
  "ds_write_b128 v7, v[36:39] offset:20480\n"
  "ds_write_b128 v7, v[40:43] offset:21504\n"
  "ds_write_b128 v7, v[44:47] offset:22528\n"
  "ds_write_b128 v7, v[48:51] offset:23552\n"
  "s_waitcnt lgkmcnt(0)\n"
  "s_waitcnt vmcnt(4)\n"
  "ds_read_b64_tr_b16 v[20:21], v17 offset:24576\n"
  "ds_read_b64_tr_b16 v[22:23], v17 offset:26624\n"
  "ds_read_b64_tr_b16 v[24:25], v17 offset:28672\n"
  "ds_read_b64_tr_b16 v[26:27], v17 offset:30720\n"
  "ds_read_b64_tr_b16 v[28:29], v17 offset:24584\n"
  "ds_read_b64_tr_b16 v[30:31], v17 offset:26632\n"
  "ds_read_b64_tr_b16 v[32:33], v17 offset:28680\n"
  "ds_read_b64_tr_b16 v[34:35], v17 offset:30728\n"
  "s_waitcnt lgkmcnt(0)\n"
  "ds_read_b64_tr_b16 v[36:37], v17 offset:24592\n"
  "ds_read_b64_tr_b16 v[38:39], v17 offset:26640\n"
  "ds_read_b64_tr_b16 v[40:41], v17 offset:28688\n"
  "ds_read_b64_tr_b16 v[42:43], v17 offset:30736\n"
  "ds_read_b64_tr_b16 v[44:45], v17 offset:24600\n"
  "ds_read_b64_tr_b16 v[46:47], v17 offset:26648\n"
  "ds_read_b64_tr_b16 v[48:49], v17 offset:28696\n"
  "ds_read_b64_tr_b16 v[50:51], v17 offset:30744\n"
  "s_waitcnt lgkmcnt(0)\n"
  "ds_write_b128 v7, v[20:23] offset:24576\n"
  "ds_write_b128 v7, v[24:27] offset:25600\n"
  "ds_write_b128 v7, v[28:31] offset:26624\n"
  "ds_write_b128 v7, v[32:35] offset:27648\n"
  "ds_write_b128 v7, v[36:39] offset:28672\n"
  "ds_write_b128 v7, v[40:43] offset:29696\n"
  "ds_write_b128 v7, v[44:47] offset:30720\n"
  "ds_write_b128 v7, v[48:51] offset:31744\n"
  "s_waitcnt lgkmcnt(0)\n"
  "s_waitcnt vmcnt(0)\n"
  "ds_read_b64_tr_b16 v[20:21], v18 offset:32768\n"
  "ds_read_b64_tr_b16 v[22:23], v18 offset:33280\n"
  "ds_read_b64_tr_b16 v[24:25], v18 offset:33792\n"
  "ds_read_b64_tr_b16 v[26:27], v18 offset:34304\n"
  "ds_read_b64_tr_b16 v[28:29], v18 offset:34816\n"
  "ds_read_b64_tr_b16 v[30:31], v18 offset:35328\n"
  "ds_read_b64_tr_b16 v[32:33], v18 offset:35840\n"
  "ds_read_b64_tr_b16 v[34:35], v18 offset:36352\n"
  "s_waitcnt lgkmcnt(0)\n"
  "ds_write_b128 v7, v[20:23] offset:32768\n"
  "ds_write_b128 v7, v[24:27] offset:33792\n"
  "ds_write_b128 v7, v[28:31] offset:34816\n"
  "ds_write_b128 v7, v[32:35] offset:35840\n"
  "s_waitcnt lgkmcnt(0)\n"
  "s_mul_i32 s56, s68, -112\n"
  "s_add_u32 s56, s56, 524288\n"
  "s_mov_b32 s70, 0\n"
  "s_mov_b32 s71, 0\n"
  "s_mov_b32 s72, 0\n"
  "s_mov_b32 s81, 0x80000000\n"
  "s_mov_b32 s84, 0x000f000f\n"
  "s_mov_b32 s85, 0x000f000f\n"
  "s_cmp_eq_u32 s68, 3072\n"
  "s_cselect_b32 s83, 32768, 0\n"
  "s_mov_b32 s50, 5958\n"
  "s_mov_b32 s51, 2816\n"
  "s_mov_b32 s52, 0\n"
  "s_mov_b32 s53, 0x80000000\n"
  "s_mov_b32 s35, 1850\n"
  "s_memtime s[20:21]\n"
  "s_memrealtime s[22:23]\n"
  "s_nop 3\n"
  "s_memrealtime s[24:25]\n"
  "s_nop 3\n"
  "s_memrealtime s[26:27]\n"
  "s_nop 3\n"
  "s_memrealtime s[28:29]\n"
  "s_nop 3\n"
  "s_memrealtime s[30:31]\n"
  "s_nop 3\n"
  "s_memrealtime s[32:33]\n"
  "s_mov_b32 s34, 0\n"
  "s_waitcnt lgkmcnt(0)\n"
  "s_cmp_eq_u32 s24, s22\n"
  "s_addc_u32 s34, s34, 0\n"
  "s_cmp_eq_u32 s26, s22\n"
  "s_addc_u32 s34, s34, 0\n"
  "s_cmp_eq_u32 s28, s22\n"
  "s_addc_u32 s34, s34, 0\n"
  "s_cmp_eq_u32 s30, s22\n"
  "s_addc_u32 s34, s34, 0\n"
  "s_cmp_eq_u32 s32, s22\n"
  "s_addc_u32 s34, s34, 0\n"
  "s_mul_i32 s34, s34, 20\n"
  "s_add_u32 s20, s20, s34\n"
  "s_add_u32 s22, s22, 4\n"
  "s_lshl_b32 s22, s22, 5\n"
  "s_sub_u32 s24, s20, s48\n"
  "s_sub_u32 s25, s22, s49\n"
  "s_mul_i32 s26, s24, s50\n"
  "s_lshr_b32 s26, s26, 12\n"
  "s_sub_u32 s26, s25, s26\n"
  "s_ashr_i32 s26, s26, 0\n"
  "s_mul_i32 s27, s25, s51\n"
  "s_lshr_b32 s27, s27, 12\n"
  "s_sub_u32 s27, s24, s27\n"
  "s_ashr_i32 s27, s27, 1\n"
  "s_cmp_lt_u32 s24, 8192\n"
  "s_cselect_b32 s26, s26, 0\n"
  "s_cselect_b32 s27, s27, 0\n"
  "s_add_u32 s50, s50, s26\n"
  "s_add_u32 s51, s51, s27\n"
  "s_mov_b32 s48, s20\n"
  "s_mov_b32 s49, s22\n"
  "s_sub_u32 s52, s48, 0x10000\n"
  "s_cmp_lt_i32 s62, 0\n"
  "s_cselect_b32 s61, -1, s61\n"
  "s_mov_b32 s62, 262016\n"
  "s_mov_b32 s80, 0\n"
  "s_mov_b32 s98, 0\n"
  "s_mov_b32 s2, 0x3f803f80\n"
  "s_mov_b32 s3, 0x3f803f80\n"
  "s_mov_b32 s1, 0\n"
  "s_mov_b32 exec_lo, 0\n"
  "buffer_load_dword v10, v9, s[36:39], 0 offen sc1\n"
  "s_waitcnt vmcnt(0)\n"
  "s_mov_b32 exec_lo, -1\n"
  "s_mov_b32 m0, s67\n"
  "s_lshr_b32 s20, s64, 17\n"
  "s_cmp_eq_u32 s68, 1024\n"
  "s_cselect_b32 s21, 210, 0\n"
  "s_mul_i32 s20, s20, 60\n"
  "s_add_u32 s20, s20, s21\n"
  "s_cmp_eq_u32 s68, 2048\n"
  "s_cselect_b32 s20, 240, s20\n"
  "s_sub_u32 s67, s20, 180\n"
  "s_mov_b32 s74, s57\n"
  "s_add_u32 s75, s57, 4096\n"
  "s_add_u32 s76, s57, 16384\n"
  "s_branch L_step_%=\n"
  "L_rel_%=:\n"
  "s_cmp_lt_u32 s70, 3\n"
  "s_cselect_b32 s22, 700, 0\n"
  "s_add_u32 s22, s22, 1080\n"
  "s_mul_i32 s22, s51, s22\n"
  "s_lshr_b32 s22, s22, 12\n"
  "s_waitcnt lgkmcnt(0)\n"
  "s_add_u32 s52, s24, s22\n"
  "L_rel2_%=:\n"
  "s_mov_b32 s22, 64\n"
  "L_w2_c_%=:\n"
  "s_memtime s[20:21]\n"
  "s_sub_u32 s22, s22, 1\n"
  "s_waitcnt lgkmcnt(0)\n"
  "s_sub_u32 s20, s52, s20\n"
  "s_cmp_eq_u32 s22, 0\n"
  "s_cselect_b32 s20, 0, s20\n"
  "s_cmp_gt_i32 s20, 190\n"
  "s_cbranch_scc1 L_w2_c_%=\n"
  "L_w2_%=:\n"
  "s_sub_u32 s20, s20, 36\n"
  "s_cmp_gt_i32 s20, 60\n"
  "s_cbranch_scc1 L_w2_%=\n"
  "L_step_%=:\n"
  "buffer_load_dwordx4 v[20:23], v0, s[36:39], s74 offen offset:0 sc1\n"
  "s_memtime s[20:21]\n"
  "s_memrealtime s[22:23]\n"
  "s_nop 3\n"
  "s_memrealtime s[24:25]\n"
  "s_nop 3\n"
  "s_memrealtime s[26:27]\n"
  "s_nop 1\n"
  "buffer_load_dwordx4 v[24:27], v0, s[36:39], s74 offen offset:1024 sc1\n"
  "s_nop 0\n"
  "s_memrealtime s[28:29]\n"
  "s_nop 3\n"
  "s_memrealtime s[30:31]\n"
  "s_nop 3\n"
  "s_memrealtime s[32:33]\n"
  "s_nop 1\n"
  "buffer_load_dwordx4 v[28:31], v0, s[36:39], s74 offen offset:2048 sc1\n"
  "v_cmp_le_i32 vcc, s70, v10\n"
  "s_and_b32 s82, s70, 15\n"
  "s_lshl_b32 s82, s82, 22\n"
  "s_add_u32 s82, s82, s63\n"
  "s_add_u32 s55, s82, s64\n"
  "v_mov_b64 v[56:57], 0\n"
  "s_add_u32 s82, s82, s65\n"
  "s_cmp_eq_u32 vcc_hi, -1\n"
  "s_cselect_b32 s0, 0, 1\n"
  "s_cselect_b32 s55, s55, 0x80000000\n"
  "v_mov_b64 v[58:59], 0\n"
  "s_add_u32 s53, s55, 524288\n"
  "s_lshr_b32 s54, s68, 1\n"
  "s_sub_u32 s54, s82, s54\n"
  "buffer_load_dwordx4 v[32:35], v0, s[36:39], s74 offen offset:3072 sc1\n"
  "v_mov_b64 v[60:61], 0\n"
  "s_add_u32 s54, s54, 4096\n"
  "s_add_u32 s87, s57, s59\n"
  "s_add_u32 s40, s80, 589824\n"
  "s_cmp_eq_u32 s40, 1179648\n"
  "v_mov_b64 v[62:63], 0\n"
  "s_cselect_b32 s40, 0, s40\n"
  "s_add_u32 s81, s100, s40\n"
  "s_add_u32 s71, s81, 65536\n"
  "s_add_u32 s79, s101, s40\n"
  "s_nop 3\n"
  "buffer_load_dwordx4 v[36:39], v0, s[36:39], s76 offen offset:0 sc1\n"
  "v_mov_b64 v[64:65], 0\n"
  "s_mov_b32 s34, 0\n"
  "s_waitcnt lgkmcnt(0)\n"
  "s_cmp_eq_u32 s24, s22\n"
  "s_addc_u32 s34, s34, 0\n"
  "v_mov_b64 v[66:67], 0\n"
  "s_cmp_eq_u32 s26, s22\n"
  "s_addc_u32 s34, s34, 0\n"
  "s_cmp_eq_u32 s28, s22\n"
  "s_addc_u32 s34, s34, 0\n"
  "s_cmp_eq_u32 s30, s22\n"
  "s_addc_u32 s34, s34, 0\n"
  "s_cmp_eq_u32 s32, s22\n"
  "s_addc_u32 s34, s34, 0\n"
  "buffer_load_dwordx4 v[40:43], v0, s[36:39], s75 offen offset:0 sc1\n"
  "v_mov_b64 v[68:69], 0\n"
  "s_mul_i32 s34, s34, 20\n"
  "s_add_u32 s20, s20, s34\n"
  "s_add_u32 s22, s22, 4\n"
  "s_lshl_b32 s22, s22, 5\n"
  "v_mov_b64 v[70:71], 0\n"
  "s_sub_u32 s24, s20, s48\n"
  "s_sub_u32 s25, s22, s49\n"
  "s_mul_i32 s26, s24, s50\n"
  "s_lshr_b32 s26, s26, 12\n"
  "s_sub_u32 s26, s25, s26\n"
  "s_ashr_i32 s26, s26, 1\n"
  "s_mul_i32 s27, s25, s51\n"
  "s_lshr_b32 s27, s27, 12\n"
  "buffer_load_dwordx4 v[44:47], v0, s[36:39], s75 offen offset:1024 sc1\n"
  "v_mov_b64 v[72:73], 0\n"
  "s_sub_u32 s27, s24, s27\n"
  "s_ashr_i32 s27, s27, 2\n"
  "s_cmp_lt_u32 s24, 8192\n"
  "s_cselect_b32 s26, s26, 0\n"
  "v_mov_b64 v[74:75], 0\n"
  "s_cselect_b32 s27, s27, 0\n"
  "s_add_u32 s50, s50, s26\n"
  "s_add_u32 s51, s51, s27\n"
  "s_mov_b32 s48, s20\n"
  "s_mov_b32 s49, s22\n"
  "s_nop 2\n"
  "buffer_load_dwordx4 v[48:51], v0, s[36:39], s75 offen offset:2048 sc1\n"
  "v_mov_b64 v[76:77], 0\n"
  "v_mov_b64 v[78:79], 0\n"
  "s_nop 11\n"
  "buffer_load_dwordx4 v[52:55], v0, s[36:39], s75 offen offset:3072 sc1\n"
  "L_p1_%=:\n"
  "s_waitcnt vmcnt(8)\n"
  "v_smfmac_f32_16x16x64_bf16 v[56:59], v[20:23], a[0:7], v1\n"
  "v_cmp_eq_u32 vcc, -1, v20\n"
  "v_smfmac_f32_16x16x64_bf16 v[64:67], v[20:23], a[16:23], v1\n"
  "buffer_load_dwordx4 v0, s[36:39], s55 offen offset:0 sc1 lds\n"
  "v_smfmac_f32_16x16x64_bf16 v[60:63], v[20:23], a[8:15], v1\n"
  "s_cbranch_vccnz L_h0_%=\n"
  "L_hb0_%=:\n"
  "v_smfmac_f32_16x16x64_bf16 v[68:71], v[20:23], a[24:31], v1\n"
  "s_waitcnt vmcnt(8)\n"
  "v_smfmac_f32_16x16x64_bf16 v[56:59], v[24:27], a[96:103], v1\n"
  "v_cmp_eq_u32 vcc, -1, v24\n"
  "v_smfmac_f32_16x16x64_bf16 v[64:67], v[24:27], a[112:119], v1\n"
  "buffer_load_dwordx4 v0, s[36:39], s55 offen offset:1024 sc1 lds\n"
  "v_smfmac_f32_16x16x64_bf16 v[60:63], v[24:27], a[104:111], v1\n"
  "s_cbranch_vccnz L_h1_%=\n"
  "L_hb1_%=:\n"
  "v_smfmac_f32_16x16x64_bf16 v[68:71], v[24:27], a[120:127], v1\n"
  "s_waitcnt vmcnt(8)\n"
  "v_smfmac_f32_16x16x64_bf16 v[56:59], v[28:31], a[192:199], v1\n"
  "v_cmp_eq_u32 vcc, -1, v28\n"
  "v_smfmac_f32_16x16x64_bf16 v[64:67], v[28:31], a[208:215], v1\n"
  "buffer_load_dwordx4 v[12:15], v0, s[36:39], s53 offen offset:0 sc1\n"
  "v_smfmac_f32_16x16x64_bf16 v[60:63], v[28:31], a[200:207], v1\n"
  "s_cbranch_vccnz L_h2_%=\n"
  "L_hb2_%=:\n"
  "v_smfmac_f32_16x16x64_bf16 v[68:71], v[28:31], a[216:223], v1\n"
  "s_waitcnt vmcnt(8)\n"
  "v_smfmac_f32_16x16x64_bf16 v[56:59], v[32:35], v[112:119], v1\n"
  "v_cmp_eq_u32 vcc, -1, v32\n"
  "v_smfmac_f32_16x16x64_bf16 v[64:67], v[32:35], v[128:135], v1\n"
  "buffer_load_dwordx4 v[16:19], v0, s[36:39], s53 offen offset:1024 sc1\n"
  "v_smfmac_f32_16x16x64_bf16 v[60:63], v[32:35], v[120:127], v1\n"
  "s_cbranch_vccnz L_h3_%=\n"
  "L_hb3_%=:\n"
  "v_smfmac_f32_16x16x64_bf16 v[68:71], v[32:35], v[136:143], v1\n"
  "s_waitcnt vmcnt(8)\n"
  "v_smfmac_f32_16x16x64_bf16 v[56:59], v[36:39], v[208:215], v1\n"
  "v_cmp_eq_u32 vcc, -1, v36\n"
  "v_smfmac_f32_16x16x64_bf16 v[64:67], v[36:39], v[224:231], v1\n"
  "v_smfmac_f32_16x16x64_bf16 v[60:63], v[36:39], v[216:223], v1\n"
  "s_cbranch_vccnz L_h4_%=\n"
  "L_hb4_%=:\n"
  "v_smfmac_f32_16x16x64_bf16 v[68:71], v[36:39], v[232:239], v1\n"
  "s_waitcnt vmcnt(7)\n"
  "v_smfmac_f32_16x16x64_bf16 v[56:59], v[40:43], a[48:55], v1\n"
  "v_cmp_eq_u32 vcc, -1, v40\n"
  "v_smfmac_f32_16x16x64_bf16 v[64:67], v[40:43], a[64:71], v1\n"
  "v_smfmac_f32_16x16x64_bf16 v[60:63], v[40:43], a[56:63], v1\n"
  "s_cbranch_vccnz L_h5_%=\n"
  "L_hb5_%=:\n"
  "v_smfmac_f32_16x16x64_bf16 v[68:71], v[40:43], a[72:79], v1\n"
  "s_waitcnt vmcnt(6)\n"
  "v_smfmac_f32_16x16x64_bf16 v[56:59], v[44:47], a[144:151], v1\n"
  "v_cmp_eq_u32 vcc, -1, v44\n"
  "v_smfmac_f32_16x16x64_bf16 v[64:67], v[44:47], a[160:167], v1\n"
  "v_smfmac_f32_16x16x64_bf16 v[60:63], v[44:47], a[152:159], v1\n"
  "s_cbranch_vccnz L_h6_%=\n"
  "L_hb6_%=:\n"
  "v_smfmac_f32_16x16x64_bf16 v[68:71], v[44:47], a[168:175], v1\n"
  "s_waitcnt vmcnt(5)\n"
  "v_smfmac_f32_16x16x64_bf16 v[56:59], v[48:51], a[240:247], v1\n"
  "v_cmp_eq_u32 vcc, -1, v48\n"
  "v_smfmac_f32_16x16x64_bf16 v[64:67], v[48:51], v[80:87], v1\n"
  "v_smfmac_f32_16x16x64_bf16 v[60:63], v[48:51], a[248:255], v1\n"
  "s_cbranch_vccnz L_h7_%=\n"
  "L_hb7_%=:\n"
  "v_smfmac_f32_16x16x64_bf16 v[68:71], v[48:51], v[88:95], v1\n"
  "s_waitcnt vmcnt(4)\n"
  "v_smfmac_f32_16x16x64_bf16 v[56:59], v[52:55], v[160:167], v1\n"
  "v_cmp_eq_u32 vcc, -1, v52\n"
  "v_smfmac_f32_16x16x64_bf16 v[64:67], v[52:55], v[176:183], v1\n"
  "v_smfmac_f32_16x16x64_bf16 v[60:63], v[52:55], v[168:175], v1\n"
  "s_cbranch_vccnz L_h8_%=\n"
  "L_hb8_%=:\n"
  "v_smfmac_f32_16x16x64_bf16 v[68:71], v[52:55], v[184:191], v1\n"
  "v_add_f32 v56, v56, v58\n"
  "v_add_f32 v57, v57, v59\n"
  "v_add_f32 v64, v64, v66\n"
  "v_add_f32 v65, v65, v67\n"
  "v_cvt_pk_bf16_f32 v66, v64, v65\n"
  "v_cvt_pk_bf16_f32 v67, v56, v57\n"
  "ds_write_b64 v2, v[66:67]\n"
  "v_add_f32 v60, v60, v62\n"
  "v_add_f32 v61, v61, v63\n"
  "v_add_f32 v68, v68, v70\n"
  "v_add_f32 v69, v69, v71\n"
  "v_cvt_pk_bf16_f32 v68, v68, v69\n"
  "v_cvt_pk_bf16_f32 v69, v60, v61\n"
  "ds_write_b64 v2, v[68:69] offset:512\n"
  "v_cmp_u_f32 vcc, v56, v57\n"
  "v_smfmac_f32_16x16x64_bf16 v[72:75], v[20:23], a[32:39], v1\n"
  "buffer_store_dwordx4 v[66:69], v0, s[36:39], s82 offen sc1\n"
  "v_smfmac_f32_16x16x64_bf16 v[76:79], v[20:23], a[40:47], v1\n"
  "s_or_b64 s[22:23], vcc, s[0:1]\n"
  "s_mov_b32 s23, 0\n"
  "v_smfmac_f32_16x16x64_bf16 v[72:75], v[24:27], a[128:135], v1\n"
  "s_cbranch_scc1 L_slow_%=\n"
  "L_ok_%=:\n"
  "v_smfmac_f32_16x16x64_bf16 v[76:79], v[24:27], a[136:143], v1\n"
  "s_waitcnt vmcnt(3) lgkmcnt(0)\n"
  "s_barrier\n"
  "ds_read_b64_tr_b16 v[56:57], v3 offset:0\n"
  "ds_read_b64_tr_b16 v[58:59], v3 offset:256\n"
  "s_memtime s[24:25]\n"
  "v_smfmac_f32_16x16x64_bf16 v[72:75], v[28:31], a[224:231], v1\n"
  "ds_read_b128 v[20:23], v4 offset:0\n"
  "ds_read_b128 v[24:27], v4 offset:256\n"
  "v_and_b32 v11, 32, v0\n"
  "v_smfmac_f32_16x16x64_bf16 v[76:79], v[28:31], a[232:239], v1\n"
  "ds_read_b64_tr_b16 v[60:61], v3 offset:1024\n"
  "ds_read_b64_tr_b16 v[62:63], v3 offset:1280\n"
  "v_lshlrev_b32 v10, 7, v0\n"
  "v_smfmac_f32_16x16x64_bf16 v[72:75], v[32:35], v[144:151], v1\n"
  "ds_read_b128 v[28:31], v4 offset:2048\n"
  "v_mad_u32_u24 v10, v11, s62, v10\n"
  "v_smfmac_f32_16x16x64_bf16 v[76:79], v[32:35], v[152:159], v1\n"
  "ds_read_b128 v[32:35], v4 offset:2304\n"
  "v_smfmac_f32_16x16x64_bf16 v[72:75], v[36:39], v[240:247], v1\n"
  "s_sub_u32 s21, s24, s52\n"
  "s_sub_u32 s20, s24, s48\n"
  "s_sub_u32 s21, s21, 1388\n"
  "v_smfmac_f32_16x16x64_bf16 v[76:79], v[36:39], v[248:255], v1\n"
  "s_max_i32 s21, s21, 0\n"
  "s_lshr_b32 s21, s21, 9\n"
  "s_waitcnt lgkmcnt(4)\n"
  "v_mfma_f32_16x16x32_bf16 v[64:67], v[20:23], v[56:59], 0\n"
  "ds_read_b128 v[20:23], v4 offset:4096\n"
  "s_cselect_b32 s22, 2200, s35\n"
  "s_add_u32 s20, s20, s22\n"
  "v_mfma_f32_16x16x32_bf16 v[68:71], v[24:27], v[56:59], 0\n"
  "ds_read_b128 v[24:27], v4 offset:4352\n"
  "s_mul_i32 s20, s20, s50\n"
  "s_min_u32 s30, s21, 3\n"
  "v_mfma_f32_16x16x32_bf16 v[36:39], v[56:59], v[56:59], 0\n"
  "ds_read_b64_tr_b16 v[56:57], v3 offset:2048\n"
  "ds_read_b64_tr_b16 v[58:59], v3 offset:2304\n"
  "s_waitcnt lgkmcnt(4)\n"
  "v_mfma_f32_16x16x32_bf16 v[64:67], v[28:31], v[60:63], v[64:67]\n"
  "ds_read_b128 v[28:31], v4 offset:6144\n"
  "s_lshr_b32 s20, s20, 13\n"
  "s_lshl1_add_u32 s20, s20, s49\n"
  "v_mfma_f32_16x16x32_bf16 v[68:71], v[32:35], v[60:63], v[68:71]\n"
  "ds_read_b128 v[32:35], v4 offset:6400\n"
  "s_lshl_b32 s22, 15, s30\n"
  "s_and_b32 s22, s22, 15\n"
  "v_mfma_f32_16x16x32_bf16 v[36:39], v[60:63], v[60:63], v[36:39]\n"
  "ds_read_b64_tr_b16 v[60:61], v3 offset:3072\n"
  "ds_read_b64_tr_b16 v[62:63], v3 offset:3328\n"
  "v_mov_b32 v11, s20\n"
  "s_mov_b64 exec, s[22:23]\n"
  "buffer_store_dword v11, v10, s[36:39], s87 offen sc1\n"
  "s_mov_b64 exec, -1\n"
  "v_smfmac_f32_16x16x64_bf16 v[72:75], v[40:43], a[80:87], v1\n"
  "v_readlane_b32 s22, v10, s30\n"
  "v_smfmac_f32_16x16x64_bf16 v[76:79], v[40:43], a[88:95], v1\n"
  "s_waitcnt lgkmcnt(4)\n"
  "v_mfma_f32_16x16x32_bf16 v[64:67], v[20:23], v[56:59], v[64:67]\n"
  "v_mov_b32 v10, v9\n"
  "s_mov_b32 exec_hi, 0\n"
  "v_add_u32 v10, s22, v9\n"
  "s_mov_b32 exec_hi, -1\n"
  "v_mfma_f32_16x16x32_bf16 v[68:71], v[24:27], v[56:59], v[68:71]\n"
  "v_mfma_f32_16x16x32_bf16 v[36:39], v[56:59], v[56:59], v[36:39]\n"
  "s_waitcnt lgkmcnt(0)\n"
  "v_mfma_f32_16x16x32_bf16 v[64:67], v[28:31], v[60:63], v[64:67]\n"
  "v_mov_b64 v[56:57], -1\n"
  "v_mov_b64 v[58:59], -1\n"
  "v_mfma_f32_16x16x32_bf16 v[68:71], v[32:35], v[60:63], v[68:71]\n"
  "buffer_store_dwordx4 v[56:59], v0, s[36:39], s81 offen sc1\n"
  "s_add_u32 s74, s58, s80\n"
  "s_add_u32 s75, s74, s56\n"
  "v_mfma_f32_16x16x32_bf16 v[36:39], v[60:63], v[60:63], v[36:39]\n"
  "v_smfmac_f32_16x16x64_bf16 v[72:75], v[44:47], a[176:183], v1\n"
  "ds_read_b128 v[20:23], v7 offset:0\n"
  "ds_read_b128 v[24:27], v7 offset:1024\n"
  "v_max3_f32 v40, v64, v65, v66\n"
  "v_max3_f32 v41, v68, v69, v70\n"
  "v_max3_f32 v40, v40, v67, v71\n"
  "v_max3_f32 v41, v41, v36, v37\n"
  "v_max_f32 v40, v40, v41\n"
  "v_max_f32 v41, v40, v41\n"
  "v_smfmac_f32_16x16x64_bf16 v[76:79], v[44:47], a[184:191], v1\n"
  "ds_read_b128 v[28:31], v7 offset:8192\n"
  "ds_read_b128 v[32:35], v7 offset:9216\n"
  "v_permlane16_swap_b32 v40, v41\n"
  "v_max_f32 v40, v40, v41\n"
  "v_max_f32 v41, v40, v41\n"
  "v_smfmac_f32_16x16x64_bf16 v[72:75], v[48:51], v[96:103], v1\n"
  "buffer_store_dwordx4 v[56:59], v0, s[36:39], s71 offen sc1\n"
  "v_permlane32_swap_b32 v40, v41\n"
  "v_max_f32 v40, v40, v41\n"
  "v_mul_f32 v40, 0xbe0293ee, v40\n"
  "v_smfmac_f32_16x16x64_bf16 v[76:79], v[48:51], v[104:111], v1\n"
  "s_add_u32 s76, s74, 65536\n"
  "s_add_u32 s78, s60, s80\n"
  "v_fmamk_f32 v64, v64, 0x3e0293ee, v40\n"
  "v_smfmac_f32_16x16x64_bf16 v[72:75], v[52:55], v[192:199], v1\n"
  "v_fmamk_f32 v65, v65, 0x3e0293ee, v40\n"
  "v_smfmac_f32_16x16x64_bf16 v[76:79], v[52:55], v[200:207], v1\n"
  "s_waitcnt vmcnt(4)\n"
  "v_fmamk_f32 v66, v66, 0x3e0293ee, v40\n"
  "v_fmamk_f32 v67, v67, 0x3e0293ee, v40\n"
  "v_fmamk_f32 v68, v68, 0x3e0293ee, v40\n"
  "v_fmamk_f32 v69, v69, 0x3e0293ee, v40\n"
  "v_fmamk_f32 v70, v70, 0x3e0293ee, v40\n"
  "v_fmamk_f32 v71, v71, 0x3e0293ee, v40\n"
  "v_fmamk_f32 v36, v36, 0x3e0293ee, v40\n"
  "v_fmamk_f32 v37, v37, 0x3e0293ee, v40\n"
  "buffer_store_dwordx4 v[56:59], v0, s[36:39], s79 offen sc1\n"
  "v_exp_f32 v64, v64\n"
  "v_exp_f32 v65, v65\n"
  "v_exp_f32 v66, v66\n"
  "v_exp_f32 v67, v67\n"
  "v_exp_f32 v68, v68\n"
  "v_exp_f32 v69, v69\n"
  "v_exp_f32 v70, v70\n"
  "v_exp_f32 v71, v71\n"
  "v_exp_f32 v36, v36\n"
  "v_exp_f32 v37, v37\n"
  "v_mov_b64 v[52:53], s[2:3]\n"
  "v_mov_b64 v[54:55], s[2:3]\n"
  "v_cvt_pk_bf16_f32 v60, v64, v65\n"
  "v_cvt_pk_bf16_f32 v61, v66, v67\n"
  "v_cvt_pk_bf16_f32 v62, v68, v69\n"
  "v_cvt_pk_bf16_f32 v63, v70, v71\n"
  "v_cvt_pk_bf16_f32 v56, v36, v37\n"
  "v_mov_b32 v57, 0\n"
  "v_mfma_f32_16x16x32_bf16 v[40:43], v[52:55], v[60:63], 0\n"
  "v_add_f32 v72, v72, v74\n"
  "v_add_f32 v73, v73, v75\n"
  "v_mfma_f32_16x16x32_bf16 v[44:47], v[12:15], v[60:63], 0\n"
  "v_add_f32 v76, v76, v78\n"
  "v_add_f32 v77, v77, v79\n"
  "v_mfma_f32_16x16x32_bf16 v[48:51], v[16:19], v[60:63], 0\n"
  "v_cvt_pk_bf16_f32 v72, v72, v73\n"
  "v_cvt_pk_bf16_f32 v73, v76, v77\n"
  "v_mfma_f32_16x16x16_bf16 v[40:43], v[52:53], v[56:57], v[40:43]\n"
  "v_mov_b32 v58, 0\n"
  "v_cvt_pk_bf16_f32 v59, v36, v37\n"
  "v_mfma_f32_16x16x16_bf16 v[44:47], v[72:73], v[56:57], v[44:47]\n"
  "s_nop 0\n"
  "v_mov_b64 v[64:65], 0\n"
  "v_mov_b64 v[66:67], 0\n"
  "v_mfma_f32_16x16x16_bf16 v[48:51], v[72:73], v[58:59], v[48:51]\n"
  "ds_read_b128 v[12:15], v7 offset:2048\n"
  "ds_read_b128 v[16:19], v7 offset:3072\n"
  "v_rcp_f32 v52, v40\n"
  "v_mov_b64 v[68:69], 0\n"
  "v_mul_f32 v44, v44, v52\n"
  "v_mul_f32 v45, v45, v52\n"
  "v_mul_f32 v46, v46, v52\n"
  "v_mul_f32 v47, v47, v52\n"
  "v_cvt_pk_bf16_f32 v60, v44, v45\n"
  "v_cvt_pk_bf16_f32 v61, v46, v47\n"
  "v_pk_mul_f32 v[48:49], v[48:49], v[52:53] op_sel_hi:[1,0]\n"
  "v_pk_mul_f32 v[50:51], v[50:51], v[52:53] op_sel_hi:[1,0]\n"
  "v_cvt_pk_bf16_f32 v62, v48, v49\n"
  "v_cvt_pk_bf16_f32 v63, v50, v51\n"
  "ds_write_b128 v6, v[60:63]\n"
  "buffer_load_dword v10, v10, s[36:39], 0 offen sc1\n"
  "v_mov_b64 v[70:71], 0\n"
  "v_mov_b64 v[76:77], 0\n"
  "v_mov_b64 v[78:79], 0\n"
  "v_mov_b64 v[44:45], 0\n"
  "v_mov_b64 v[46:47], 0\n"
  "v_mov_b64 v[48:49], 0\n"
  "v_mov_b64 v[50:51], 0\n"
  "v_mov_b64 v[52:53], 0\n"
  "v_mov_b64 v[54:55], 0\n"
  "v_lshrrev_b32 v11, 1, v0\n"
  "s_cmp_eq_u32 s70, 63\n"
  "s_waitcnt lgkmcnt(0)\n"
  "s_barrier\n"
  "ds_read_b128 v[56:59], v8\n"
  "ds_read_b128 v[60:63], v8 offset:1024\n"
  "ds_read_b128 v[36:39], v7 offset:10240\n"
  "ds_read_b128 v[40:43], v7 offset:11264\n"
  "buffer_store_dwordx2 v[72:73], v11, s[36:39], s54 offen sc1\n"
  "s_cbranch_scc1 L_last_%=\n"
  "s_waitcnt lgkmcnt(3)\n"
  "v_smfmac_f32_16x16x64_bf16 v[64:67], v[56:59], v[20:27], v1\n"
  "ds_read_b128 v[20:23], v7 offset:4096\n"
  "ds_read_b128 v[24:27], v7 offset:5120\n"
  "s_waitcnt lgkmcnt(4)\n"
  "v_smfmac_f32_16x16x64_bf16 v[64:67], v[60:63], v[28:35], v1\n"
  "ds_read_b128 v[28:31], v7 offset:12288\n"
  "ds_read_b128 v[32:35], v7 offset:13312\n"
  "v_smfmac_f32_16x16x64_bf16 v[68:71], v[56:59], v[12:19], v1\n"
  "ds_read_b128 v[12:15], v7 offset:6144\n"
  "ds_read_b128 v[16:19], v7 offset:7168\n"
  "s_waitcnt lgkmcnt(6)\n"
  "v_smfmac_f32_16x16x64_bf16 v[68:71], v[60:63], v[36:43], v1\n"
  "ds_read_b128 v[36:39], v7 offset:14336\n"
  "ds_read_b128 v[40:43], v7 offset:15360\n"
  "v_add_f32 v64, v64, v66\n"
  "v_add_f32 v65, v65, v67\n"
  "v_cvt_pk_bf16_f32 v72, v64, v65\n"
  "s_waitcnt lgkmcnt(4)\n"
  "v_smfmac_f32_16x16x64_bf16 v[76:79], v[56:59], v[20:27], v1\n"
  "ds_read_b128 v[20:23], v7 offset:32768\n"
  "ds_read_b128 v[24:27], v7 offset:33792\n"
  "v_mov_b64 v[64:65], 0\n"
  "v_mov_b64 v[66:67], 0\n"
  "v_smfmac_f32_16x16x64_bf16 v[76:79], v[60:63], v[28:35], v1\n"
  "ds_read_b128 v[28:31], v7 offset:34816\n"
  "ds_read_b128 v[32:35], v7 offset:35840\n"
  "v_add_f32 v68, v68, v70\n"
  "v_add_f32 v69, v69, v71\n"
  "v_cvt_pk_bf16_f32 v73, v68, v69\n"
  "s_waitcnt lgkmcnt(4)\n"
  "v_smfmac_f32_16x16x64_bf16 v[44:47], v[56:59], v[12:19], v1\n"
  "ds_read_b128 v[12:15], v7 offset:16384\n"
  "ds_read_b128 v[16:19], v7 offset:17408\n"
  "v_mov_b64 v[68:69], 0\n"
  "v_mov_b64 v[70:71], 0\n"
  "v_smfmac_f32_16x16x64_bf16 v[44:47], v[60:63], v[36:43], v1\n"
  "ds_read_b128 v[36:39], v7 offset:24576\n"
  "ds_read_b128 v[40:43], v7 offset:25600\n"
  "v_add_f32 v76, v76, v78\n"
  "v_add_f32 v77, v77, v79\n"
  "v_cvt_pk_bf16_f32 v74, v76, v77\n"
  "s_waitcnt lgkmcnt(4)\n"
  "v_smfmac_f32_16x16x64_bf16 v[48:51], v[56:59], v[20:27], v1\n"
  "ds_read_b128 v[20:23], v7 offset:18432\n"
  "ds_read_b128 v[24:27], v7 offset:19456\n"
  "v_mov_b64 v[76:77], 0\n"
  "v_mov_b64 v[78:79], 0\n"
  "v_smfmac_f32_16x16x64_bf16 v[48:51], v[60:63], v[28:35], v1\n"
  "ds_read_b128 v[28:31], v7 offset:26624\n"
  "ds_read_b128 v[32:35], v7 offset:27648\n"
  "v_add_f32 v44, v44, v46\n"
  "v_add_f32 v45, v45, v47\n"
  "v_cvt_pk_bf16_f32 v75, v44, v45\n"
  "buffer_store_dwordx4 v[72:75], v5, s[36:39], s74 offen sc1\n"
  "s_waitcnt lgkmcnt(4)\n"
  "v_smfmac_f32_16x16x64_bf16 v[52:55], v[56:59], v[12:19], v1\n"
  "ds_read_b128 v[12:15], v7 offset:20480\n"
  "ds_read_b128 v[16:19], v7 offset:21504\n"
  "v_smfmac_f32_16x16x64_bf16 v[52:55], v[60:63], v[36:43], v1\n"
  "ds_read_b128 v[36:39], v7 offset:28672\n"
  "ds_read_b128 v[40:43], v7 offset:29696\n"
  "v_add_f32 v48, v48, v50\n"
  "v_add_f32 v49, v49, v51\n"
  "v_cvt_pk_bf16_f32 v72, v48, v49\n"
  "s_waitcnt lgkmcnt(4)\n"
  "v_smfmac_f32_16x16x64_bf16 v[64:67], v[56:59], v[20:27], v1\n"
  "ds_read_b128 v[20:23], v7 offset:22528\n"
  "ds_read_b128 v[24:27], v7 offset:23552\n"
  "v_mov_b32_dpp v73, v72 row_shl:4 row_mask:0xf bank_mask:0xf bound_ctrl:1\n"
  "v_mov_b32_dpp v74, v72 row_shl:8 row_mask:0xf bank_mask:0xf bound_ctrl:1\n"
  "v_mov_b32_dpp v75, v72 row_shl:12 row_mask:0xf bank_mask:0xf bound_ctrl:1\n"
  "s_mov_b64 exec, s[84:85]\n"
  "buffer_store_dwordx4 v[72:75], v5, s[36:39], s75 offen sc1\n"
  "s_mov_b64 exec, -1\n"
  "v_smfmac_f32_16x16x64_bf16 v[64:67], v[60:63], v[28:35], v1\n"
  "ds_read_b128 v[28:31], v7 offset:30720\n"
  "ds_read_b128 v[32:35], v7 offset:31744\n"
  "s_waitcnt vmcnt(3)\n"
  "v_subrev_u32 v11, s49, v10\n"
  "v_add_f32 v52, v52, v54\n"
  "v_add_f32 v53, v53, v55\n"
  "v_cvt_pk_bf16_f32 v72, v52, v53\n"
  "v_max_i32_dpp v11, v11, v11 row_ror:8 row_mask:0xf bank_mask:0xf\n"
  "s_waitcnt lgkmcnt(4)\n"
  "v_smfmac_f32_16x16x64_bf16 v[68:71], v[56:59], v[12:19], v1\n"
  "s_nop 0\n"
  "v_max_i32_dpp v11, v11, v11 row_ror:4 row_mask:0xf bank_mask:0xf\n"
  "v_cmp_eq_u32 vcc, -1, v10\n"
  "v_smfmac_f32_16x16x64_bf16 v[68:71], v[60:63], v[36:43], v1\n"
  "v_max_i32_dpp v11, v11, v11 row_ror:2 row_mask:0xf bank_mask:0xf\n"
  "v_add_f32 v64, v64, v66\n"
  "v_add_f32 v65, v65, v67\n"
  "v_cvt_pk_bf16_f32 v73, v64, v65\n"
  "v_max_i32_dpp v11, v11, v11 row_ror:1 row_mask:0xf bank_mask:0xf\n"
  "s_waitcnt lgkmcnt(0)\n"
  "v_smfmac_f32_16x16x64_bf16 v[76:79], v[56:59], v[20:27], v1\n"
  "s_memtime s[20:21]\n"
  "v_readlane_b32 s22, v11, 0\n"
  "v_readlane_b32 s23, v11, 16\n"
  "v_smfmac_f32_16x16x64_bf16 v[76:79], v[60:63], v[28:35], v1\n"
  "s_max_i32 s22, s22, s23\n"
  "s_mul_i32 s22, s22, s51\n"
  "v_add_f32 v68, v68, v70\n"
  "v_add_f32 v69, v69, v71\n"
  "v_cvt_pk_bf16_f32 v74, v68, v69\n"
  "s_ashr_i32 s22, s22, 12\n"
  "s_add_u32 s52, s22, s48\n"
  "s_cmp_eq_u32 s83, 0\n"
  "v_add_f32 v76, v76, v78\n"
  "v_add_f32 v77, v77, v79\n"
  "v_cvt_pk_bf16_f32 v75, v76, v77\n"
  "buffer_store_dwordx4 v[72:75], v5, s[36:39], s76 offen sc1\n"
  "s_cbranch_scc1 L_red_%=\n"
  "s_waitcnt lgkmcnt(0)\n"
  "s_sub_u32 s20, s52, s20\n"
  "s_cmp_gt_i32 s20, 500\n"
  "s_cselect_b32 s21, 1, vcc_lo\n"
  "s_cmp_lg_u32 s21, 0\n"
  "s_cbranch_scc1 L_tkslow_%=\n"
  "L_w_%=:\n"
  "s_sub_u32 s20, s20, 36\n"
  "s_cmp_gt_i32 s20, 110\n"
  "s_cbranch_scc1 L_w_%=\n"
  "L_be_%=:\n"
  "s_barrier\n"
  "s_add_u32 s52, s52, 350\n"
  "s_mov_b32 s22, 64\n"
  "L_w1_c_%=:\n"
  "s_memtime s[20:21]\n"
  "s_sub_u32 s22, s22, 1\n"
  "s_waitcnt lgkmcnt(0)\n"
  "s_sub_u32 s20, s52, s20\n"
  "s_cmp_eq_u32 s22, 0\n"
  "s_cselect_b32 s20, 0, s20\n"
  "s_cmp_gt_i32 s20, 190\n"
  "s_cbranch_scc1 L_w1_c_%=\n"
  "L_w1_%=:\n"
  "s_sub_u32 s20, s20, 36\n"
  "s_cmp_gt_i32 s20, 60\n"
  "s_cbranch_scc1 L_w1_%=\n"
  "s_barrier\n"
  "s_memtime s[24:25]\n"
  "v_mov_b32 v20, s70\n"
  "s_add_u32 s70, s70, 1\n"
  "s_add_u32 s57, s57, 32768\n"
  "s_mov_b32 exec_hi, 0\n"
  "v_add_u32 v9, s83, v9\n"
  "s_mov_b32 exec_hi, -1\n"
  "s_mov_b32 s80, s40\n"
  "s_mov_b32 s74, s57\n"
  "s_add_u32 s75, s57, 4096\n"
  "s_add_u32 s76, s57, 16384\n"
  "s_sleep 1\n"
  "s_waitcnt vmcnt(3)\n"
  "s_mov_b64 exec, 1\n"
  "buffer_store_dword v20, off, s[36:39], s66 sc1\n"
  "s_mov_b64 exec, -1\n"
  "s_branch L_rel_%=\n"
  "L_tkslow_%=:\n"
  "s_cmp_eq_u32 vcc_lo, 0\n"
  "s_cbranch_scc1 L_tkw_%=\n"
  "s_mov_b32 s73, 0\n"
  "L_tkck_%=:\n"
  "s_add_u32 s73, s73, 1\n"
  "s_cmp_gt_u32 s73, 32768\n"
  "s_cselect_b32 s72, 1, s72\n"
  "s_cmp_lg_u32 s72, 0\n"
  "s_cbranch_scc1 L_tkgo_%=\n"
  "s_add_u32 s30, s30, 1\n"
  "s_min_u32 s30, s30, 3\n"
  "s_lshr_b32 s21, s30, 1\n"
  "s_lshl_b32 s21, s21, 23\n"
  "s_and_b32 s22, s30, 1\n"
  "s_lshl_b32 s22, s22, 11\n"
  "s_or_b32 s21, s21, s22\n"
  "s_mov_b32 exec_hi, 0\n"
  "buffer_load_dword v10, v9, s[36:39], s21 offen sc1\n"
  "s_mov_b32 exec_hi, -1\n"
  "s_waitcnt vmcnt(0)\n"
  "v_cmp_eq_u32 vcc, -1, v10\n"
  "s_cmp_lg_u32 vcc_lo, 0\n"
  "s_cbranch_scc1 L_tkck_%=\n"
  "L_tkgo_%=:\n"
  "v_subrev_u32 v11, s49, v10\n"
  "s_nop 1\n"
  "v_max_i32_dpp v11, v11, v11 row_ror:8 row_mask:0xf bank_mask:0xf\n"
  "s_nop 1\n"
  "v_max_i32_dpp v11, v11, v11 row_ror:4 row_mask:0xf bank_mask:0xf\n"
  "s_nop 1\n"
  "v_max_i32_dpp v11, v11, v11 row_ror:2 row_mask:0xf bank_mask:0xf\n"
  "s_nop 1\n"
  "v_max_i32_dpp v11, v11, v11 row_ror:1 row_mask:0xf bank_mask:0xf\n"
  "s_nop 1\n"
  "v_readlane_b32 s22, v11, 0\n"
  "v_readlane_b32 s23, v11, 16\n"
  "s_max_i32 s22, s22, s23\n"
  "s_mul_i32 s22, s22, s51\n"
  "s_ashr_i32 s22, s22, 12\n"
  "s_add_u32 s52, s22, s48\n"
  "L_tkw_%=:\n"
  "s_mov_b32 s22, 64\n"
  "L_w0_c_%=:\n"
  "s_memtime s[20:21]\n"
  "s_sub_u32 s22, s22, 1\n"
  "s_waitcnt lgkmcnt(0)\n"
  "s_sub_u32 s20, s52, s20\n"
  "s_cmp_eq_u32 s22, 0\n"
  "s_cselect_b32 s20, 0, s20\n"
  "s_cmp_gt_i32 s20, 218\n"
  "s_cbranch_scc1 L_w0_c_%=\n"
  "L_w0_%=:\n"
  "s_sub_u32 s20, s20, 36\n"
  "s_cmp_gt_i32 s20, 88\n"
  "s_cbranch_scc1 L_w0_%=\n"
  "s_branch L_be_%=\n"
  "L_red_%=:\n"
  "s_waitcnt vmcnt(3)\n"
  "s_cmp_lt_i32 s61, 0\n"
  "s_cbranch_scc1 L_idle_%=\n"
  "s_cmp_eq_u32 s68, 1024\n"
  "s_cbranch_scc0 L_early_%=\n"
  "s_barrier\n"
  "L_early_%=:\n"
  "s_cmp_eq_u32 s68, 2048\n"
  "s_barrier\n"
  "s_cbranch_scc1 L_w2p_%=\n"
  "L_w2b_%=:\n"
  "buffer_load_dwordx4 v[28:31], v0, s[36:39], s78 offen offset:0 sc1\n"
  "buffer_load_dwordx4 v[32:35], v0, s[36:39], s78 offen offset:1024 sc1\n"
  "buffer_load_dwordx4 v[36:39], v0, s[36:39], s78 offen offset:2048 sc1\n"
  "buffer_load_dwordx4 v[40:43], v0, s[36:39], s78 offen offset:3072 sc1\n"
  "s_cmp_eq_u32 s68, 1024\n"
  "s_cbranch_scc1 L_nb_%=\n"
  "s_barrier\n"
  "L_nb_%=:\n"
  "s_memtime s[24:25]\n"
  "ds_read_b128 v[24:27], v8 offset:2048\n"
  "v_lshrrev_b32 v22, 2, v0\n"
  "v_and_b32 v22, 0x40, v22\n"
  "v_bfe_u32 v23, v0, 4, 3\n"
  "v_lshl_add_u32 v22, v23, 3, v22\n"
  "v_and_b32 v23, 0x280, v0\n"
  "v_lshl_or_b32 v22, v23, 22, v22\n"
  "s_add_u32 s77, s57, s61\n"
  "s_add_u32 s70, s70, 1\n"
  "s_add_u32 s57, s57, 32768\n"
  "s_mov_b32 exec_hi, 0\n"
  "v_add_u32 v9, s83, v9\n"
  "s_mov_b32 exec_hi, -1\n"
  "s_mov_b32 s80, s40\n"
  "s_mov_b32 s74, s57\n"
  "s_add_u32 s75, s57, 4096\n"
  "s_add_u32 s76, s57, 16384\n"
  "s_cmp_lt_u32 s70, 3\n"
  "s_cselect_b32 s26, 0, s68\n"
  "s_cselect_b32 s22, 700, 0\n"
  "s_add_u32 s22, s22, 1080\n"
  "s_mul_i32 s22, s51, s22\n"
  "s_lshr_b32 s22, s22, 12\n"
  "s_waitcnt lgkmcnt(0)\n"
  "s_add_u32 s52, s24, s22\n"
  "s_add_u32 s27, s52, s67\n"
  "L_rsw_%=:\n"
  "s_waitcnt vmcnt(3)\n"
  "v_mfma_f32_16x16x32_bf16 v[44:47], v[28:31], v[24:27], 0\n"
  "s_waitcnt vmcnt(2)\n"
  "v_mfma_f32_16x16x32_bf16 v[44:47], v[32:35], v[24:27], v[44:47]\n"
  "s_waitcnt vmcnt(1)\n"
  "v_mfma_f32_16x16x32_bf16 v[44:47], v[36:39], v[24:27], v[44:47]\n"
  "s_waitcnt vmcnt(0)\n"
  "v_mfma_f32_16x16x32_bf16 v[44:47], v[40:43], v[24:27], v[44:47]\n"
  "L_rsfin_%=:\n"
  "s_memtime s[20:21]\n"
  "s_nop 6\n"
  "v_permlane32_swap_b32 v44, v46\n"
  "v_permlane32_swap_b32 v45, v47\n"
  "v_pk_add_f32 v[44:45], v[44:45], v[46:47]\n"
  "v_cmp_u_f32 vcc, v44, v45\n"
  "s_waitcnt lgkmcnt(0)\n"
  "v_cvt_pk_bf16_f32 v46, v44, v45\n"
  "v_cvt_pk_bf16_f32 v47, v44, v45\n"
  "s_sub_u32 s20, s27, s20\n"
  "s_and_b32 s20, s20, 0x80000000\n"
  "v_permlane32_swap_b32 v46, v47\n"
  "s_or_b32 s20, s20, s77\n"
  "s_cbranch_vccnz L_rsslow_%=\n"
  "L_rsok_%=:\n"
  "buffer_store_dwordx2 v[46:47], v22, s[36:39], s20 offen sc1\n"
  "s_bitset1_b32 s77, 23\n"
  "buffer_store_dwordx2 v[46:47], v22, s[36:39], s77 offen sc1\n"
  "s_cmp_eq_u32 s26, 1024\n"
  "s_cbranch_scc0 L_rel2_%=\n"
  "buffer_load_dwordx4 v[20:23], v0, s[36:39], s74 offen offset:0 sc1\n"
  "v_cmp_le_i32 vcc, s70, v10\n"
  "v_mov_b64 v[56:57], 0\n"
  "s_and_b32 s82, s70, 15\n"
  "s_lshl_b32 s82, s82, 22\n"
  "s_add_u32 s82, s82, s63\n"
  "s_nop 7\n"
  "buffer_load_dwordx4 v[24:27], v0, s[36:39], s74 offen offset:1024 sc1\n"
  "v_mov_b64 v[58:59], 0\n"
  "v_mov_b64 v[60:61], 0\n"
  "s_add_u32 s55, s82, s64\n"
  "s_add_u32 s82, s82, s65\n"
  "s_cmp_eq_u32 vcc_hi, -1\n"
  "s_nop 7\n"
  "buffer_load_dwordx4 v[28:31], v0, s[36:39], s74 offen offset:2048 sc1\n"
  "v_mov_b64 v[62:63], 0\n"
  "v_mov_b64 v[64:65], 0\n"
  "s_cselect_b32 s0, 0, 1\n"
  "s_cselect_b32 s55, s55, 0x80000000\n"
  "s_add_u32 s53, s55, 524288\n"
  "s_nop 7\n"
  "buffer_load_dwordx4 v[32:35], v0, s[36:39], s74 offen offset:3072 sc1\n"
  "v_mov_b64 v[66:67], 0\n"
  "v_mov_b64 v[68:69], 0\n"
  "s_lshr_b32 s54, s68, 1\n"
  "s_sub_u32 s54, s82, s54\n"
  "s_add_u32 s54, s54, 4096\n"
  "s_nop 7\n"
  "buffer_load_dwordx4 v[36:39], v0, s[36:39], s76 offen offset:0 sc1\n"
  "v_mov_b64 v[70:71], 0\n"
  "v_mov_b64 v[72:73], 0\n"
  "s_add_u32 s87, s57, s59\n"
  "s_add_u32 s40, s80, 589824\n"
  "s_cmp_eq_u32 s40, 1179648\n"
  "s_nop 7\n"
  "buffer_load_dwordx4 v[40:43], v0, s[36:39], s75 offen offset:0 sc1\n"
  "v_mov_b64 v[74:75], 0\n"
  "v_mov_b64 v[76:77], 0\n"
  "s_cselect_b32 s40, 0, s40\n"
  "s_add_u32 s81, s100, s40\n"
  "s_add_u32 s71, s81, 65536\n"
  "s_nop 7\n"
  "buffer_load_dwordx4 v[44:47], v0, s[36:39], s75 offen offset:1024 sc1\n"
  "v_mov_b64 v[78:79], 0\n"
  "s_add_u32 s79, s101, s40\n"
  "s_nop 10\n"
  "buffer_load_dwordx4 v[48:51], v0, s[36:39], s75 offen offset:2048 sc1\n"
  "s_nop 12\n"
  "buffer_load_dwordx4 v[52:55], v0, s[36:39], s75 offen offset:3072 sc1\n"
  "s_branch L_p1_%=\n"
  "L_w2p_%=:\n"
  "s_branch L_w2b_%=\n"
  "L_idle_%=:\n"
  "s_barrier\n"
  "s_barrier\n"
  "s_memtime s[24:25]\n"
  "s_add_u32 s70, s70, 1\n"
  "s_add_u32 s57, s57, 32768\n"
  "s_mov_b32 exec_hi, 0\n"
  "v_add_u32 v9, s83, v9\n"
  "s_mov_b32 exec_hi, -1\n"
  "s_mov_b32 s80, s40\n"
  "s_mov_b32 s74, s57\n"
  "s_add_u32 s75, s57, 4096\n"
  "s_add_u32 s76, s57, 16384\n"
  "s_branch L_rel_%=\n"
  "L_h0_%=:\n"
  "s_add_u32 s98, s98, 1\n"
  "s_cmp_gt_u32 s98, 32768\n"
  "s_cselect_b32 s72, 1, s72\n"
  "s_cmp_lg_u32 s72, 0\n"
  "s_cbranch_scc1 L_hb0_%=\n"
  "s_add_u32 s74, s57, 8388608\n"
  "s_add_u32 s75, s74, 4096\n"
  "s_add_u32 s76, s74, 16384\n"
  "s_branch L_re0_%=\n"
  "L_h1_%=:\n"
  "s_add_u32 s98, s98, 1\n"
  "s_cmp_gt_u32 s98, 32768\n"
  "s_cselect_b32 s72, 1, s72\n"
  "s_cmp_lg_u32 s72, 0\n"
  "s_cbranch_scc1 L_hb1_%=\n"
  "s_add_u32 s74, s57, 8388608\n"
  "s_add_u32 s75, s74, 4096\n"
  "s_add_u32 s76, s74, 16384\n"
  "s_branch L_re1_%=\n"
  "L_h2_%=:\n"
  "s_add_u32 s98, s98, 1\n"
  "s_cmp_gt_u32 s98, 32768\n"
  "s_cselect_b32 s72, 1, s72\n"
  "s_cmp_lg_u32 s72, 0\n"
  "s_cbranch_scc1 L_hb2_%=\n"
  "s_add_u32 s74, s57, 8388608\n"
  "s_add_u32 s75, s74, 4096\n"
  "s_add_u32 s76, s74, 16384\n"
  "s_branch L_re2_%=\n"
  "L_h3_%=:\n"
  "s_add_u32 s98, s98, 1\n"
  "s_cmp_gt_u32 s98, 32768\n"
  "s_cselect_b32 s72, 1, s72\n"
  "s_cmp_lg_u32 s72, 0\n"
  "s_cbranch_scc1 L_hb3_%=\n"
  "s_add_u32 s74, s57, 8388608\n"
  "s_add_u32 s75, s74, 4096\n"
  "s_add_u32 s76, s74, 16384\n"
  "s_branch L_re3_%=\n"
  "L_h4_%=:\n"
  "s_add_u32 s98, s98, 1\n"
  "s_cmp_gt_u32 s98, 32768\n"
  "s_cselect_b32 s72, 1, s72\n"
  "s_cmp_lg_u32 s72, 0\n"
  "s_cbranch_scc1 L_hb4_%=\n"
  "s_add_u32 s74, s57, 8388608\n"
  "s_add_u32 s75, s74, 4096\n"
  "s_add_u32 s76, s74, 16384\n"
  "s_branch L_re4_%=\n"
  "L_h5_%=:\n"
  "s_add_u32 s98, s98, 1\n"
  "s_cmp_gt_u32 s98, 32768\n"
  "s_cselect_b32 s72, 1, s72\n"
  "s_cmp_lg_u32 s72, 0\n"
  "s_cbranch_scc1 L_hb5_%=\n"
  "s_add_u32 s74, s57, 8388608\n"
  "s_add_u32 s75, s74, 4096\n"
  "s_add_u32 s76, s74, 16384\n"
  "s_branch L_re5_%=\n"
  "L_h6_%=:\n"
  "s_add_u32 s98, s98, 1\n"
  "s_cmp_gt_u32 s98, 32768\n"
  "s_cselect_b32 s72, 1, s72\n"
  "s_cmp_lg_u32 s72, 0\n"
  "s_cbranch_scc1 L_hb6_%=\n"
  "s_add_u32 s74, s57, 8388608\n"
  "s_add_u32 s75, s74, 4096\n"
  "s_add_u32 s76, s74, 16384\n"
  "s_branch L_re6_%=\n"
  "L_h7_%=:\n"
  "s_add_u32 s98, s98, 1\n"
  "s_cmp_gt_u32 s98, 32768\n"
  "s_cselect_b32 s72, 1, s72\n"
  "s_cmp_lg_u32 s72, 0\n"
  "s_cbranch_scc1 L_hb7_%=\n"
  "s_add_u32 s74, s57, 8388608\n"
  "s_add_u32 s75, s74, 4096\n"
  "s_add_u32 s76, s74, 16384\n"
  "s_branch L_re7_%=\n"
  "L_h8_%=:\n"
  "s_add_u32 s98, s98, 1\n"
  "s_cmp_gt_u32 s98, 32768\n"
  "s_cselect_b32 s72, 1, s72\n"
  "s_cmp_lg_u32 s72, 0\n"
  "s_cbranch_scc1 L_hb8_%=\n"
  "s_add_u32 s74, s57, 8388608\n"
  "s_add_u32 s75, s74, 4096\n"
  "s_add_u32 s76, s74, 16384\n"
  "s_branch L_re8_%=\n"
  "L_re0_%=:\n"
  "buffer_load_dwordx4 v[20:23], v0, s[36:39], s74 offen offset:0 sc1\n"
  "s_nop 11\n"
  "L_re1_%=:\n"
  "buffer_load_dwordx4 v[24:27], v0, s[36:39], s74 offen offset:1024 sc1\n"
  "s_nop 11\n"
  "L_re2_%=:\n"
  "buffer_load_dwordx4 v[28:31], v0, s[36:39], s74 offen offset:2048 sc1\n"
  "s_nop 11\n"
  "L_re3_%=:\n"
  "buffer_load_dwordx4 v[32:35], v0, s[36:39], s74 offen offset:3072 sc1\n"
  "s_nop 11\n"
  "L_re4_%=:\n"
  "buffer_load_dwordx4 v[36:39], v0, s[36:39], s76 offen offset:0 sc1\n"
  "s_nop 11\n"
  "L_re5_%=:\n"
  "buffer_load_dwordx4 v[40:43], v0, s[36:39], s75 offen offset:0 sc1\n"
  "s_nop 11\n"
  "L_re6_%=:\n"
  "buffer_load_dwordx4 v[44:47], v0, s[36:39], s75 offen offset:1024 sc1\n"
  "s_nop 11\n"
  "L_re7_%=:\n"
  "buffer_load_dwordx4 v[48:51], v0, s[36:39], s75 offen offset:2048 sc1\n"
  "s_nop 11\n"
  "L_re8_%=:\n"
  "buffer_load_dwordx4 v[52:55], v0, s[36:39], s75 offen offset:3072 sc1\n"
  "v_mov_b64 v[56:57], 0\n"
  "v_mov_b64 v[58:59], 0\n"
  "v_mov_b64 v[60:61], 0\n"
  "v_mov_b64 v[62:63], 0\n"
  "v_mov_b64 v[64:65], 0\n"
  "v_mov_b64 v[66:67], 0\n"
  "v_mov_b64 v[68:69], 0\n"
  "v_mov_b64 v[70:71], 0\n"
  "v_mov_b64 v[72:73], 0\n"
  "v_mov_b64 v[74:75], 0\n"
  "v_mov_b64 v[76:77], 0\n"
  "v_mov_b64 v[78:79], 0\n"
  "s_branch L_p1_%=\n"
  "L_slow_%=:\n"
  "s_cbranch_vccz L_kvslow_%=\n"
  "v_max_u32 v11, v20, v21\n"
  "v_max3_u32 v11, v11, v22, v23\n"
  "v_max3_u32 v11, v11, v24, v25\n"
  "v_max3_u32 v11, v11, v26, v27\n"
  "v_max3_u32 v11, v11, v28, v29\n"
  "v_max3_u32 v11, v11, v30, v31\n"
  "v_max3_u32 v11, v11, v32, v33\n"
  "v_max3_u32 v11, v11, v34, v35\n"
  "v_max3_u32 v11, v11, v36, v37\n"
  "v_max3_u32 v11, v11, v38, v39\n"
  "v_max3_u32 v11, v11, v40, v41\n"
  "v_max3_u32 v11, v11, v42, v43\n"
  "v_max3_u32 v11, v11, v44, v45\n"
  "v_max3_u32 v11, v11, v46, v47\n"
  "v_max3_u32 v11, v11, v48, v49\n"
  "v_max3_u32 v11, v11, v50, v51\n"
  "v_max3_u32 v11, v11, v52, v53\n"
  "v_max3_u32 v11, v11, v54, v55\n"
  "v_cmp_eq_u32 vcc, -1, v11\n"
  "s_cbranch_vccz L_kvslow_%=\n"
  "s_add_u32 s98, s98, 1\n"
  "s_cmp_gt_u32 s98, 32768\n"
  "s_cselect_b32 s72, 1, s72\n"
  "s_cmp_lg_u32 s72, 0\n"
  "s_cbranch_scc1 L_ok_%=\n"
  "s_add_u32 s74, s57, 8388608\n"
  "s_add_u32 s75, s74, 4096\n"
  "s_add_u32 s76, s74, 16384\n"
  "s_waitcnt lgkmcnt(0)\n"
  "s_branch L_re0_%=\n"
  "L_kvslow_%=:\n"
  "s_cmp_eq_u32 s0, 0\n"
  "s_cbranch_scc1 L_ok_%=\n"
  "s_cmp_eq_u32 s70, 63\n"
  "s_cbranch_scc1 L_ok_%=\n"
  "s_mov_b32 s73, 0\n"
  "s_mov_b32 exec_lo, 0\n"
  "L_kvpoll_%=:\n"
  "buffer_load_dword v10, v9, s[36:39], 0 offen sc1\n"
  "s_waitcnt vmcnt(0)\n"
  "v_cmp_le_i32 vcc, s70, v10\n"
  "s_cmp_eq_u32 vcc_hi, -1\n"
  "s_cbranch_scc1 L_kvgo_%=\n"
  "s_add_u32 s73, s73, 1\n"
  "s_cmp_gt_u32 s73, 32768\n"
  "s_cselect_b32 s72, 1, s72\n"
  "s_cmp_lg_u32 s72, 0\n"
  "s_cbranch_scc1 L_kvgo_%=\n"
  "s_sleep 1\n"
  "s_branch L_kvpoll_%=\n"
  "L_kvgo_%=:\n"
  "s_mov_b64 exec, -1\n"
  "s_and_b32 s82, s70, 15\n"
  "s_lshl_b32 s82, s82, 22\n"
  "s_add_u32 s82, s82, s63\n"
  "s_add_u32 s55, s82, s64\n"
  "s_add_u32 s82, s82, s65\n"
  "s_add_u32 s53, s55, 524288\n"
  "buffer_load_dwordx4 v0, s[36:39], s55 offen offset:0 sc1 lds\n"
  "buffer_load_dwordx4 v0, s[36:39], s55 offen offset:1024 sc1 lds\n"
  "buffer_load_dwordx4 v[12:15], v0, s[36:39], s53 offen offset:0 sc1\n"
  "buffer_load_dwordx4 v[16:19], v0, s[36:39], s53 offen offset:1024 sc1\n"
  "s_waitcnt vmcnt(0)\n"
  "s_branch L_ok_%=\n"
  "L_rsslow_%=:\n"
  "v_max_u32 v11, v28, v29\n"
  "v_max3_u32 v11, v11, v30, v31\n"
  "v_max3_u32 v11, v11, v32, v33\n"
  "v_max3_u32 v11, v11, v34, v35\n"
  "v_max3_u32 v11, v11, v36, v37\n"
  "v_max3_u32 v11, v11, v38, v39\n"
  "v_max3_u32 v11, v11, v40, v41\n"
  "v_max3_u32 v11, v11, v42, v43\n"
  "v_cmp_eq_u32 vcc, -1, v11\n"
  "s_cbranch_vccz L_rsok_%=\n"
  "s_cmp_lg_u32 s72, 0\n"
  "s_cbranch_scc1 L_rsok_%=\n"
  "s_mov_b32 s73, 0\n"
  "L_rspoll_%=:\n"
  "buffer_load_dwordx4 v[28:31], v0, s[36:39], s78 offen offset:0 sc1\n"
  "buffer_load_dwordx4 v[32:35], v0, s[36:39], s78 offen offset:1024 sc1\n"
  "buffer_load_dwordx4 v[36:39], v0, s[36:39], s78 offen offset:2048 sc1\n"
  "buffer_load_dwordx4 v[40:43], v0, s[36:39], s78 offen offset:3072 sc1\n"
  "s_waitcnt vmcnt(0)\n"
  "v_max_u32 v11, v28, v29\n"
  "v_max3_u32 v11, v11, v30, v31\n"
  "v_max3_u32 v11, v11, v32, v33\n"
  "v_max3_u32 v11, v11, v34, v35\n"
  "v_max3_u32 v11, v11, v36, v37\n"
  "v_max3_u32 v11, v11, v38, v39\n"
  "v_max3_u32 v11, v11, v40, v41\n"
  "v_max3_u32 v11, v11, v42, v43\n"
  "v_cmp_eq_u32 vcc, -1, v11\n"
  "s_cbranch_vccz L_rsgo_%=\n"
  "s_add_u32 s73, s73, 1\n"
  "s_cmp_gt_u32 s73, 32768\n"
  "s_cselect_b32 s72, 1, s72\n"
  "s_cmp_lg_u32 s72, 0\n"
  "s_cbranch_scc1 L_rsgo_%=\n"
  "s_branch L_rspoll_%=\n"
  "L_rsgo_%=:\n"
  "v_mfma_f32_16x16x32_bf16 v[44:47], v[28:31], v[24:27], 0\n"
  "v_mfma_f32_16x16x32_bf16 v[44:47], v[32:35], v[24:27], v[44:47]\n"
  "v_mfma_f32_16x16x32_bf16 v[44:47], v[36:39], v[24:27], v[44:47]\n"
  "v_mfma_f32_16x16x32_bf16 v[44:47], v[40:43], v[24:27], v[44:47]\n"
  "s_branch L_rsfin_%=\n"
  "L_last_%=:\n"
  "s_waitcnt vmcnt(0) lgkmcnt(0)\n"
  "s_barrier\n"
  "v_mov_b32 v20, s70\n"
  "s_mov_b64 exec, 1\n"
  "buffer_store_dword v20, off, s[36:39], s66 sc1\n"
  "s_mov_b64 exec, -1\n"
  "s_waitcnt vmcnt(0)\n"
  "L_c1_%=:\n"
  "s_endpgm\n"
      :
      : "{v0}"(l16), "{v1}"((ln & 2u) ? 0xEEEEu : 0x4444u),
        "{v2}"(C_QK + w * 1024u + ((n >> 2) & 1u) * 256u + (n >> 3) * 128u + pp * 32u + (g ^ (2u * (n >> 3))) * 8u),
        "{v3}"(C_QK + (g >> 1) * 512u + (g & 1u) * 128u + q * 32u + (pp ^ (2u * (g & 1u))) * 8u), "{v4}"(C_KX + g * 512u + n * 16u),
        "{v5}"(q * 16384u + (g >> 1) * 4096u + (g & 1u) * 64u + pp * 16u),
        "{v6}"(((ln & 2u) ? C_OX + w * 512u + (g & 1u) * 32u : C_DUM + (~g & 1u) * 32u) + (g >> 1) * 256u + q * 64u + (ln & 1u) * 16u),
        "{v7}"(wbase + l16), "{v8}"(C_OX + l16),
        "{v9}"(ln < 32 ? (w == 3 ? ag0 + 20480u + ln * 64u : OOB) : ((ln & 1u) ? c.flag(F_V + h / 3) : c.flag(F_XK + h))),
        "{v12}"(v64), "{v13}"(vo0), "{v14}"(vo8), "{v15}"(r64 + 8u * pp), "{v16}"(r64 + 16u * pp),
        "{v17}"(wbase + (4u * g + q) * 128u + 32u * pp), "{v18}"(wbase + (4u * g + q) * 32u + 8u * pp), "{v[20:23]}"(sel),
        "{s[36:39]}"(c.ws), "{s[40:43]}"(mkrsrc(p.Wq, 576 * 4096 * 2)), "{s[44:47]}"(mkrsrc(p.Wk, 576 * 4096 * 2)),
        "{s[48:51]}"(mkrsrc(p.Wv, 576 * 4096 * 2)), "{s[52:55]}"(mkrsrc(p.Wo, 4096 * 576 * 2)),
        "{s[56:59]}"(uni((v4u){wbase, ag0, P_RS + c.bit + 8u * w * 16384u + (unsigned)h * 128u, w == 3 ? 20480u + (unsigned)h * 64u : OOB})),
        "{s[60:63]}"(uni((v4u){P_RS + c.bit + TR((unsigned)max(page, 0) * 4096u), 32768u + (w < 2 ? w * 4096u + (unsigned)h * 128u : 16384u + (unsigned)(h >> 2) * 128u), (unsigned)page, P_SLAB + c.bit})),
        "{s[64:67]}"(uni((v4u){(unsigned)h * 16384u + w * 2048u, TR(L_KVM) + (unsigned)h * 16384u + w * 1024u, w == 3 ? c.flag(F_C + h) : OOB, C_KX + w * 2048u})),
        "{s[68:69]}"((v2u){w * 1024u, 204u * MB + ((unsigned)c.vb * 4u + w) * 64u}),
        "{s[100:101]}"((v2u){P_RS + c.bit + TR((unsigned)page0 * 4096u) + w * 1024u, page2 >= 0 ? P_RS + c.bit + TR((unsigned)page2 * 4096u) + w * 1024u : OOB})
      : OWNED);
}

__device__ const unsigned char kRoleMap[128] = {
    0, 1, 2, 3, 4, 5, 6, 7, 8, 9, 10, 11, 12, 13, 14, 15, 32, 33, 34, 35, 36, 37, 38, 39, 40, 41, 42, 43, 44, 45, 46, 47,
    16, 17, 18, 19, 20, 21, 22, 23, 24, 25, 26, 27, 28, 29, 30, 31, 48, 49, 50, 51, 52, 53, 54, 55, 56, 57, 58, 59, 60, 61, 62, 63,
    64, 65, 66, 67, 68, 69, 128, 129, 130, 131, 132, 133, 140, 141, 142, 96, 97, 98, 99, 160, 161, 162, 163, 164, 192, 193, 194, 195, 196, 197, 198, 199,
    70, 71, 72, 73, 74, 75, 134, 135, 136, 137, 138, 139, 100, 101, 102, 103, 165, 166, 167, 168, 169, 170, 200, 201, 202, 203, 204, 205, 206, 207, 208, 209
};
DI char* ws_base(char* w) { return w + ((0 - (uintptr_t)w) & (4 * MB - 1)); }
__global__ void __launch_bounds__(256, 1) rmt_main(Params p) {
  __shared__ __attribute__((aligned(16))) char smem[163840];
  asm volatile("" ::"v"((LAS char*)smem) : "memory");
  p.ws = ws_base(p.ws);
  Ctx c;
  c.ws = mkrsrc(p.ws, WS_USED);
  c.lane = threadIdx.x & 63;
  c.wave = __builtin_amdgcn_readfirstlane(threadIdx.x >> 6);
  const int hint = __builtin_amdgcn_readfirstlane((int)*(const unsigned*)(p.ws + P_POL));
  const int r = blockIdx.x >> 3, die = (blockIdx.x + (hint >> 1)) & 7, xq = die & 3;
  c.L = die >> 2;
  c.bit = (unsigned)((c.L ^ hint) & 1) * 8192u;
  const int code = __builtin_amdgcn_readfirstlane((int)kRoleMap[xq * 32 + r]), role = code >> 5, id = code & 31;
  c.vb = role == 0 ? (id >> 2) * 8 + c.L * 4 + (id & 3) : 64 + c.L * 96 + (role == 1 ? 0 : role == 2 ? 32 : role == 3 ? 44 : role == 4 ? 52 : role == 5 ? 67 : 78) + id;
  c.dead = 0;
  STAMP(c, 64, 0);
  if (c.L && role != 3) {
    const unsigned o = (unsigned)(xq * 32 + r) * 4096u + threadIdx.x * 16u;
    const void* m[6] = {p.Wk, p.Wq, p.Wv, p.Wo, p.w1, p.w2};
    unsigned acc = 0;
#pragma unroll
    for (int k = PRE0; k < PRE1; ++k) {
      const rsrc_t rs = mkrsrc(m[k], k < 4 ? 576 * 4096 * 2 : 576 * 4608 * 2);
#pragma unroll
      for (int i = 0; i < (k < 4 ? 9 : 10); ++i) acc ^= __builtin_amdgcn_raw_buffer_load_b128(rs, o, (unsigned)i * 524288u, 0)[0];
    }
    asm volatile("" ::"v"(acc));
  }
  {
    const int wait = ((c.L && role != 3) ? DLY_L : 0) + (role == 2 ? DLY_O : role == 6 ? DLY_F1 : role == 4 ? DLY_F2 : 0);
    for (int i = 0; i < wait; ++i) __builtin_amdgcn_s_sleep(32);
  }
  if (role == 0) role_chain(c, p, id);
  else if (role == 1) role_x(c, p, id);
  else if (role == 2) role_o(c, p, id / 3, id % 3);
  else if (role == 3) role_r(c, p, id >> 2, id & 3);
  else if (role == 4) role_f2(c, p, id / 3, id % 3);
  else if (role == 5) role_v(c, p, id);
  else role_f1(c, p, id);
}

__global__ void __launch_bounds__(256) rmt_init(Params p) {
  const int b = blockIdx.x, tid = threadIdx.x, lane = tid & 63, wave = tid >> 6;
  p.ws = ws_base(p.ws);
  unsigned pre;
  if (b > 256) return;
  if (b == 256) {
    if (wave) return;
    const rsrc_t ws = mkrsrc(p.ws, WS_USED);
    unsigned t[4] = {~0u, ~0u, ~0u, ~0u};
    st16((v4u)0u, ws, P_POL + 16384u + (unsigned)(lane >> 4) * 4096u + (unsigned)(lane & 15) * 16u, 0);
    vmwait<0>();
#pragma unroll 1
    for (int i = 0; i < 8; ++i) {
      u64 t0, t1;
      asm volatile("s_memtime %0\n\ts_waitcnt lgkmcnt(0)" : "=s"(t0)::"memory");
      const unsigned v = ld4(ws, P_POL + 16384u + (unsigned)(i & 1) * 8192u + (unsigned)(i & 2) * 2048u + (unsigned)(i >> 2) * 128u + (unsigned)(lane & 31) * 4u, 0);
      asm volatile("s_waitcnt vmcnt(0)\n\ts_memtime %0\n\ts_waitcnt lgkmcnt(0)" : "=s"(t1) : "v"(v) : "memory");
      const unsigned d = (unsigned)(t1 - t0);
      if (i & 1) t[1] = min(t[1], d); else t[0] = min(t[0], d);
      if (i >= 3 && (t[0] < 540 || t[1] < 540)) break;
    }
    if (tid == 0) {
      const unsigned xcc = (unsigned)__builtin_amdgcn_s_getreg(20 | (3 << 11)) & 0xfu;
      *(unsigned*)(p.ws + P_POL) = (((min(t[1], t[3]) < min(t[0], t[2])) ? 1u : 0u) ^ ((xcc >> 2) & 1u)) | ((xcc & 7u) << 1);
    }
    return;
  }
  const int gt = b * 256 + tid;
  {
    const void* m[4] = {p.Wk, p.Wq, p.Wv, p.Wo};
    unsigned acc = 0;
#pragma unroll
    for (int k = 0; k < IPRE; ++k) {
      const rsrc_t rs = mkrsrc(m[k], 576 * 4096 * 2);
      acc ^= __builtin_amdgcn_raw_buffer_load_b32(rs, (unsigned)gt * 64u, 0, 0) ^ __builtin_amdgcn_raw_buffer_load_b32(rs, (unsigned)gt * 64u, 4194304u, 0);
    }
    pre = acc;
  }
  const v4u ones = (v4u)~0u;
  for (int i = gt; i < 624 * 64; i += 65536) ((v4u*)p.ws)[i < 576 * 64 ? i : i + 1152 * 64] = ones;
  for (int i = gt; i < 64 * 1280; i += 65536) {
    const int s = i / 1280, j0 = i % 1280;
    char* dst = p.ws + P_AG + (unsigned)s * 32768u;
    const int j = j0 < 1088 ? j0 : (j0 < 1152 ? j0 + 448 : 1280 + ((j0 >> 6) & 1) * 512 + (j0 & 63) * 4);
    v4u v = ones;
    if (s == 0 && j0 < 1152) {
      const int e = j & 63, kap = j < 1024 ? 2 * ((j >> 6) & 3) + ((j >> 8) & 1) : 8;
      v = ((const v4u*)p.mem0)[(2 * ((e >> 2) & 3) + (e & 1)) * 72 + 8 * kap + 2 * (e >> 4) + ((e >> 1) & 1)];
      v = (v4u){nosent(v[0]), nosent(v[1]), nosent(v[2]), nosent(v[3])};
    }
    ((v4u*)dst)[j] = v;
    ((v4u*)(dst + 8 * MB))[j] = v;
  }
  asm volatile("" ::"v"(pre));
}

extern "C" void kernel_launch(void* const* d_in, const int* in_sizes, int n_in, void* d_out, int out_size, void* d_ws, size_t ws_size,
                              hipStream_t stream) {
  Params p;
  p.x = d_in[0]; p.mem0 = d_in[1]; p.Wq = d_in[2]; p.Wk = d_in[3]; p.Wv = d_in[4]; p.Wo = d_in[5];
  p.w1 = d_in[8]; p.b1 = d_in[9]; p.w2 = d_in[10]; p.b2 = d_in[11];
  p.out = d_out;
  p.ws = (char*)d_ws;
  rmt_init<<<264, 256, 0, stream>>>(p);
  rmt_main<<<256, 256, 0, stream>>>(p);
}
```
